```python
import torch

def _softmax(x, axis=-1):
    return torch.softmax(x, dim=axis)
def _gelu(x):
    return torch.nn.functional.gelu(x, approximate="tanh")
def _silu(x):
    return torch.nn.functional.silu(x)
def _sigmoid(x):
    return torch.sigmoid(x)
def _layernorm(x, axis=-1, eps=1e-5):
    m = x.mean(dim=axis, keepdim=True)
    v = x.var(dim=axis, keepdim=True, unbiased=False)
    return (x - m) / torch.sqrt(v + eps)
def _scatter_add(tgt, idx, src, axis=0):
    return tgt.clone().index_add_(axis, idx, src)
def _segment_sum(src, seg, n):
    out = torch.zeros((n,) + src.shape[1:], dtype=src.dtype)
    return out.index_add_(0, seg, src)
def _pad(x, pw):
    flat = tuple(v for pair in reversed(pw) for v in pair)
    return torch.nn.functional.pad(x, flat)

kernel_name = 'sequential_preLN__linear_kernel_hedgehog__mixer_channel_mlp__prompt'

def setup_inputs():

    _w = (lambda a: (lambda t: t.to(torch.bfloat16) if t.is_floating_point() else t)(torch.as_tensor(a)))
    def _A_setup():
        B, S, D, H = 1, 2048, 448, 16
        Dh = D // H
        return {
            "x": _w((lambda *s: torch.randn(*s, dtype=torch.bfloat16) * 0.05)(B, S, D)),
            "wqkv": _w((lambda *s: torch.randn(*s, dtype=torch.bfloat16) * 0.2)(D, 3 * D)),
            "w_phi": _w((lambda *s: torch.randn(*s, dtype=torch.bfloat16) * 0.2)(H, Dh, Dh) * (Dh ** -0.5)),
            "wo": _w((lambda *s: torch.randn(*s, dtype=torch.bfloat16) * 0.05)(D, D)),
        }
    _A_in = _A_setup()

    def _B_setup():
        B, S, D, F = 1, 2048, 448, 1792
        return {
            "x": _w((lambda *s: torch.randn(*s, dtype=torch.bfloat16) * 0.05)(B, S, D)),
            "w1": _w((lambda *s: torch.randn(*s, dtype=torch.bfloat16) * 3.2)(D, F) * (D ** -0.5)),
            "b1": _w((lambda *s: torch.randn(*s, dtype=torch.bfloat16) * 0.8)(F)),
            "w2": _w((lambda *s: torch.randn(*s, dtype=torch.bfloat16) * 0.1)(F, D) * (F ** -0.5)),
            "b2": _w((lambda *s: torch.randn(*s, dtype=torch.bfloat16) * 0.1)(D)),
        }
    _B_in = _B_setup()

    return {
        "A_x": _A_in["x"],
        "A_wqkv": _A_in["wqkv"],
        "A_w_phi": _A_in["w_phi"],
        "A_wo": _A_in["wo"],
        "B_x": _B_in["x"],
        "B_w1": _B_in["w1"],
        "B_b1": _B_in["b1"],
        "B_w2": _B_in["w2"],
        "B_b2": _B_in["b2"],
    }

def reference(A_x, A_wqkv, A_w_phi, A_wo, B_x, B_w1, B_b1, B_w2, B_b2):
    def _A_ref(x, wqkv, w_phi, wo):
        B, S, D = x.shape
        H = 16; Dh = D // H
        qkv = (lambda x, *d: x.permute(*d))((x @ wqkv).reshape(B, S, 3, H, Dh), 2, 0, 3, 1, 4)
        q, k, v = qkv[0], qkv[1], qkv[2]
        pq = torch.einsum("bhsd,hdf->bhsf", q, w_phi)
        pk = torch.einsum("bhsd,hdf->bhsf", k, w_phi)
        phi_q = torch.concatenate([_softmax(pq, axis=-1), _softmax(-pq, axis=-1)], axis=-1)
        phi_k = torch.concatenate([_softmax(pk, axis=-1), _softmax(-pk, axis=-1)], axis=-1)
        kv = torch.einsum("bhsf,bhsd->bhfd", phi_k, v)
        z  = torch.einsum("bhsf->bhf", phi_k)
        num = torch.einsum("bhqf,bhfd->bhqd", phi_q, kv)
        den = torch.einsum("bhqf,bhf->bhq", phi_q, z)[..., None] + 1e-6
        o = num / den
        return (lambda x, *d: x.permute(*d))(o, 0, 2, 1, 3).reshape(B, S, D) @ wo

    def _B_ref(x, w1, b1, w2, b2):
        y = _layernorm(x, axis=-1)
        h = _gelu(torch.einsum("bsd,df->bsf", y, w1) + b1)
        return torch.einsum("bsf,fd->bsd", h, w2) + b2

    x = A_x
    h = x + _A_ref(_layernorm(x, axis=-1), A_wqkv, A_w_phi, A_wo)
    h2 = h + _B_ref(_layernorm(h, axis=-1), B_w1, B_b1, B_w2, B_b2)
    return h2
```

```cpp
#include <hip/hip_runtime.h>
#include <hip/hip_bf16.h>
#include <stdint.h>
namespace naive {
typedef unsigned short u16;
__device__ __forceinline__ float b2f(u16 v){ return __uint_as_float(((uint32_t)v)<<16); }
__device__ __forceinline__ u16 f2b(float f){ uint32_t u=__float_as_uint(f); u += 0x7fff + ((u>>16)&1); return (u16)(u>>16); }
__device__ __forceinline__ float rb(float f){ return b2f(f2b(f)); }
constexpr int S=2048,D=448,H=16,DH=28,F=1792;
__global__ void k_ln(const u16* in, u16* out, int times){
  int r=blockIdx.x, t=threadIdx.x; float v[7];
  for(int i=0;i<7;i++) v[i]=b2f(in[r*D+t+64*i]);
  for(int it=0;it<times;it++){
    float s=0; for(int i=0;i<7;i++) s+=v[i];
    for(int o=32;o;o>>=1) s+=__shfl_xor(s,o);
    float m=s/D; float q=0; for(int i=0;i<7;i++){ float d=v[i]-m; q+=d*d; }
    for(int o=32;o;o>>=1) q+=__shfl_xor(q,o);
    float var=rb(q/D); float mb=rb(m);
    float sd=rb(sqrtf(rb(var+1e-5f)));
    for(int i=0;i<7;i++) v[i]=rb(rb(v[i]-mb)/sd);
  }
  for(int i=0;i<7;i++) out[r*D+t+64*i]=f2b(v[i]);
}
__global__ void k_gemm(const u16* A,const u16* B,u16* C,int M,int N,int K,int mode,const u16* bias,const u16* res){
  int n=blockIdx.x*64+threadIdx.x; int m=blockIdx.y; if(n>=N) return;
  float acc=0; for(int k=0;k<K;k++) acc+=b2f(A[m*K+k])*b2f(B[k*N+n]);
  float r=rb(acc);
  if(mode==1) r=rb(r+b2f(res[m*N+n]));
  else if(mode==2){ r=rb(r+b2f(bias[n])); float u=0.7978845608028654f*(r+0.044715f*r*r*r); r=rb(0.5f*r*(1.f+tanhf(u))); }
  else if(mode==3){ r=rb(r+b2f(bias[n])); r=rb(r+b2f(res[m*N+n])); }
  C[m*N+n]=f2b(r);
}
__global__ void k_phi(const u16* qkv,const u16* wphi,u16* phi){
  int s=blockIdx.x; int t=threadIdx.x; if(t>=32) return; int w=t>>4,h=t&15;
  float p[DH];
  for(int f=0;f<DH;f++){ float a=0; for(int d=0;d<DH;d++) a+=b2f(qkv[s*3*D+w*D+h*DH+d])*b2f(wphi[(h*DH+d)*DH+f]); p[f]=rb(a);}
  float mx=-1e30f,mn=1e30f; for(int f=0;f<DH;f++){ mx=fmaxf(mx,p[f]); mn=fminf(mn,p[f]); }
  float s1=0,s2=0; for(int f=0;f<DH;f++){ s1+=expf(p[f]-mx); s2+=expf(mn-p[f]); }
  u16* o=phi+((size_t)(w*H+h)*S+s)*56;
  for(int f=0;f<DH;f++){ o[f]=f2b(expf(p[f]-mx)/s1); o[28+f]=f2b(expf(mn-p[f])/s2);}
}
__global__ void k_kv(const u16* qkv,const u16* phi,u16* kv){
  int h=blockIdx.x, f=blockIdx.y, d=threadIdx.x; if(d>=29) return;
  const u16* pk=phi+((size_t)(H+h)*S)*56; float a=0;
  for(int s=0;s<S;s++){ float v= d<28? b2f(qkv[s*3*D+2*D+h*DH+d]) : 1.f; a+=b2f(pk[s*56+f])*v; }
  kv[(h*56+f)*29+d]=f2b(a);
}
__global__ void k_o(const u16* phi,const u16* kv,u16* o){
  int s=blockIdx.x; int t=threadIdx.x; if(t>=D) return; int h=t/DH,d=t%DH;
  const u16* pq=phi+((size_t)h*S+s)*56; float a=0,z=0;
  for(int f=0;f<56;f++){ float p=b2f(pq[f]); a+=p*b2f(kv[(h*56+f)*29+d]); z+=p*b2f(kv[(h*56+f)*29+28]); }
  o[s*D+t]=f2b(rb(a)/rb(rb(z)+1e-6f));
}
inline void launch(void* const* d_in, void* d_out, void* d_ws, hipStream_t st){
  const u16* x=(const u16*)d_in[0]; const u16* wqkv=(const u16*)d_in[1]; const u16* wphi=(const u16*)d_in[2]; const u16* wo=(const u16*)d_in[3];
  const u16* w1=(const u16*)d_in[5]; const u16* b1=(const u16*)d_in[6]; const u16* w2=(const u16*)d_in[7]; const u16* b2=(const u16*)d_in[8];
  u16* ws=(u16*)d_ws; u16* xn=ws; u16* qkv=xn+S*D; u16* phi=qkv+S*3*D; u16* kv=phi+2*H*S*56; u16* o=kv+H*56*32; u16* h=o+S*D; u16* y=h+S*D; u16* hh=y+S*D;
  k_ln<<<S,64,0,st>>>(x,xn,1);
  k_gemm<<<dim3(21,S),64,0,st>>>(xn,wqkv,qkv,S,3*D,D,0,nullptr,nullptr);
  k_phi<<<S,64,0,st>>>(qkv,wphi,phi);
  k_kv<<<dim3(H,56),64,0,st>>>(qkv,phi,kv);
  k_o<<<S,448,0,st>>>(phi,kv,o);
  k_gemm<<<dim3(7,S),64,0,st>>>(o,wo,h,S,D,D,1,nullptr,x);
  k_ln<<<S,64,0,st>>>(h,y,2);
  k_gemm<<<dim3(28,S),64,0,st>>>(y,w1,hh,S,F,D,2,b1,nullptr);
  k_gemm<<<dim3(7,S),64,0,st>>>(hh,w2,(u16*)d_out,S,D,F,3,b2,h);
}
}
extern "C" void kernel_launch(void* const* d_in, const int* in_sizes, int n_in, void* d_out, int out_size, void* d_ws, size_t ws_size, hipStream_t stream){
  naive::launch(d_in,d_out,d_ws,stream);
}
```

```cpp
#include <hip/hip_runtime.h>
#include <stdint.h>
namespace fk {
typedef unsigned short u16; typedef unsigned u32;
typedef __attribute__((ext_vector_type(4))) u32 u32x4;
typedef __attribute__((ext_vector_type(2))) u32 u32x2;
typedef __attribute__((ext_vector_type(8))) __bf16 bf16x8;
typedef __attribute__((ext_vector_type(2))) __bf16 bf16x2;
typedef __attribute__((ext_vector_type(2))) float f32x2;
typedef __attribute__((ext_vector_type(4))) float f32x4;
typedef __attribute__((ext_vector_type(16))) float f32x16;
typedef short v4i16_t __attribute__((ext_vector_type(4)));
typedef __attribute__((address_space(3))) char* lptr;
typedef __amdgpu_buffer_rsrc_t rsrc_t;
#define DI __device__ __forceinline__
#define LDS(T,a) (*(__attribute__((address_space(3))) T*)(a))
#define STAMP(n)
#define CST(n)
constexpr u32 WS_FLAG=4096, WS_KV=1u<<20, WS_O=2u<<20, WS_H=4u<<20, WS_HS=6u<<20, WS_P=8u<<20;
constexpr int L_WK=98304, L_WV=126976, L_MISC=155648, L_KVX=L_MISC+3072;
constexpr int L_WQ=0, L_PHI=32768, L_VI=49152, L_WO=65536, L_W1A=98304, L_W1B=0;
constexpr int xs(int c){ return (c%6==0?4:c%6==5?5:c%6-1)*16384; }
constexpr int w2c(int c){ return c<5?65536+c*16384:(c-5)*16384; }
constexpr float LOG2E=1.4426950408889634f;

template<int V> struct IC{ static constexpr int v=V; };
template<int I,int N,class F> DI void sfor(F&& f){ if constexpr(I<N){ f(IC<I>{}); sfor<I+1,N>(f); } }
constexpr int adr(int s){ return s<28?L_W1A+s*2048: s<56?L_W1B+(s-28)*2048: s<88?w2c(((s-56)>>1)&3)+(((s-56)>>3)*2+((s-56)&1))*2048: w2c(4+((s-88)>>3))+((s-88)&7)*2048; }
DI rsrc_t mk(const void* p,u32 bytes){ return __builtin_amdgcn_make_buffer_rsrc((void*)p,0,bytes,0x00020000); }
DI void dma(u32 l,u32 voff,rsrc_t r,u32 soff){ asm volatile("s_mov_b32 m0, %0\n\ts_nop 0\n\tbuffer_load_dwordx4 %1, %2, %3 offen lds"::"s"(l),"v"(voff),"s"(r),"s"(soff):"memory"); }
#define WAITV(n) asm volatile("s_waitcnt vmcnt(" #n ")":::"memory")
#define BAR() asm volatile("s_waitcnt lgkmcnt(0)\n\ts_barrier":::"memory")
#define PIN(x) asm volatile(""::"v"(x))
#define PINW(x) asm volatile("":"+v"(x))
#define SB() __builtin_amdgcn_sched_barrier(0)
DI f32x16 mfma(u32x4 a,u32x4 b,f32x16 c){ return __builtin_amdgcn_mfma_f32_32x32x16_bf16(__builtin_bit_cast(bf16x8,a),__builtin_bit_cast(bf16x8,b),c,0,0,0); }
DI f32x4 m44(u32x2 a,u32x2 b,f32x4 c){ return __builtin_amdgcn_mfma_f32_4x4x4bf16_1k(__builtin_bit_cast(v4i16_t,a),__builtin_bit_cast(v4i16_t,b),c,0,0,0); }
DI u32x2 tr(lptr p){ return __builtin_bit_cast(u32x2,__builtin_amdgcn_ds_read_tr16_b64_v4i16((__attribute__((address_space(3))) v4i16_t*)p)); }
DI u32x4 tr2(lptr p0,lptr p1){ u32x2 a=tr(p0),b=tr(p1); return u32x4{a[0],a[1],b[0],b[1]}; }
DI u32 pk(float lo,float hi){ return __builtin_bit_cast(u32,__builtin_convertvector(f32x2{lo,hi},bf16x2)); }
DI float dot2(u32 a,u32 b,float c){ return __builtin_amdgcn_fdot2_f32_bf16(__builtin_bit_cast(bf16x2,a),__builtin_bit_cast(bf16x2,b),c,false); }
DI float swp_lo(float v){ auto r=__builtin_amdgcn_permlane32_swap(__float_as_uint(v),__float_as_uint(v),false,false); return __uint_as_float(r[0]); }
DI float swp_hi(float v){ auto r=__builtin_amdgcn_permlane32_swap(__float_as_uint(v),__float_as_uint(v),false,false); return __uint_as_float(r[1]); }
DI float swp_add(float v){ auto r=__builtin_amdgcn_permlane32_swap(__float_as_uint(v),__float_as_uint(v),false,false); return __uint_as_float(r[0])+__uint_as_float(r[1]); }
DI float swp_max(float v){ auto r=__builtin_amdgcn_permlane32_swap(__float_as_uint(v),__float_as_uint(v),false,false); return __builtin_fmaxf(__uint_as_float(r[0]),__uint_as_float(r[1])); }
DI float swp_min(float v){ auto r=__builtin_amdgcn_permlane32_swap(__float_as_uint(v),__float_as_uint(v),false,false); return __builtin_fminf(__uint_as_float(r[0]),__uint_as_float(r[1])); }
DI u32x4 pk8(const f32x16& c,int b){ return u32x4{pk(c[b],c[b+1]),pk(c[b+2],c[b+3]),pk(c[b+4],c[b+5]),pk(c[b+6],c[b+7])}; }
DI void dsoftmax(const f32x16& p,float c,u32x4* ph){
  float mx=p[0],mn=p[0];
#pragma unroll
  for(int r=1;r<14;r++){ mx=__builtin_fmaxf(mx,p[r]); mn=__builtin_fminf(mn,p[r]); }
  mx=swp_max(mx); mn=swp_min(mn);
  const float a=-mx*c,b=mn*c; f32x16 e,g; float s=0,u=0;
#pragma unroll
  for(int r=0;r<14;r++){ e[r]=__builtin_amdgcn_exp2f(__builtin_fmaf(p[r],c,a)); g[r]=__builtin_amdgcn_exp2f(__builtin_fmaf(p[r],-c,b)); s+=e[r]; u+=g[r]; }
  s=__builtin_amdgcn_rcpf(swp_add(s)); u=__builtin_amdgcn_rcpf(swp_add(u));
#pragma unroll
  for(int r=0;r<14;r++){ e[r]*=s; g[r]*=u; }
  ph[0]=pk8(e,0); ph[1]=u32x4{pk(e[8],e[9]),pk(e[10],e[11]),pk(e[12],e[13]),0u};
  ph[2]=pk8(g,0); ph[3]=u32x4{pk(g[8],g[9]),pk(g[10],g[11]),pk(g[12],g[13]),0u};
}
DI void setflag(char* ws,int ph,int grp,int mem,int w,u32 tag){ __hip_atomic_store((u32*)(ws+WS_FLAG+((ph*16+grp)*16+mem)*128+w*4),tag,__ATOMIC_RELAXED,__HIP_MEMORY_SCOPE_AGENT); }
DI void poll(char* ws,int ph,int grp,bool on,u32 tag,int lane){
  u32* f=(u32*)(ws+WS_FLAG+((ph*16+grp)*16+(lane>>2))*128+(lane&3)*4);
  for(int it=0;it<40000;it++){ u32 v=tag; if(on) v=__hip_atomic_load(f,__ATOMIC_RELAXED,__HIP_MEMORY_SCOPE_AGENT); if(__all(v==tag)) break; __builtin_amdgcn_s_sleep(1); }
}

__global__ __launch_bounds__(256,2) void fused(const u16* __restrict__ x,const u16* __restrict__ wqkv,const u16* __restrict__ wphi,const u16* __restrict__ wo,
    const u16* __restrict__ w1,const u16* __restrict__ b1,const u16* __restrict__ w2,const u16* __restrict__ b2,u16* __restrict__ out,char* __restrict__ ws
    ){
  extern __shared__ __attribute__((aligned(16))) char smem[];
  const lptr L=(lptr)smem;
  const int tid=threadIdx.x,lane=tid&63,w=__builtin_amdgcn_readfirstlane(tid>>6),t=lane&31,hf=lane>>5;
  const int b=blockIdx.x,i=(b&7)*2+((b>>3)&1),j=b>>4,odd=j&1;
  STAMP(0); CST(46);
  const u32 epoch=*(const __attribute__((address_space(4))) u32*)ws;
  const rsrc_t rw=mk(wqkv,448*2688), rx=mk(x+(size_t)(i*128+w*32)*448,32*896);
  const u32 vw=(lane>>2)*2688+(lane&3)*16, hoff=56*j-8*odd+w*16*2688;
  const u32 vx0=(lane>>3)*896+(((lane&7)^(lane>>4))<<4), vx1=vx0^64;
#define WKDMA(c) dma(L_WK+w*1024+(c)*4096,vw,rw,hoff+896+(c)*64*2688)
#define WVDMA(c) dma(L_WV+w*1024+(c)*4096,vw,rw,hoff+1792+(c)*64*2688)
#define XD(c,g) dma(xs(c)+w*4096+(g)*1024,((g)&1)?vx1:vx0,rx,(g)*7168+(c)*128)
#define QD(u) dma(L_WQ+((u)>>2)*16384+w*4096+((u)&3)*1024,vw,rw,hoff+(u)*64*2688)
#define XDMA(c) do{ XD(c,0); XD(c,1); XD(c,2); XD(c,3); }while(0)
  WKDMA(0); WVDMA(0); WKDMA(1); WVDMA(1); XDMA(0);
  dma(L_KVX+(w&1)*1024,(tid&127)*16,mk(wphi+j*784,1568),0);
  WKDMA(2); WVDMA(2); XDMA(1); WKDMA(3); WVDMA(3); XDMA(2);
  const int q4=(lane&15)>>2,mu=((lane>>4)&1)*4+(lane&3),sg=4*(mu&1)+(mu>>1);
  const int rowo=(8*hf+q4)*64;
  const lptr aK=L+L_WK+rowo+((mu+odd)&7)*8;
  const lptr aV=L+L_WV+rowo+(odd?(sg==7?0:((sg+1)%7)+1):sg)*8;
  const lptr aQ=L+L_WQ+rowo+((mu+odd)&7)*8;
  const lptr oK=aK+w*1024,oV=aV+w*1024,pp=L+L_WK+w*1024+(lane&15)*64+(odd?0:56);
  const lptr xb=L+(w*32+t)*128; const int sw=hf^((t>>1)&7);
  u32 tag=epoch*0x9E3779B1u+0x7F4A7C15u; asm volatile("":"+s"(tag));
  STAMP(12);
  float ck=0,cv=0;
const u32x2 pv={0x3F80u,0u};
#define PATCH(c) asm volatile("s_mov_b64 exec, 0xffff\n\tds_write_b64 %0, %1 offset:%2\n\ts_mov_b64 exec, -1"::"v"(pp),"v"(pv),"i"((c)*4096):"memory")
#define OWNRD(c) ok=tr2(oK+(c)*4096,oK+(c)*4096+256); ov=tr2(oV+(c)*4096,oV+(c)*4096+256)
#define OWNSUM() do{ _Pragma("unroll") for(int e=0;e<4;e++){ ck=dot2(ok[e],0x3F803F80u,ck); cv=dot2(ov[e],0x3F803F80u,cv); } PINW(ck); PINW(cv); }while(0)
  u32x4 ok,ov;
  WAITV(19); PATCH(0); OWNRD(0); OWNSUM();
  STAMP(1);
  BAR();
  STAMP(2);
  u32x4 xf[28],fk[4],fv[4],wp[2]; f32x16 cK={},cV={},cG={};
#define XRD(ks) xf[ks]=LDS(u32x4,xb+xs((ks)>>2)+((sw^(((ks)&3)*2))<<4))
#define ARD(ks) fk[(ks)&3]=tr2(aK+(ks)*1024,aK+(ks)*1024+256); fv[(ks)&3]=tr2(aV+(ks)*1024,aV+(ks)*1024+256)
#define MG(ks) cG=mfma(xf[ks],xf[ks],cG); PINW(cG)
#define MK(ks) cK=mfma(fk[(ks)&3],xf[ks],cK); PINW(cK)
#define MV(ks) cV=mfma(fv[(ks)&3],xf[ks],cV); PINW(cV)
#define STAGE(c,N,D0,D1,D2,D3,D4,D5,EXTRA) do{ \
    ARD(4*(c)); ARD(4*(c)+1); SB(); WAITV(N); XRD(4*(c)); XRD(4*(c)+1); XRD(4*(c)+2); XRD(4*(c)+3); SB(); \
    MG(4*(c)); SB(); ARD(4*(c)+2); SB(); MK(4*(c)); SB(); if((c)<6){ PATCH((c)+1); OWNRD((c)+1); } SB(); MV(4*(c)); SB(); \
    MG(4*(c)+1); SB(); ARD(4*(c)+3); SB(); MK(4*(c)+1); SB(); D0; SB(); MV(4*(c)+1); SB(); D1; SB(); \
    MG(4*(c)+2); SB(); if((c)<6) OWNSUM(); SB(); MK(4*(c)+2); SB(); D2; SB(); MV(4*(c)+2); SB(); D3; SB(); \
    MG(4*(c)+3); SB(); D4; SB(); MK(4*(c)+3); SB(); D5; EXTRA; SB(); MV(4*(c)+3); SB(); if((c)<6) BAR(); }while(0)
#define NOP_ do{}while(0)
  STAGE(0,12,WKDMA(4),WVDMA(4),XD(3,0),XD(3,1),XD(3,2),XD(3,3),NOP_);
  { const bool mv=(t&26)!=26; const lptr p=L+L_KVX+(t<26?t:t-2)*2+hf*224; u32 e[16];
#pragma unroll
    for(int q=0;q<16;q++){ const int d=16*(q>>3)+8*((q>>2)&1)+(q&3); e[q]=LDS(u16,p+d*56); }
#pragma unroll
    for(int q=0;q<8;q++){ u32 v=e[2*q]|(e[2*q+1]<<16); v=(mv&&(q<6||hf==0))?v:0u; asm volatile("":"+v"(v)); wp[q>>2][q&3]=v; } }
  STAGE(1,12,WKDMA(5),WVDMA(5),XD(4,0),XD(4,1),XD(4,2),XD(4,3),NOP_);
  STAGE(2,12,WKDMA(6),WVDMA(6),XD(5,0),XD(5,1),XD(5,2),XD(5,3),NOP_);
  STAGE(3,12,XD(6,0),XD(6,1),XD(6,2),XD(6,3),NOP_,NOP_,NOP_);
  STAGE(4,10,QD(0),QD(1),QD(2),QD(3),NOP_,NOP_,NOP_);
  STAGE(5,8,QD(4),QD(5),QD(6),NOP_,NOP_,NOP_,{ LDS(float,L+L_MISC+lane*16+w*4)=ck; LDS(float,L+L_MISC+1024+lane*16+w*4)=cv; });
  STAGE(6,7,NOP_,NOP_,NOP_,NOP_,NOP_,NOP_,NOP_);
  STAMP(3);
  float rstd,nmean;
  { f32x4 a=LDS(f32x4,L+L_MISC+lane*16),c=LDS(f32x4,L+L_MISC+1024+lane*16);
    nmean=swp_hi(cK[12])*(-1.f/448);
    cK=__builtin_amdgcn_mfma_f32_32x32x2f32((a[0]+a[1])+(a[2]+a[3]),nmean,cK,0,0,0);
    cV=__builtin_amdgcn_mfma_f32_32x32x2f32((c[0]+c[1])+(c[2]+c[3]),nmean,cV,0,0,0);
    float g8[8],g4[4];
#pragma unroll
    for(int e=0;e<8;e++){ float u0=cG[2*e],u1=cG[2*e+1]; asm("":"+v"(u0)); g8[e]=(t&1)?u1:u0; }
#pragma unroll
    for(int e=0;e<4;e++){ float u0=g8[2*e]; asm("":"+v"(u0)); g4[e]=(t&2)?g8[2*e+1]:u0; }
    float g2a=(t&8)?g4[1]:g4[0],g2b=(t&8)?g4[3]:g4[2],g1=(t&16)?g2b:g2a;
    auto r=__builtin_amdgcn_permlane32_swap(__float_as_uint(g1),__float_as_uint(g1),false,false);
    float ss=__uint_as_float((t&4)?r[1]:r[0]);
    rstd=__builtin_amdgcn_rsqf(__builtin_fmaxf(__builtin_fmaf(ss,1.f/448,-nmean*nmean),0.f)+1e-5f); }
  const int wro=(w*4096+t*64),c0=((2*hf)^((t>>2)&3))<<4;
  { f32x16 p=mfma(wp[0],pk8(cK,0),f32x16{}); p=mfma(wp[1],pk8(cK,8),p);
#pragma unroll
    for(int r=0;r<16;r++) cV[r]*=rstd;
    u32x4 v0=pk8(cV,0),v1=pk8(cV,8); if(hf){ v1[2]=0x3F80u; v1[3]=0u; }
    LDS(u32x4,L+L_VI+wro+c0)=v0; LDS(u32x4,L+L_VI+wro+(c0^16))=v1;
    u32x4 ph[4]; dsoftmax(p,rstd*LOG2E,ph);
    LDS(u32x4,L+L_PHI+wro+c0)=ph[0]; LDS(u32x4,L+L_PHI+wro+(c0^16))=ph[1];
    LDS(u32x4,L+L_PHI+2048+wro+c0)=ph[2]; LDS(u32x4,L+L_PHI+2048+wro+(c0^16))=ph[3]; }
  WAITV(0);
  STAMP(4);
  BAR();
  const bool act=j<14; const u32 rot=2*j;
  auto kap=[&](int n){ const u32 k=n+rot; return k>=28?k-28:k; };
  const rsrc_t rkv=mk(ws+WS_KV+j*65536,65536), r1=mk(w1,448*3584);
  const u32 v8=(lane>>3), p8=((lane&7)^(lane>>2&4))<<4, vw1=v8*3584+p8;
#define W1D(base,half,u) dma((base)+(u)*1024,vw1,r1,(kap((u)>>1)*2+((u)&1))*8*3584+j*256+(half)*128)
  if(w<2){
    const int ko=(8*hf+q4)*64+(sg&1)*8; const lptr k0=L+ko+(((sg>>1)^(2*hf))<<4),k1=L+ko+256+(((sg>>1)^(2*hf+1))<<4);
    f32x16 c={};
#pragma unroll
    for(int u=0;u<8;u++){ const int o=(u>>1)*4096+(u&1)*1024; c=mfma(tr2(k0+L_PHI+w*2048+o,k1+L_PHI+w*2048+o),tr2(k0+L_VI+o,k1+L_VI+o),c); }
    __builtin_amdgcn_raw_buffer_store_b128(pk8(c,0),rkv,i*4096+w*2048+lane*16,0,16);
    __builtin_amdgcn_raw_buffer_store_b128(pk8(c,8),rkv,i*4096+w*2048+1024+lane*16,0,16);
  } else if(act){
    const rsrc_t r0=mk(wo,448*896); const u32 vo=(lane>>2)*896+(lane&3)*16;
#pragma unroll 1
    for(int u=(w-2)*14;u<(w-1)*14;u++) dma(L_WO+u*1024,vo,r0,kap(u)*16*896+j*64);
  }
  { f32x4 cq={};
#pragma unroll
    for(int u=0;u<7;u++){ const lptr p=aQ+(u>>2)*16384+w*4096+(u&3)*1024; cq=m44(u32x2{0x3F803F80u,0x3F803F80u},tr(p),cq); cq=m44(u32x2{0x3F803F80u,0x3F803F80u},tr(p+256),cq); }
    LDS(float,L+L_MISC+2048+lane*16+w*4)=cq[0]; }
  STAMP(5);
  BAR();
  u32x4 phq[4];
  { f32x16 cQ={};
#pragma unroll
    for(int ks=0;ks<28;ks++){ const lptr p=aQ+((ks>>2)>>2)*16384+(ks&3)*4096+((ks>>2)&3)*1024; cQ=mfma(tr2(p,p+256),xf[ks],cQ);
      if(w>=2){ if(act) W1D(L_W1A,0,(w-2)*28+ks); }
      else if(ks==12){ WAITV(0); if(lane==0) setflag(ws,0,j,i,w,tag); STAMP(6); } }
    f32x4 a=LDS(f32x4,L+L_MISC+2048+lane*16);
    cQ=__builtin_amdgcn_mfma_f32_32x32x2f32((a[0]+a[1])+(a[2]+a[3]),nmean,cQ,0,0,0);
    f32x16 p=mfma(wp[0],pk8(cQ,0),f32x16{}); p=mfma(wp[1],pk8(cQ,8),p);
    dsoftmax(p,rstd*LOG2E,phq); }
  STAMP(7);
  if(w==0) poll(ws,0,j,(lane&2)==0,tag,lane);
  BAR();
  STAMP(8);
  const u32x2 id4={(lane&3)==0?0x3F80u:(lane&3)==1?0x3F800000u:0u,(lane&3)==2?0x3F80u:(lane&3)==3?0x3F800000u:0u};
  u32x4 xr0,xr1;
  { u32x4 g[16];
#pragma unroll
    for(int n=0;n<16;n++) g[n]=__builtin_amdgcn_raw_buffer_load_b128(rkv,w*1024+lane*16,((n+i)&15)*4096,16);
    xr0=__builtin_amdgcn_raw_buffer_load_b128(rx,t*896+hf*16+j*64,0,0); xr1=__builtin_amdgcn_raw_buffer_load_b128(rx,t*896+hf*16+32+j*64,0,0);
    f32x4 s[4]={};
#pragma unroll
    for(int n=0;n<16;n++){ s[2*(n&1)]=m44(id4,u32x2{g[n][0],g[n][1]},s[2*(n&1)]); s[2*(n&1)+1]=m44(id4,u32x2{g[n][2],g[n][3]},s[2*(n&1)+1]); }
    s[0]+=s[2]; s[1]+=s[3];
    LDS(u32x4,L+L_KVX+w*1024+lane*16)=u32x4{pk(s[0][0],s[0][1]),pk(s[0][2],s[0][3]),pk(s[1][0],s[1][1]),pk(s[1][2],s[1][3])}; }
  BAR();
  STAMP(9);
  const rsrc_t ro=mk(ws+WS_O+i*114688,114688);
  { f32x16 c={};
#pragma unroll
    for(int n=0;n<4;n++) c=mfma(LDS(u32x4,L+L_KVX+n*1024+lane*16),phq[n],c);
    const float rd=__builtin_amdgcn_rcpf(swp_hi(c[12])+1e-6f);
#pragma unroll
    for(int r=0;r<16;r++) c[r]*=rd;
    const u32x4 o0=pk8(c,0),o1=pk8(c,8); const u32 ub=w*28672+t*16+((7*j)>>1)*512;
    __builtin_amdgcn_raw_buffer_store_b128(o0,ro,ub+(odd+2*hf)*512,0,16);
    if(hf==0) __builtin_amdgcn_raw_buffer_store_b128(o1,ro,ub+(odd+1)*512,0,16);
    else __builtin_amdgcn_raw_buffer_store_b64(u32x2{o1[0],o1[1]},ro,ub+(odd?8:1536),0,16);
    WAITV(0); }
  if(lane==0) setflag(ws,1,i,j,w,tag);
  STAMP(10);
  const rsrc_t rh=mk(ws+WS_H+i*114688,114688), rp=mk(ws+WS_P+i*(14*114688),14*114688);
  if(act){
    if(w==0) poll(ws,1,i,true,tag,lane);
    else {
#pragma unroll 1
      for(int u=w-1;u<56;u+=3) W1D(L_W1B,1,u);
    }
    const int nq=(mu&4)|((mu&1)<<1)|((mu>>1)&1);
    if(w==0){ const int tq=t>>2, f=((tq&4)|((tq&1)<<1)|((tq>>1)&1))*4+(t&3)+hf*32;
      LDS(u32,L+L_MISC+2048+lane*4)=(u32)b1[j*128+f]<<16; LDS(u32,L+L_MISC+2048+256+lane*4)=(u32)b1[j*128+64+f]<<16; }
    BAR();
    STAMP(11);
    if(b==0&&tid==0) __hip_atomic_store((u32*)ws,tag,__ATOMIC_RELAXED,__HIP_MEMORY_SCOPE_AGENT);
    const rsrc_t rs=mk(ws+WS_HS+i*14336,14336);
    { u32x4 of[28];
#pragma unroll
      for(int n=0;n<28;n++) of[n]=__builtin_amdgcn_raw_buffer_load_b128(ro,w*28672+lane*16,kap(n)*1024,16);
      f32x16 c;
#pragma unroll
      for(int e=0;e<4;e++){ c[2*e]=__uint_as_float(xr0[e]<<16); c[2*e+1]=__uint_as_float(xr0[e]&0xffff0000u); c[8+2*e]=__uint_as_float(xr1[e]<<16); c[9+2*e]=__uint_as_float(xr1[e]&0xffff0000u); }
      const lptr aO=L+L_WO+rowo+nq*8;
#pragma unroll
      for(int n=0;n<28;n++) c=mfma(tr2(aO+n*1024,aO+n*1024+256),of[n],c);
      __builtin_amdgcn_raw_buffer_store_b128(pk8(c,0),rh,w*28672+j*2048+lane*16,0,16);
      __builtin_amdgcn_raw_buffer_store_b128(pk8(c,8),rh,w*28672+j*2048+1024+lane*16,0,16);
      float s1=0,s2=0;
#pragma unroll
      for(int r=0;r<16;r++){ s1+=c[r]; s2=__builtin_fmaf(c[r],c[r],s2); }
      s1=swp_add(s1); s2=swp_add(s2);
      if(hf==0) __builtin_amdgcn_raw_buffer_store_b64(u32x2{__float_as_uint(s1),__float_as_uint(s2)},rs,w*3584+j*256+t*8,0,16);
      WAITV(0); }
    if(lane==0) setflag(ws,2,i,j,w,tag);
    STAMP(13);
    BAR();
    const rsrc_t r2=mk(w2+(size_t)j*128*448,128*896); const u32 vw2=v8*896+p8;
#define W2D(c,u) dma(w2c(c)+(u)*1024,vw2,r2,(u)*7168+(c)*128)
    const lptr a0=L+(8*hf+q4)*128+(q4>>1)*64+nq*8; const lptr a1=L+(((8*hf+q4)*128+(q4>>1)*64+nq*8)^64);
    {
      const lptr p=((w&1)?a1:a0)+((w&2)?L_W1B:L_W1A); f32x4 cs={};
      if(w){
#pragma unroll 1
        for(int u=w-1;u<32;u+=3) dma(65536+u*1024,vw2,r2,(u&15)*7168+(u>>4)*128);
      }
#pragma unroll
      for(int n=0;n<28;n++){ cs=m44(u32x2{0x3F803F80u,0x3F803F80u},tr(p+n*2048),cs); cs=m44(u32x2{0x3F803F80u,0x3F803F80u},tr(p+n*2048+512),cs); }
      const float v=swp_add(cs[0]); if(hf==0) LDS(float,L+L_MISC+1024+w*128+t*4)=v; }
    STAMP(14);
    if(w==0) poll(ws,2,i,lane<56,tag,lane);
    BAR();
    STAMP(15);
    u32x4 hf_[28],fa[3][2],hb[8]; f32x16 c4[4],c5[8]; float sg_,K1,K2,Lg;
    { u32x2 st[7];
#pragma unroll
      for(int n=0;n<7;n++) st[n]=__builtin_amdgcn_raw_buffer_load_b64(rs,w*3584+n*512+lane*8,0,16);
#pragma unroll
      for(int n=0;n<28;n++) hf_[n]=__builtin_amdgcn_raw_buffer_load_b128(rh,w*28672+lane*16,kap(n)*1024,16);
      float cb[4];
#pragma unroll
      for(int T=0;T<4;T++) cb[T]=LDS(float,L+L_MISC+1024+hf*1024+T*128+t*4);
      float s1=0,s2=0;
#pragma unroll
      for(int n=0;n<7;n++){ s1+=__uint_as_float(st[n][0]); s2+=__uint_as_float(st[n][1]); }
      s1=swp_add(s1)*(-1.f/448); s2=swp_add(s2);
      const float v=__builtin_fmaxf(__builtin_fmaf(s2,1.f/448,-s1*s1),0.f), vv=__builtin_fmaf(v,1.00001f,1e-10f), rs_=__builtin_amdgcn_rsqf(vv);
      sg_=vv*rs_; K1=-2.302208198f*rs_; K2=-0.1029432397f*rs_*rs_*rs_; Lg=0.5f*__builtin_amdgcn_logf(vv);
      const float bb=hf?sg_:s1;
#pragma unroll
      for(int T=0;T<4;T++) c4[T]=__builtin_amdgcn_mfma_f32_32x32x2f32(cb[T],bb,f32x16{},0,0,0); }
    float t0,t1,e0,e1,g0;
#define GA(p) c4[(p)>>3][2*((p)&7)]
#define GB(p) c4[(p)>>3][2*((p)&7)+1]
#define G5(p) do{ float g1=GB(p)*e1; PINW(g0); PINW(g1); hb[(p)>>2][(p)&3]=pk(g0,g1); }while(0)
#define GS(q) do{ constexpr int p_=(q)>>2; \
      if constexpr(((q)&3)==0){ if constexpr(p_>0&&p_!=24) G5(p_>0?p_-1:0); t0=GA(p_)*GA(p_); PINW(t0); t1=GB(p_)*GB(p_); t0=__builtin_fmaf(t0,K2,K1); PINW(t0); t1=__builtin_fmaf(t1,K2,K1); PINW(t1);} \
      else if constexpr(((q)&3)==1){ t0=__builtin_fmaf(GA(p_),t0,Lg); PINW(t0); t1=__builtin_fmaf(GB(p_),t1,Lg); e0=__builtin_amdgcn_exp2f(t0); PINW(e0); PINW(t1);} \
      else if constexpr(((q)&3)==2){ e1=__builtin_amdgcn_exp2f(t1); e0+=sg_; e0=__builtin_amdgcn_rcpf(e0); PINW(e0); PINW(e1);} \
      else { e1+=sg_; e1=__builtin_amdgcn_rcpf(e1); g0=GA(p_)*e0; PINW(e1); PINW(g0);} }while(0)
#define RD2(s,off) do{ fa[s][0]=tr2(a0+(off),a0+(off)+512); fa[s][1]=tr2(a1+(off),a1+(off)+512); }while(0)
    RD2(0,adr(0)); RD2(1,adr(1)); SB();
    sfor<0,56>([&](auto S){ constexpr int s=S.v,T=s<28?0:2,n=s<28?s:s-28;
      c4[T]=mfma(fa[s%3][0],hf_[n],c4[T]); PINW(c4[T]); SB();
      if constexpr(s==54){ WAITV(4); asm volatile("s_barrier":::"memory"); }
      fa[(s+2)%3][0]=tr2(a0+adr(s+2),a0+adr(s+2)+512);
      if constexpr(s>=28) GS(2*(s-28)); SB();
      c4[T+1]=mfma(fa[s%3][1],hf_[n],c4[T+1]); PINW(c4[T+1]); SB();
      if constexpr(s==27||s==55) asm volatile("s_barrier":::"memory");
      fa[(s+2)%3][1]=tr2(a1+adr(s+2),a1+adr(s+2)+512);
      if constexpr(s>=28) GS(2*(s-28)+1);
      if constexpr(s>=27&&s<51&&(s&1)) W2D(2+((s-27)>>3),w*4+(((s-27)>>1)&3));
      SB(); });
    STAMP(16);
    const u32 vp=w*28672+lane*16, sp=j*114688;
#define PST(n,c) do{ __builtin_amdgcn_raw_buffer_store_b128(pk8(c,0),rp,vp+(n)*2048,sp,16); __builtin_amdgcn_raw_buffer_store_b128(pk8(c,8),rp,vp+(n)*2048+1024,sp,16); }while(0)
    sfor<56,112>([&](auto S){ constexpr int s=S.v,u=s-56, ks=u<32?(u>>3)*2+(u&1):(u&7), A=u<32?(u&6):((u>>3)&1)*2; constexpr bool first=u<32?ks==0:(u&7)==0;
      if constexpr(u==16){ sfor<88,96>([&](auto Q){ GS(Q.v); }); G5(23); SB(); }
      if constexpr(u==24){ sfor<112,128>([&](auto Q){ GS(Q.v); }); G5(31); SB(); }
      if constexpr(first) c5[A]=mfma(fa[s%3][0],hb[ks],f32x16{}); else c5[A]=mfma(fa[s%3][0],hb[ks],c5[A]);
      PINW(c5[A]); SB();
      if constexpr(u==30){ WAITV(8); asm volatile("s_barrier":::"memory"); }
      if constexpr(s+2<112) fa[(s+2)%3][0]=tr2(a0+adr(s+2),a0+adr(s+2)+512);
      if constexpr(u<16) GS(56+2*u); else if constexpr(u<24) GS(96+2*(u-16));
      if constexpr(u>=27&&u<=33&&(u&1)) PST(u-27,c5[u-27]);
      if constexpr(u==42||u==50) PST(8+((u-42)>>2),c5[((u-42)>>2)&2]);
      SB();
      if constexpr(first) c5[A+1]=mfma(fa[s%3][1],hb[ks],f32x16{}); else c5[A+1]=mfma(fa[s%3][1],hb[ks],c5[A+1]);
      PINW(c5[A+1]); SB();
      if constexpr(s+2<112) fa[(s+2)%3][1]=tr2(a1+adr(s+2),a1+adr(s+2)+512);
      if constexpr(u<16) GS(57+2*u); else if constexpr(u<24) GS(97+2*(u-16));
      if constexpr(u<8) W2D(5+(u>>2),w*4+(u&3));
      if constexpr(u>=27&&u<=33&&(u&1)) PST(u-26,c5[u-26]);
      if constexpr(u==42||u==50) PST(9+((u-42)>>2),c5[(((u-42)>>2)&2)+1]);
      SB(); });
    PST(12,c5[0]); PST(13,c5[1]);
    STAMP(17);
    WAITV(0);
    if(lane==0) setflag(ws,3,i,j,w,tag);
    STAMP(19);
  }
  { const u32 U=w*14+(lane>>3), vb=lane<56?U*16:0x80000000u, vq=lane<56?(j>>2)*28672+U*512+((j&3)*8+(lane&7))*16:0x80000000u;
    const rsrc_t rb=mk(b2,896);
    const u32x4 q0=__builtin_amdgcn_raw_buffer_load_b128(rb,vb,0,0),q1=__builtin_amdgcn_raw_buffer_load_b128(rb,vb+112,0,0);
    if(w==0) poll(ws,3,i,lane<56&&(lane&3)==(j>>2),tag,lane);
    BAR();
    STAMP(20);
    u32x4 g[15][2];
#pragma unroll
    for(int n=0;n<14;n++){ const u32 k=n+j, so=(k>=28?k-28:k>=14?k-14:k)*114688; g[n][0]=__builtin_amdgcn_raw_buffer_load_b128(rp,vq,so,16); g[n][1]=__builtin_amdgcn_raw_buffer_load_b128(rp,vq+3584,so,16); }
    g[14][0]=__builtin_amdgcn_raw_buffer_load_b128(rh,vq,0,16); g[14][1]=__builtin_amdgcn_raw_buffer_load_b128(rh,vq+3584,0,16);
    f32x4 s[4]; s[0]=m44(id4,u32x2{q0[0],q0[1]},f32x4{}); s[1]=m44(id4,u32x2{q0[2],q0[3]},f32x4{}); s[2]=m44(id4,u32x2{q1[0],q1[1]},f32x4{}); s[3]=m44(id4,u32x2{q1[2],q1[3]},f32x4{});
#pragma unroll
    for(int n=0;n<15;n++){ s[0]=m44(id4,u32x2{g[n][0][0],g[n][0][1]},s[0]); s[1]=m44(id4,u32x2{g[n][0][2],g[n][0][3]},s[1]); s[2]=m44(id4,u32x2{g[n][1][0],g[n][1][1]},s[2]); s[3]=m44(id4,u32x2{g[n][1][2],g[n][1][3]},s[3]); }
    const rsrc_t rout=mk(out+(size_t)(i*128+j*8)*448,8*896); const u32 vo=lane<56?(lane&7)*896+U*16:0x80000000u;
    __builtin_amdgcn_raw_buffer_store_b128(u32x4{pk(s[0][0],s[0][1]),pk(s[0][2],s[0][3]),pk(s[1][0],s[1][1]),pk(s[1][2],s[1][3])},rout,vo,0,0);
    __builtin_amdgcn_raw_buffer_store_b128(u32x4{pk(s[2][0],s[2][1]),pk(s[2][2],s[2][3]),pk(s[3][0],s[3][1]),pk(s[3][2],s[3][3])},rout,vo+112,0,0); }
  STAMP(21); CST(47);
}
}
extern "C" void kernel_launch(void* const* d_in,const int* in_sizes,int n_in,void* d_out,int out_size,void* d_ws,size_t ws_size,hipStream_t stream){
  typedef const unsigned short* P;
  (void)hipFuncSetAttribute((const void*)fk::fused,hipFuncAttributeMaxDynamicSharedMemorySize,163840);
  fk::fused<<<256,256,163840,stream>>>((P)d_in[0],(P)d_in[1],(P)d_in[2],(P)d_in[3],(P)d_in[5],(P)d_in[6],(P)d_in[7],(P)d_in[8],(unsigned short*)d_out,(char*)d_ws);
}
```

```cpp
#include <hip/hip_runtime.h>
#include <stdint.h>
namespace fk {
typedef unsigned short u16; typedef unsigned u32;
typedef __attribute__((ext_vector_type(4))) u32 u32x4;
typedef __attribute__((ext_vector_type(2))) u32 u32x2;
typedef __attribute__((ext_vector_type(8))) __bf16 bf16x8;
typedef __attribute__((ext_vector_type(2))) __bf16 bf16x2;
typedef __attribute__((ext_vector_type(2))) float f32x2;
typedef __attribute__((ext_vector_type(4))) float f32x4;
typedef __attribute__((ext_vector_type(16))) float f32x16;
typedef short v4i16_t __attribute__((ext_vector_type(4)));
typedef __attribute__((address_space(3))) char* lptr;
typedef __amdgpu_buffer_rsrc_t rsrc_t;
#define DI __device__ __forceinline__
#define LDS(T,a) (*(__attribute__((address_space(3))) T*)(a))
#define STAMP(n)
#define CST(n)
constexpr u32 WS_FLAG=4096, WS_KV=1u<<20, WS_O=2u<<20, WS_H=4u<<20, WS_HS=6u<<20, WS_P=8u<<20;
constexpr int L_WK=98304, L_WV=126976, L_MISC=155648, L_KVX=L_MISC+3072;
constexpr int L_WQ=0, L_PHI=32768, L_VI=49152, L_WO=65536, L_W1A=98304, L_W1B=0;
constexpr int xs(int c){ return (c%6==0?4:c%6==5?5:c%6-1)*16384; }
constexpr int w2c(int c){ return c<5?65536+c*16384:(c-5)*16384; }
constexpr float LOG2E=1.4426950408889634f;

template<int V> struct IC{ static constexpr int v=V; };
template<int I,int N,class F> DI void sfor(F&& f){ if constexpr(I<N){ f(IC<I>{}); sfor<I+1,N>(f); } }
constexpr int adr(int s){ return s<28?L_W1A+s*2048: s<56?L_W1B+(s-28)*2048: s<88?w2c(((s-56)>>1)&3)+(((s-56)>>3)*2+((s-56)&1))*2048: w2c(4+((s-88)>>3))+((s-88)&7)*2048; }
DI rsrc_t mk(const void* p,u32 bytes){ return __builtin_amdgcn_make_buffer_rsrc((void*)p,0,bytes,0x00020000); }
DI void dma(u32 l,u32 voff,rsrc_t r,u32 soff){ asm volatile("s_mov_b32 m0, %0\n\ts_nop 0\n\tbuffer_load_dwordx4 %1, %2, %3 offen lds"::"s"(l),"v"(voff),"s"(r),"s"(soff):"memory"); }
#define WAITV(n) asm volatile("s_waitcnt vmcnt(" #n ")":::"memory")
#define BAR() asm volatile("s_waitcnt lgkmcnt(0)\n\ts_barrier":::"memory")
#define PIN(x) asm volatile(""::"v"(x))
#define PINW(x) asm volatile("":"+v"(x))
#define SB() __builtin_amdgcn_sched_barrier(0)
DI f32x16 mfma(u32x4 a,u32x4 b,f32x16 c){ return __builtin_amdgcn_mfma_f32_32x32x16_bf16(__builtin_bit_cast(bf16x8,a),__builtin_bit_cast(bf16x8,b),c,0,0,0); }
DI f32x4 m44(u32x2 a,u32x2 b,f32x4 c){ return __builtin_amdgcn_mfma_f32_4x4x4bf16_1k(__builtin_bit_cast(v4i16_t,a),__builtin_bit_cast(v4i16_t,b),c,0,0,0); }
DI u32x2 tr(lptr p){ return __builtin_bit_cast(u32x2,__builtin_amdgcn_ds_read_tr16_b64_v4i16((__attribute__((address_space(3))) v4i16_t*)p)); }
DI u32x4 tr2(lptr p0,lptr p1){ u32x2 a=tr(p0),b=tr(p1); return u32x4{a[0],a[1],b[0],b[1]}; }
DI u32 pk(float lo,float hi){ return __builtin_bit_cast(u32,__builtin_convertvector(f32x2{lo,hi},bf16x2)); }
DI float dot2(u32 a,u32 b,float c){ return __builtin_amdgcn_fdot2_f32_bf16(__builtin_bit_cast(bf16x2,a),__builtin_bit_cast(bf16x2,b),c,false); }
DI float swp_lo(float v){ auto r=__builtin_amdgcn_permlane32_swap(__float_as_uint(v),__float_as_uint(v),false,false); return __uint_as_float(r[0]); }
DI float swp_hi(float v){ auto r=__builtin_amdgcn_permlane32_swap(__float_as_uint(v),__float_as_uint(v),false,false); return __uint_as_float(r[1]); }
DI float swp_add(float v){ auto r=__builtin_amdgcn_permlane32_swap(__float_as_uint(v),__float_as_uint(v),false,false); return __uint_as_float(r[0])+__uint_as_float(r[1]); }
DI float swp_max(float v){ auto r=__builtin_amdgcn_permlane32_swap(__float_as_uint(v),__float_as_uint(v),false,false); return __builtin_fmaxf(__uint_as_float(r[0]),__uint_as_float(r[1])); }
DI float swp_min(float v){ auto r=__builtin_amdgcn_permlane32_swap(__float_as_uint(v),__float_as_uint(v),false,false); return __builtin_fminf(__uint_as_float(r[0]),__uint_as_float(r[1])); }
DI u32x4 pk8(const f32x16& c,int b){ return u32x4{pk(c[b],c[b+1]),pk(c[b+2],c[b+3]),pk(c[b+4],c[b+5]),pk(c[b+6],c[b+7])}; }
DI void dsoftmax(const f32x16& p,float c,u32x4* ph){
  float mx=p[0],mn=p[0];
#pragma unroll
  for(int r=1;r<14;r++){ mx=__builtin_fmaxf(mx,p[r]); mn=__builtin_fminf(mn,p[r]); }
  mx=swp_max(mx); mn=swp_min(mn);
  const float a=-mx*c,b=mn*c; f32x16 e,g; float s=0,u=0;
#pragma unroll
  for(int r=0;r<14;r++){ e[r]=__builtin_amdgcn_exp2f(__builtin_fmaf(p[r],c,a)); g[r]=__builtin_amdgcn_exp2f(__builtin_fmaf(p[r],-c,b)); s+=e[r]; u+=g[r]; }
  s=__builtin_amdgcn_rcpf(swp_add(s)); u=__builtin_amdgcn_rcpf(swp_add(u));
#pragma unroll
  for(int r=0;r<14;r++){ e[r]*=s; g[r]*=u; }
  ph[0]=pk8(e,0); ph[1]=u32x4{pk(e[8],e[9]),pk(e[10],e[11]),pk(e[12],e[13]),0u};
  ph[2]=pk8(g,0); ph[3]=u32x4{pk(g[8],g[9]),pk(g[10],g[11]),pk(g[12],g[13]),0u};
}
DI void setflag(char* ws,int ph,int grp,int mem,int w,u32 tag){ __hip_atomic_store((u32*)(ws+WS_FLAG+((ph*16+grp)*16+mem)*128+w*4),tag,__ATOMIC_RELAXED,__HIP_MEMORY_SCOPE_AGENT); }
DI void poll(char* ws,int ph,int grp,bool on,u32 tag,int lane){
  u32* f=(u32*)(ws+WS_FLAG+((ph*16+grp)*16+(lane>>2))*128+(lane&3)*4);
  for(int it=0;it<40000;it++){ u32 v=tag; if(on) v=__hip_atomic_load(f,__ATOMIC_RELAXED,__HIP_MEMORY_SCOPE_AGENT); if(__all(v==tag)) break; __builtin_amdgcn_s_sleep(1); }
}

__global__ __launch_bounds__(256,2) void fused(const u16* __restrict__ x,const u16* __restrict__ wqkv,const u16* __restrict__ wphi,const u16* __restrict__ wo,
    const u16* __restrict__ w1,const u16* __restrict__ b1,const u16* __restrict__ w2,const u16* __restrict__ b2,u16* __restrict__ out,char* __restrict__ ws
    ){
  extern __shared__ __attribute__((aligned(16))) char smem[];
  const lptr L=(lptr)smem;
  const int tid=threadIdx.x,lane=tid&63,w=__builtin_amdgcn_readfirstlane(tid>>6),t=lane&31,hf=lane>>5;
  const int b=blockIdx.x,i=(b&7)*2+((b>>3)&1),j=b>>4,odd=j&1;
  STAMP(0); CST(46);
  const u32 epoch=*(const __attribute__((address_space(4))) u32*)ws;
  const rsrc_t rw=mk(wqkv,448*2688), rx=mk(x+(size_t)(i*128+w*32)*448,32*896);
  const u32 vw=(lane>>2)*2688+(lane&3)*16, hoff=56*j-8*odd+w*16*2688;
  const u32 vx0=(lane>>3)*896+(((lane&7)^(lane>>4))<<4), vx1=vx0^64;
#define WKDMA(c) dma(L_WK+w*1024+(c)*4096,vw,rw,hoff+896+(c)*64*2688)
#define WVDMA(c) dma(L_WV+w*1024+(c)*4096,vw,rw,hoff+1792+(c)*64*2688)
#define XD(c,g) dma(xs(c)+w*4096+(g)*1024,((g)&1)?vx1:vx0,rx,(g)*7168+(c)*128)
#define QD(u) dma(L_WQ+((u)>>2)*16384+w*4096+((u)&3)*1024,vw,rw,hoff+(u)*64*2688)
#define XDMA(c) do{ XD(c,0); XD(c,1); XD(c,2); XD(c,3); }while(0)
  WKDMA(0); WVDMA(0); WKDMA(1); WVDMA(1); XDMA(0);
  dma(L_KVX+(w&1)*1024,(tid&127)*16,mk(wphi+j*784,1568),0);
  WKDMA(2); WVDMA(2); XDMA(1); WKDMA(3); WVDMA(3); XDMA(2);
  const int q4=(lane&15)>>2,mu=((lane>>4)&1)*4+(lane&3),sg=4*(mu&1)+(mu>>1);
  const int rowo=(8*hf+q4)*64;
  const lptr aK=L+L_WK+rowo+((mu+odd)&7)*8;
  const lptr aV=L+L_WV+rowo+(odd?(sg==7?0:((sg+1)%7)+1):sg)*8;
  const lptr aQ=L+L_WQ+rowo+((mu+odd)&7)*8;
  const lptr oK=aK+w*1024,oV=aV+w*1024,pp=L+L_WK+w*1024+(lane&15)*64+(odd?0:56);
  const lptr xb=L+(w*32+t)*128; const int sw=hf^((t>>1)&7);
  u32 tag=epoch*0x9E3779B1u+0x7F4A7C15u; asm volatile("":"+s"(tag));
  STAMP(12);
  float ck=0,cv=0;
const u32x2 pv={0x3F80u,0u};
#define PATCH(c) asm volatile("s_mov_b64 exec, 0xffff\n\tds_write_b64 %0, %1 offset:%2\n\ts_mov_b64 exec, -1"::"v"(pp),"v"(pv),"i"((c)*4096):"memory")
#define OWNRD(c) ok=tr2(oK+(c)*4096,oK+(c)*4096+256); ov=tr2(oV+(c)*4096,oV+(c)*4096+256)
#define OWNSUM() do{ _Pragma("unroll") for(int e=0;e<4;e++){ ck=dot2(ok[e],0x3F803F80u,ck); cv=dot2(ov[e],0x3F803F80u,cv); } PINW(ck); PINW(cv); }while(0)
  u32x4 ok,ov;
  WAITV(19); PATCH(0); OWNRD(0); OWNSUM();
  STAMP(1);
  BAR();
  STAMP(2);
  u32x4 xf[28],fk[4],fv[4],wp[2]; f32x16 cK={},cV={},cG={};
#define XRD(ks) xf[ks]=LDS(u32x4,xb+xs((ks)>>2)+((sw^(((ks)&3)*2))<<4))
#define ARD(ks) fk[(ks)&3]=tr2(aK+(ks)*1024,aK+(ks)*1024+256); fv[(ks)&3]=tr2(aV+(ks)*1024,aV+(ks)*1024+256)
#define MG(ks) cG=mfma(xf[ks],xf[ks],cG); PINW(cG)
#define MK(ks) cK=mfma(fk[(ks)&3],xf[ks],cK); PINW(cK)
#define MV(ks) cV=mfma(fv[(ks)&3],xf[ks],cV); PINW(cV)
#define STAGE(c,N,D0,D1,D2,D3,D4,D5,EXTRA) do{ \
    ARD(4*(c)); ARD(4*(c)+1); SB(); WAITV(N); XRD(4*(c)); XRD(4*(c)+1); XRD(4*(c)+2); XRD(4*(c)+3); SB(); \
    MG(4*(c)); SB(); ARD(4*(c)+2); SB(); MK(4*(c)); SB(); if((c)<6){ PATCH((c)+1); OWNRD((c)+1); } SB(); MV(4*(c)); SB(); \
    MG(4*(c)+1); SB(); ARD(4*(c)+3); SB(); MK(4*(c)+1); SB(); D0; SB(); MV(4*(c)+1); SB(); D1; SB(); \
    MG(4*(c)+2); SB(); if((c)<6) OWNSUM(); SB(); MK(4*(c)+2); SB(); D2; SB(); MV(4*(c)+2); SB(); D3; SB(); \
    MG(4*(c)+3); SB(); D4; SB(); MK(4*(c)+3); SB(); D5; EXTRA; SB(); MV(4*(c)+3); SB(); if((c)<6) BAR(); }while(0)
#define NOP_ do{}while(0)
  STAGE(0,12,WKDMA(4),WVDMA(4),XD(3,0),XD(3,1),XD(3,2),XD(3,3),NOP_);
  { const bool mv=(t&26)!=26; const lptr p=L+L_KVX+(t<26?t:t-2)*2+hf*224; u32 e[16];
#pragma unroll
    for(int q=0;q<16;q++){ const int d=16*(q>>3)+8*((q>>2)&1)+(q&3); e[q]=LDS(u16,p+d*56); }
#pragma unroll
    for(int q=0;q<8;q++){ u32 v=e[2*q]|(e[2*q+1]<<16); v=(mv&&(q<6||hf==0))?v:0u; asm volatile("":"+v"(v)); wp[q>>2][q&3]=v; } }
  STAGE(1,12,WKDMA(5),WVDMA(5),XD(4,0),XD(4,1),XD(4,2),XD(4,3),NOP_);
  STAGE(2,12,WKDMA(6),WVDMA(6),XD(5,0),XD(5,1),XD(5,2),XD(5,3),NOP_);
  STAGE(3,12,XD(6,0),XD(6,1),XD(6,2),XD(6,3),NOP_,NOP_,NOP_);
  STAGE(4,10,QD(0),QD(1),QD(2),QD(3),NOP_,NOP_,NOP_);
  STAGE(5,8,QD(4),QD(5),QD(6),NOP_,NOP_,NOP_,{ LDS(float,L+L_MISC+lane*16+w*4)=ck; LDS(float,L+L_MISC+1024+lane*16+w*4)=cv; });
  STAGE(6,7,NOP_,NOP_,NOP_,NOP_,NOP_,NOP_,NOP_);
  STAMP(3);
  float rstd,nmean;
  { f32x4 a=LDS(f32x4,L+L_MISC+lane*16),c=LDS(f32x4,L+L_MISC+1024+lane*16);
    nmean=swp_hi(cK[12])*(-1.f/448);
    cK=__builtin_amdgcn_mfma_f32_32x32x2f32((a[0]+a[1])+(a[2]+a[3]),nmean,cK,0,0,0);
    cV=__builtin_amdgcn_mfma_f32_32x32x2f32((c[0]+c[1])+(c[2]+c[3]),nmean,cV,0,0,0);
    float g8[8],g4[4];
#pragma unroll
    for(int e=0;e<8;e++){ float u0=cG[2*e],u1=cG[2*e+1]; asm("":"+v"(u0)); g8[e]=(t&1)?u1:u0; }
#pragma unroll
    for(int e=0;e<4;e++){ float u0=g8[2*e]; asm("":"+v"(u0)); g4[e]=(t&2)?g8[2*e+1]:u0; }
    float g2a=(t&8)?g4[1]:g4[0],g2b=(t&8)?g4[3]:g4[2],g1=(t&16)?g2b:g2a;
    auto r=__builtin_amdgcn_permlane32_swap(__float_as_uint(g1),__float_as_uint(g1),false,false);
    float ss=__uint_as_float((t&4)?r[1]:r[0]);
    rstd=__builtin_amdgcn_rsqf(__builtin_fmaxf(__builtin_fmaf(ss,1.f/448,-nmean*nmean),0.f)+1e-5f); }
  const int wro=(w*4096+t*64),c0=((2*hf)^((t>>2)&3))<<4;
  { f32x16 p=mfma(wp[0],pk8(cK,0),f32x16{}); p=mfma(wp[1],pk8(cK,8),p);
#pragma unroll
    for(int r=0;r<16;r++) cV[r]*=rstd;
    u32x4 v0=pk8(cV,0),v1=pk8(cV,8); if(hf){ v1[2]=0x3F80u; v1[3]=0u; }
    LDS(u32x4,L+L_VI+wro+c0)=v0; LDS(u32x4,L+L_VI+wro+(c0^16))=v1;
    u32x4 ph[4]; dsoftmax(p,rstd*LOG2E,ph);
    LDS(u32x4,L+L_PHI+wro+c0)=ph[0]; LDS(u32x4,L+L_PHI+wro+(c0^16))=ph[1];
    LDS(u32x4,L+L_PHI+2048+wro+c0)=ph[2]; LDS(u32x4,L+L_PHI+2048+wro+(c0^16))=ph[3]; }
  WAITV(0);
  STAMP(4);
  BAR();
  const bool act=j<14; const u32 rot=2*j;
  auto kap=[&](int n){ const u32 k=n+rot; return k>=28?k-28:k; };
  const rsrc_t rkv=mk(ws+WS_KV+j*65536,65536), r1=mk(w1,448*3584);
  const u32 v8=(lane>>3), p8=((lane&7)^(lane>>2&4))<<4, vw1=v8*3584+p8;
#define W1D(base,half,u) dma((base)+(u)*1024,vw1,r1,(kap((u)>>1)*2+((u)&1))*8*3584+j*256+(half)*128)
  if(w<2){
    const int ko=(8*hf+q4)*64+(sg&1)*8; const lptr k0=L+ko+(((sg>>1)^(2*hf))<<4),k1=L+ko+256+(((sg>>1)^(2*hf+1))<<4);
    f32x16 c={};
#pragma unroll
    for(int u=0;u<8;u++){ const int o=(u>>1)*4096+(u&1)*1024; c=mfma(tr2(k0+L_PHI+w*2048+o,k1+L_PHI+w*2048+o),tr2(k0+L_VI+o,k1+L_VI+o),c); }
    __builtin_amdgcn_raw_buffer_store_b128(pk8(c,0),rkv,i*4096+w*2048+lane*16,0,16);
    __builtin_amdgcn_raw_buffer_store_b128(pk8(c,8),rkv,i*4096+w*2048+1024+lane*16,0,16);
  } else if(act){
    const rsrc_t r0=mk(wo,448*896); const u32 vo=(lane>>2)*896+(lane&3)*16;
#pragma unroll 1
    for(int u=(w-2)*14;u<(w-1)*14;u++) dma(L_WO+u*1024,vo,r0,kap(u)*16*896+j*64);
  }
  { f32x4 cq={};
#pragma unroll
    for(int u=0;u<7;u++){ const lptr p=aQ+(u>>2)*16384+w*4096+(u&3)*1024; cq=m44(u32x2{0x3F803F80u,0x3F803F80u},tr(p),cq); cq=m44(u32x2{0x3F803F80u,0x3F803F80u},tr(p+256),cq); }
    LDS(float,L+L_MISC+2048+lane*16+w*4)=cq[0]; }
  STAMP(5);
  BAR();
  u32x4 phq[4];
  { f32x16 cQ={};
#pragma unroll
    for(int ks=0;ks<28;ks++){ const lptr p=aQ+((ks>>2)>>2)*16384+(ks&3)*4096+((ks>>2)&3)*1024; cQ=mfma(tr2(p,p+256),xf[ks],cQ);
      if(w>=2){ if(act) W1D(L_W1A,0,(w-2)*28+ks); }
      else if(ks==12){ WAITV(0); if(lane==0) setflag(ws,0,j,i,w,tag); STAMP(6); } }
    f32x4 a=LDS(f32x4,L+L_MISC+2048+lane*16);
    cQ=__builtin_amdgcn_mfma_f32_32x32x2f32((a[0]+a[1])+(a[2]+a[3]),nmean,cQ,0,0,0);
    f32x16 p=mfma(wp[0],pk8(cQ,0),f32x16{}); p=mfma(wp[1],pk8(cQ,8),p);
    dsoftmax(p,rstd*LOG2E,phq); }
  STAMP(7);
  if(w==0) poll(ws,0,j,(lane&2)==0,tag,lane);
  BAR();
  STAMP(8);
  const u32x2 id4={(lane&3)==0?0x3F80u:(lane&3)==1?0x3F800000u:0u,(lane&3)==2?0x3F80u:(lane&3)==3?0x3F800000u:0u};
  u32x4 xr0,xr1;
  { u32x4 g[16];
#pragma unroll
    for(int n=0;n<16;n++) g[n]=__builtin_amdgcn_raw_buffer_load_b128(rkv,w*1024+lane*16,((n+i)&15)*4096,16);
    xr0=__builtin_amdgcn_raw_buffer_load_b128(rx,t*896+hf*16+j*64,0,0); xr1=__builtin_amdgcn_raw_buffer_load_b128(rx,t*896+hf*16+32+j*64,0,0);
    f32x4 s[4]={};
#pragma unroll
    for(int n=0;n<16;n++){ s[2*(n&1)]=m44(id4,u32x2{g[n][0],g[n][1]},s[2*(n&1)]); s[2*(n&1)+1]=m44(id4,u32x2{g[n][2],g[n][3]},s[2*(n&1)+1]); }
    s[0]+=s[2]; s[1]+=s[3];
    LDS(u32x4,L+L_KVX+w*1024+lane*16)=u32x4{pk(s[0][0],s[0][1]),pk(s[0][2],s[0][3]),pk(s[1][0],s[1][1]),pk(s[1][2],s[1][3])}; }
  BAR();
  STAMP(9);
  const rsrc_t ro=mk(ws+WS_O+i*114688,114688);
  { f32x16 c={};
#pragma unroll
    for(int n=0;n<4;n++) c=mfma(LDS(u32x4,L+L_KVX+n*1024+lane*16),phq[n],c);
    const float rd=__builtin_amdgcn_rcpf(swp_hi(c[12])+1e-6f);
#pragma unroll
    for(int r=0;r<16;r++) c[r]*=rd;
    const u32x4 o0=pk8(c,0),o1=pk8(c,8); const u32 ub=w*28672+t*16+((7*j)>>1)*512;
    __builtin_amdgcn_raw_buffer_store_b128(o0,ro,ub+(odd+2*hf)*512,0,16);
    if(hf==0) __builtin_amdgcn_raw_buffer_store_b128(o1,ro,ub+(odd+1)*512,0,16);
    else __builtin_amdgcn_raw_buffer_store_b64(u32x2{o1[0],o1[1]},ro,ub+(odd?8:1536),0,16);
    WAITV(0); }
  if(lane==0) setflag(ws,1,i,j,w,tag);
  STAMP(10);
  const rsrc_t rh=mk(ws+WS_H+i*114688,114688), rp=mk(ws+WS_P+i*(14*114688),14*114688);
  if(act){
    if(w==0) poll(ws,1,i,true,tag,lane);
    else {
#pragma unroll 1
      for(int u=w-1;u<56;u+=3) W1D(L_W1B,1,u);
    }
    const int nq=(mu&4)|((mu&1)<<1)|((mu>>1)&1);
    if(w==0){ const int tq=t>>2, f=((tq&4)|((tq&1)<<1)|((tq>>1)&1))*4+(t&3)+hf*32;
      LDS(u32,L+L_MISC+2048+lane*4)=(u32)b1[j*128+f]<<16; LDS(u32,L+L_MISC+2048+256+lane*4)=(u32)b1[j*128+64+f]<<16; }
    BAR();
    STAMP(11);
    if(b==0&&tid==0) __hip_atomic_store((u32*)ws,tag,__ATOMIC_RELAXED,__HIP_MEMORY_SCOPE_AGENT);
    const rsrc_t rs=mk(ws+WS_HS+i*14336,14336);
    { u32x4 of[28];
#pragma unroll
      for(int n=0;n<28;n++) of[n]=__builtin_amdgcn_raw_buffer_load_b128(ro,w*28672+lane*16,kap(n)*1024,16);
      f32x16 c;
#pragma unroll
      for(int e=0;e<4;e++){ c[2*e]=__uint_as_float(xr0[e]<<16); c[2*e+1]=__uint_as_float(xr0[e]&0xffff0000u); c[8+2*e]=__uint_as_float(xr1[e]<<16); c[9+2*e]=__uint_as_float(xr1[e]&0xffff0000u); }
      const lptr aO=L+L_WO+rowo+nq*8;
#pragma unroll
      for(int n=0;n<28;n++) c=mfma(tr2(aO+n*1024,aO+n*1024+256),of[n],c);
      __builtin_amdgcn_raw_buffer_store_b128(pk8(c,0),rh,w*28672+j*2048+lane*16,0,16);
      __builtin_amdgcn_raw_buffer_store_b128(pk8(c,8),rh,w*28672+j*2048+1024+lane*16,0,16);
      float s1=0,s2=0;
#pragma unroll
      for(int r=0;r<16;r++){ s1+=c[r]; s2=__builtin_fmaf(c[r],c[r],s2); }
      s1=swp_add(s1); s2=swp_add(s2);
      if(hf==0) __builtin_amdgcn_raw_buffer_store_b64(u32x2{__float_as_uint(s1),__float_as_uint(s2)},rs,w*3584+j*256+t*8,0,16);
      WAITV(0); }
    if(lane==0) setflag(ws,2,i,j,w,tag);
    STAMP(13);
    BAR();
    const rsrc_t r2=mk(w2+(size_t)j*128*448,128*896); const u32 vw2=v8*896+p8;
#define W2D(c,u) dma(w2c(c)+(u)*1024,vw2,r2,(u)*7168+(c)*128)
    const lptr a0=L+(8*hf+q4)*128+(q4>>1)*64+nq*8; const lptr a1=L+(((8*hf+q4)*128+(q4>>1)*64+nq*8)^64);
    {
      const lptr p=((w&1)?a1:a0)+((w&2)?L_W1B:L_W1A); f32x4 cs={};
      if(w){
#pragma unroll 1
        for(int u=w-1;u<32;u+=3) dma(65536+u*1024,vw2,r2,(u&15)*7168+(u>>4)*128);
      }
#pragma unroll
      for(int n=0;n<28;n++){ cs=m44(u32x2{0x3F803F80u,0x3F803F80u},tr(p+n*2048),cs); cs=m44(u32x2{0x3F803F80u,0x3F803F80u},tr(p+n*2048+512),cs); }
      const float v=swp_add(cs[0]); if(hf==0) LDS(float,L+L_MISC+1024+w*128+t*4)=v; }
    STAMP(14);
    if(w==0) poll(ws,2,i,lane<56,tag,lane);
    BAR();
    STAMP(15);
    u32x4 hf_[28],fa[3][2],hb[8]; f32x16 c4[4],c5[8]; float sg_,K1,K2,Lg;
    { u32x2 st[7];
#pragma unroll
      for(int n=0;n<7;n++) st[n]=__builtin_amdgcn_raw_buffer_load_b64(rs,w*3584+n*512+lane*8,0,16);
#pragma unroll
      for(int n=0;n<28;n++) hf_[n]=__builtin_amdgcn_raw_buffer_load_b128(rh,w*28672+lane*16,kap(n)*1024,16);
      float cb[4];
#pragma unroll
      for(int T=0;T<4;T++) cb[T]=LDS(float,L+L_MISC+1024+hf*1024+T*128+t*4);
      float s1=0,s2=0;
#pragma unroll
      for(int n=0;n<7;n++){ s1+=__uint_as_float(st[n][0]); s2+=__uint_as_float(st[n][1]); }
      s1=swp_add(s1)*(-1.f/448); s2=swp_add(s2);
      const float v=__builtin_fmaxf(__builtin_fmaf(s2,1.f/448,-s1*s1),0.f), vv=__builtin_fmaf(v,1.00001f,1e-10f), rs_=__builtin_amdgcn_rsqf(vv);
      sg_=vv*rs_; K1=-2.302208198f*rs_; K2=-0.1029432397f*rs_*rs_*rs_; Lg=0.5f*__builtin_amdgcn_logf(vv);
      const float bb=hf?sg_:s1;
#pragma unroll
      for(int T=0;T<4;T++) c4[T]=__builtin_amdgcn_mfma_f32_32x32x2f32(cb[T],bb,f32x16{},0,0,0); }
    float t0,t1,e0,e1,g0;
#define GA(p) c4[(p)>>3][2*((p)&7)]
#define GB(p) c4[(p)>>3][2*((p)&7)+1]
#define G5(p) do{ float g1=GB(p)*e1; PINW(g0); PINW(g1); hb[(p)>>2][(p)&3]=pk(g0,g1); }while(0)
#define GS(q) do{ constexpr int p_=(q)>>2; \
      if constexpr(((q)&3)==0){ if constexpr(p_>0&&p_!=24) G5(p_>0?p_-1:0); t0=GA(p_)*GA(p_); PINW(t0); t1=GB(p_)*GB(p_); t0=__builtin_fmaf(t0,K2,K1); PINW(t0); t1=__builtin_fmaf(t1,K2,K1); PINW(t1);} \
      else if constexpr(((q)&3)==1){ t0=__builtin_fmaf(GA(p_),t0,Lg); PINW(t0); t1=__builtin_fmaf(GB(p_),t1,Lg); e0=__builtin_amdgcn_exp2f(t0); PINW(e0); PINW(t1);} \
      else if constexpr(((q)&3)==2){ e1=__builtin_amdgcn_exp2f(t1); e0+=sg_; e0=__builtin_amdgcn_rcpf(e0); PINW(e0); PINW(e1);} \
      else { e1+=sg_; e1=__builtin_amdgcn_rcpf(e1); g0=GA(p_)*e0; PINW(e1); PINW(g0);} }while(0)
lptr wb[3][2]={{a0,a1},{a0+65536,a1+65536},{a0+98304,a1+98304}};
#pragma unroll
    for(int q=1;q<3;q++){ PINW(wb[q][0]); PINW(wb[q][1]); }
#define RDA(s,h) do{ constexpr int o_=adr(s), wi_=o_>=98304?2:o_>=65536?1:0, r_=o_-(wi_==2?98304:wi_==1?65536:0); fa[(s)%3][h]=tr2(wb[wi_][h]+r_,wb[wi_][h]+r_+512); }while(0)
    CST(24); RDA(0,0); RDA(0,1); RDA(1,0); RDA(1,1); SB();
    sfor<0,56>([&](auto S){ constexpr int s=S.v,T=s<28?0:2,n=s<28?s:s-28;
      c4[T]=mfma(fa[s%3][0],hf_[n],c4[T]); PINW(c4[T]); SB();
      if constexpr(s==54){ WAITV(4); asm volatile("s_barrier":::"memory"); }
      RDA(s+2,0);
      if constexpr(s>=28) GS(2*(s-28)); SB();
      c4[T+1]=mfma(fa[s%3][1],hf_[n],c4[T+1]); PINW(c4[T+1]); SB();
      if constexpr(s==13) CST(25); if constexpr(s==27) CST(26); if constexpr(s==41) CST(28); if constexpr(s==55) CST(29);
      if constexpr(s==27||s==55) asm volatile("s_barrier":::"memory"); if constexpr(s==27) CST(27); if constexpr(s==55) CST(30);
      RDA(s+2,1);
      if constexpr(s>=28) GS(2*(s-28)+1);
      if constexpr(s>=27&&s<51&&(s&1)) W2D(2+((s-27)>>3),w*4+(((s-27)>>1)&3));
      SB(); });
    STAMP(16);
    const u32 vp=w*28672+lane*16, sp=j*114688;
#define PST(n,c) do{ __builtin_amdgcn_raw_buffer_store_b128(pk8(c,0),rp,vp+(n)*2048,sp,16); __builtin_amdgcn_raw_buffer_store_b128(pk8(c,8),rp,vp+(n)*2048+1024,sp,16); }while(0)
    sfor<56,112>([&](auto S){ constexpr int s=S.v,u=s-56, ks=u<32?(u>>3)*2+(u&1):(u&7), A=u<32?(u&6):((u>>3)&1)*2; constexpr bool first=u<32?ks==0:(u&7)==0;
      if constexpr(u==16) CST(31); if constexpr(u==24) CST(32); if constexpr(u==32) CST(33); if constexpr(u==40) CST(34); if constexpr(u==48) CST(35);
      if constexpr(u==16){ sfor<88,96>([&](auto Q){ GS(Q.v); }); G5(23); SB(); }
      if constexpr(u==24){ sfor<112,128>([&](auto Q){ GS(Q.v); }); G5(31); SB(); }
      if constexpr(first) c5[A]=mfma(fa[s%3][0],hb[ks],f32x16{}); else c5[A]=mfma(fa[s%3][0],hb[ks],c5[A]);
      PINW(c5[A]); SB();
      if constexpr(u==30){ WAITV(8); asm volatile("s_barrier":::"memory"); }
      if constexpr(s+2<112) RDA(s+2,0);
      if constexpr(u<16) GS(56+2*u); else if constexpr(u<24) GS(96+2*(u-16));
      if constexpr(u>=27&&u<=33&&(u&1)) PST(u-27,c5[u-27]);
      if constexpr(u==42||u==50) PST(8+((u-42)>>2),c5[((u-42)>>2)&2]);
      SB();
      if constexpr(first) c5[A+1]=mfma(fa[s%3][1],hb[ks],f32x16{}); else c5[A+1]=mfma(fa[s%3][1],hb[ks],c5[A+1]);
      PINW(c5[A+1]); SB();
      if constexpr(s+2<112) RDA(s+2,1);
      if constexpr(u<16) GS(57+2*u); else if constexpr(u<24) GS(97+2*(u-16));
      if constexpr(u<8) W2D(5+(u>>2),w*4+(u&3));
      if constexpr(u>=27&&u<=33&&(u&1)) PST(u-26,c5[u-26]);
      if constexpr(u==42||u==50) PST(9+((u-42)>>2),c5[(((u-42)>>2)&2)+1]);
      SB(); });
    CST(36); PST(12,c5[0]); PST(13,c5[1]); CST(37);
    STAMP(17);
    WAITV(0);
    if(lane==0) setflag(ws,3,i,j,w,tag);
    STAMP(19);
  }
  { const u32 U=w*14+(lane>>3), vb=lane<56?U*16:0x80000000u, vq=lane<56?(j>>2)*28672+U*512+((j&3)*8+(lane&7))*16:0x80000000u;
    const rsrc_t rb=mk(b2,896);
    const u32x4 q0=__builtin_amdgcn_raw_buffer_load_b128(rb,vb,0,0),q1=__builtin_amdgcn_raw_buffer_load_b128(rb,vb+112,0,0);
    if(w==0) poll(ws,3,i,lane<56&&(lane&3)==(j>>2),tag,lane);
    BAR();
    STAMP(20);
    u32x4 g[15][2];
#pragma unroll
    for(int n=0;n<14;n++){ const u32 k=n+j, so=(k>=28?k-28:k>=14?k-14:k)*114688; g[n][0]=__builtin_amdgcn_raw_buffer_load_b128(rp,vq,so,16); g[n][1]=__builtin_amdgcn_raw_buffer_load_b128(rp,vq+3584,so,16); }
    g[14][0]=__builtin_amdgcn_raw_buffer_load_b128(rh,vq,0,16); g[14][1]=__builtin_amdgcn_raw_buffer_load_b128(rh,vq+3584,0,16);
    f32x4 s[4]; s[0]=m44(id4,u32x2{q0[0],q0[1]},f32x4{}); s[1]=m44(id4,u32x2{q0[2],q0[3]},f32x4{}); s[2]=m44(id4,u32x2{q1[0],q1[1]},f32x4{}); s[3]=m44(id4,u32x2{q1[2],q1[3]},f32x4{});
#pragma unroll
    for(int n=0;n<15;n++){ s[0]=m44(id4,u32x2{g[n][0][0],g[n][0][1]},s[0]); s[1]=m44(id4,u32x2{g[n][0][2],g[n][0][3]},s[1]); s[2]=m44(id4,u32x2{g[n][1][0],g[n][1][1]},s[2]); s[3]=m44(id4,u32x2{g[n][1][2],g[n][1][3]},s[3]); }
    const rsrc_t rout=mk(out+(size_t)(i*128+j*8)*448,8*896); const u32 vo=lane<56?(lane&7)*896+U*16:0x80000000u;
    __builtin_amdgcn_raw_buffer_store_b128(u32x4{pk(s[0][0],s[0][1]),pk(s[0][2],s[0][3]),pk(s[1][0],s[1][1]),pk(s[1][2],s[1][3])},rout,vo,0,0);
    __builtin_amdgcn_raw_buffer_store_b128(u32x4{pk(s[2][0],s[2][1]),pk(s[2][2],s[2][3]),pk(s[3][0],s[3][1]),pk(s[3][2],s[3][3])},rout,vo+112,0,0); }
  STAMP(21); CST(47);
}
}
extern "C" void kernel_launch(void* const* d_in,const int* in_sizes,int n_in,void* d_out,int out_size,void* d_ws,size_t ws_size,hipStream_t stream){
  typedef const unsigned short* P;
  (void)hipFuncSetAttribute((const void*)fk::fused,hipFuncAttributeMaxDynamicSharedMemorySize,163840);
  fk::fused<<<256,256,163840,stream>>>((P)d_in[0],(P)d_in[1],(P)d_in[2],(P)d_in[3],(P)d_in[5],(P)d_in[6],(P)d_in[7],(P)d_in[8],(unsigned short*)d_out,(char*)d_ws);
}
```

```cpp
#define PMUL 0
#include <hip/hip_runtime.h>
#include <stdint.h>
namespace fk {
typedef unsigned short u16; typedef unsigned u32;
typedef __attribute__((ext_vector_type(4))) u32 u32x4;
typedef __attribute__((ext_vector_type(2))) u32 u32x2;
typedef __attribute__((ext_vector_type(8))) __bf16 bf16x8;
typedef __attribute__((ext_vector_type(2))) __bf16 bf16x2;
typedef __attribute__((ext_vector_type(2))) float f32x2;
typedef __attribute__((ext_vector_type(4))) float f32x4;
typedef __attribute__((ext_vector_type(16))) float f32x16;
typedef short v4i16_t __attribute__((ext_vector_type(4)));
typedef __attribute__((address_space(3))) char* lptr;
typedef __amdgpu_buffer_rsrc_t rsrc_t;
#define DI __device__ __forceinline__
#define LDS(T,a) (*(__attribute__((address_space(3))) T*)(a))
#define STAMP(n)
#define CST(n)
constexpr u32 WS_FLAG=4096, WS_KV=1u<<20, WS_O=2u<<20, WS_H=4u<<20, WS_HS=6u<<20, WS_P=8u<<20;
constexpr int L_WK=98304, L_WV=126976, L_MISC=155648, L_KVX=L_MISC+3072;
constexpr int L_WQ=0, L_PHI=32768, L_VI=49152, L_WO=65536, L_W1A=98304, L_W1B=0;
constexpr int xs(int c){ return (c%6==0?4:c%6==5?5:c%6-1)*16384; }
constexpr int w2c(int c){ return c<5?65536+c*16384:(c-5)*16384; }
constexpr float LOG2E=1.4426950408889634f;

template<int V> struct IC{ static constexpr int v=V; };
template<int I,int N,class F> DI void sfor(F&& f){ if constexpr(I<N){ f(IC<I>{}); sfor<I+1,N>(f); } }
constexpr int adr(int s){ return s<28?L_W1A+s*2048: s<56?L_W1B+(s-28)*2048: s<88?w2c(((s-56)>>1)&3)+(((s-56)>>3)*2+((s-56)&1))*2048: w2c(4+((s-88)>>3))+((s-88)&7)*2048; }
DI rsrc_t mk(const void* p,u32 bytes){ return __builtin_amdgcn_make_buffer_rsrc((void*)p,0,bytes,0x00020000); }
DI void dma(u32 l,u32 voff,rsrc_t r,u32 soff){ asm volatile("s_mov_b32 m0, %0\n\ts_nop 0\n\tbuffer_load_dwordx4 %1, %2, %3 offen lds"::"s"(l),"v"(voff),"s"(r),"s"(soff):"memory"); }
#define WAITV(n) asm volatile("s_waitcnt vmcnt(" #n ")":::"memory")
#define BAR() asm volatile("s_waitcnt lgkmcnt(0)\n\ts_barrier":::"memory")
#define PIN(x) asm volatile(""::"v"(x))
#define PINW(x) asm volatile("":"+v"(x))
#define SB() __builtin_amdgcn_sched_barrier(0)
DI f32x16 mfma(u32x4 a,u32x4 b,f32x16 c){ return __builtin_amdgcn_mfma_f32_32x32x16_bf16(__builtin_bit_cast(bf16x8,a),__builtin_bit_cast(bf16x8,b),c,0,0,0); }
DI f32x4 m44(u32x2 a,u32x2 b,f32x4 c){ return __builtin_amdgcn_mfma_f32_4x4x4bf16_1k(__builtin_bit_cast(v4i16_t,a),__builtin_bit_cast(v4i16_t,b),c,0,0,0); }
DI u32x2 tr(lptr p){ return __builtin_bit_cast(u32x2,__builtin_amdgcn_ds_read_tr16_b64_v4i16((__attribute__((address_space(3))) v4i16_t*)p)); }
DI u32x4 tr2(lptr p0,lptr p1){ u32x2 a=tr(p0),b=tr(p1); return u32x4{a[0],a[1],b[0],b[1]}; }
DI u32 pk(float lo,float hi){ return __builtin_bit_cast(u32,__builtin_convertvector(f32x2{lo,hi},bf16x2)); }
DI float dot2(u32 a,u32 b,float c){ return __builtin_amdgcn_fdot2_f32_bf16(__builtin_bit_cast(bf16x2,a),__builtin_bit_cast(bf16x2,b),c,false); }
DI float swp_lo(float v){ auto r=__builtin_amdgcn_permlane32_swap(__float_as_uint(v),__float_as_uint(v),false,false); return __uint_as_float(r[0]); }
DI float swp_hi(float v){ auto r=__builtin_amdgcn_permlane32_swap(__float_as_uint(v),__float_as_uint(v),false,false); return __uint_as_float(r[1]); }
DI float swp_add(float v){ auto r=__builtin_amdgcn_permlane32_swap(__float_as_uint(v),__float_as_uint(v),false,false); return __uint_as_float(r[0])+__uint_as_float(r[1]); }
DI float swp_max(float v){ auto r=__builtin_amdgcn_permlane32_swap(__float_as_uint(v),__float_as_uint(v),false,false); return __builtin_fmaxf(__uint_as_float(r[0]),__uint_as_float(r[1])); }
DI float swp_min(float v){ auto r=__builtin_amdgcn_permlane32_swap(__float_as_uint(v),__float_as_uint(v),false,false); return __builtin_fminf(__uint_as_float(r[0]),__uint_as_float(r[1])); }
DI u32x4 pk8(const f32x16& c,int b){ return u32x4{pk(c[b],c[b+1]),pk(c[b+2],c[b+3]),pk(c[b+4],c[b+5]),pk(c[b+6],c[b+7])}; }
DI void dsoftmax(const f32x16& p,float c,u32x4* ph){
  float mx=p[0],mn=p[0];
#pragma unroll
  for(int r=1;r<14;r++){ mx=__builtin_fmaxf(mx,p[r]); mn=__builtin_fminf(mn,p[r]); }
  mx=swp_max(mx); mn=swp_min(mn);
  const float a=-mx*c,b=mn*c; f32x16 e,g; float s=0,u=0;
#pragma unroll
  for(int r=0;r<14;r++){ e[r]=__builtin_amdgcn_exp2f(__builtin_fmaf(p[r],c,a)); g[r]=__builtin_amdgcn_exp2f(__builtin_fmaf(p[r],-c,b)); s+=e[r]; u+=g[r]; }
  s=__builtin_amdgcn_rcpf(swp_add(s)); u=__builtin_amdgcn_rcpf(swp_add(u));
#pragma unroll
  for(int r=0;r<14;r++){ e[r]*=s; g[r]*=u; }
  ph[0]=pk8(e,0); ph[1]=u32x4{pk(e[8],e[9]),pk(e[10],e[11]),pk(e[12],e[13]),0u};
  ph[2]=pk8(g,0); ph[3]=u32x4{pk(g[8],g[9]),pk(g[10],g[11]),pk(g[12],g[13]),0u};
}
DI void setflag(char* ws,int ph,int grp,int mem,int w,u32 tag){ __hip_atomic_store((u32*)(ws+WS_FLAG+((ph*16+grp)*16+mem)*128+w*4),tag,__ATOMIC_RELAXED,__HIP_MEMORY_SCOPE_AGENT); }
DI void poll(char* ws,int ph,int grp,bool on,u32 tag,int lane){
  u32* f=(u32*)(ws+WS_FLAG+((ph*16+grp)*16+(lane>>2))*128+(lane&3)*4);
  for(int it=0;it<40000;it++){ u32 v=tag; if(on) v=__hip_atomic_load(f,__ATOMIC_RELAXED,__HIP_MEMORY_SCOPE_AGENT); if(__all(v==tag)) break; __builtin_amdgcn_s_sleep(1); }
}

__global__ __launch_bounds__(256,2) void fused(const u16* __restrict__ x,const u16* __restrict__ wqkv,const u16* __restrict__ wphi,const u16* __restrict__ wo,
    const u16* __restrict__ w1,const u16* __restrict__ b1,const u16* __restrict__ w2,const u16* __restrict__ b2,u16* __restrict__ out,char* __restrict__ ws
    ){
  extern __shared__ __attribute__((aligned(16))) char smem[];
  const lptr L=(lptr)smem;
  const int tid=threadIdx.x,lane=tid&63,w=__builtin_amdgcn_readfirstlane(tid>>6),t=lane&31,hf=lane>>5;
  const int b=blockIdx.x,i=(b&7)*2+((b>>3)&1),j=b>>4,odd=j&1;
  STAMP(0); CST(46);
  const u32 epoch=*(const __attribute__((address_space(4))) u32*)ws;
  const rsrc_t rw=mk(wqkv,448*2688), rx=mk(x+(size_t)(i*128+w*32)*448,32*896);
  const u32 vw=(lane>>2)*2688+(lane&3)*16, hoff=56*j-8*odd+w*16*2688;
  const u32 vx0=(lane>>3)*896+(((lane&7)^(lane>>4))<<4), vx1=vx0^64;
#define WKDMA(c) dma(L_WK+w*1024+(c)*4096,vw,rw,hoff+896+(c)*64*2688)
#define WVDMA(c) dma(L_WV+w*1024+(c)*4096,vw,rw,hoff+1792+(c)*64*2688)
#define XD(c,g) dma(xs(c)+w*4096+(g)*1024,((g)&1)?vx1:vx0,rx,(g)*7168+(c)*128)
#define QD(u) dma(L_WQ+((u)>>2)*16384+w*4096+((u)&3)*1024,vw,rw,hoff+(u)*64*2688)
#define XDMA(c) do{ XD(c,0); XD(c,1); XD(c,2); XD(c,3); }while(0)
  WKDMA(0); WVDMA(0); WKDMA(1); WVDMA(1); XDMA(0);
  dma(L_KVX+(w&1)*1024,(tid&127)*16,mk(wphi+j*784,1568),0);
  WKDMA(2); WVDMA(2); XDMA(1); WKDMA(3); WVDMA(3); XDMA(2);
  const int q4=(lane&15)>>2,mu=((lane>>4)&1)*4+(lane&3),sg=4*(mu&1)+(mu>>1);
  const int rowo=(8*hf+q4)*64;
  const lptr aK=L+L_WK+rowo+((mu+odd)&7)*8;
  const lptr aV=L+L_WV+rowo+(odd?(sg==7?0:((sg+1)%7)+1):sg)*8;
  const lptr aQ=L+L_WQ+rowo+((mu+odd)&7)*8;
  const lptr oK=aK+w*1024,oV=aV+w*1024,pp=L+L_WK+w*1024+(lane&15)*64+(odd?0:56);
  const lptr xb=L+(w*32+t)*128; const int sw=hf^((t>>1)&7);
  u32 tag=epoch*0x9E3779B1u+0x7F4A7C15u; asm volatile("":"+s"(tag));
  STAMP(12);
  float ck=0,cv=0;
const u32x2 pv={0x3F80u,0u};
#define PATCH(c) asm volatile("s_mov_b64 exec, 0xffff\n\tds_write_b64 %0, %1 offset:%2\n\ts_mov_b64 exec, -1"::"v"(pp),"v"(pv),"i"((c)*4096):"memory")
#define OWNRD(c) ok=tr2(oK+(c)*4096,oK+(c)*4096+256); ov=tr2(oV+(c)*4096,oV+(c)*4096+256)
#define OWNSUM() do{ _Pragma("unroll") for(int e=0;e<4;e++){ ck=dot2(ok[e],0x3F803F80u,ck); cv=dot2(ov[e],0x3F803F80u,cv); } PINW(ck); PINW(cv); }while(0)
  u32x4 ok,ov;
  WAITV(19); PATCH(0); OWNRD(0); OWNSUM();
  STAMP(1);
  BAR();
  STAMP(2);
  u32x4 xf[28],fk[4],fv[4],wp[2]; f32x16 cK={},cV={},cG={};
#define XRD(ks) xf[ks]=LDS(u32x4,xb+xs((ks)>>2)+((sw^(((ks)&3)*2))<<4))
#define ARD(ks) fk[(ks)&3]=tr2(aK+(ks)*1024,aK+(ks)*1024+256); fv[(ks)&3]=tr2(aV+(ks)*1024,aV+(ks)*1024+256)
#define MG(ks) cG=mfma(xf[ks],xf[ks],cG); PINW(cG)
#define MK(ks) cK=mfma(fk[(ks)&3],xf[ks],cK); PINW(cK)
#define MV(ks) cV=mfma(fv[(ks)&3],xf[ks],cV); PINW(cV)
#define STAGE(c,N,D0,D1,D2,D3,D4,D5,EXTRA) do{ \
    ARD(4*(c)); ARD(4*(c)+1); SB(); WAITV(N); XRD(4*(c)); XRD(4*(c)+1); XRD(4*(c)+2); XRD(4*(c)+3); SB(); \
    MG(4*(c)); SB(); ARD(4*(c)+2); SB(); MK(4*(c)); SB(); if((c)<6){ PATCH((c)+1); OWNRD((c)+1); } SB(); MV(4*(c)); SB(); \
    MG(4*(c)+1); SB(); ARD(4*(c)+3); SB(); MK(4*(c)+1); SB(); D0; SB(); MV(4*(c)+1); SB(); D1; SB(); \
    MG(4*(c)+2); SB(); if((c)<6) OWNSUM(); SB(); MK(4*(c)+2); SB(); D2; SB(); MV(4*(c)+2); SB(); D3; SB(); \
    MG(4*(c)+3); SB(); D4; SB(); MK(4*(c)+3); SB(); D5; EXTRA; SB(); MV(4*(c)+3); SB(); if((c)<6) BAR(); }while(0)
#define NOP_ do{}while(0)
  STAGE(0,12,WKDMA(4),WVDMA(4),XD(3,0),XD(3,1),XD(3,2),XD(3,3),NOP_);
  { const bool mv=(t&26)!=26; const lptr p=L+L_KVX+(t<26?t:t-2)*2+hf*224; u32 e[16];
#pragma unroll
    for(int q=0;q<16;q++){ const int d=16*(q>>3)+8*((q>>2)&1)+(q&3); e[q]=LDS(u16,p+d*56); }
#pragma unroll
    for(int q=0;q<8;q++){ u32 v=e[2*q]|(e[2*q+1]<<16); v=(mv&&(q<6||hf==0))?v:0u; asm volatile("":"+v"(v)); wp[q>>2][q&3]=v; } }
  STAGE(1,12,WKDMA(5),WVDMA(5),XD(4,0),XD(4,1),XD(4,2),XD(4,3),NOP_);
  STAGE(2,12,WKDMA(6),WVDMA(6),XD(5,0),XD(5,1),XD(5,2),XD(5,3),NOP_);
  STAGE(3,12,XD(6,0),XD(6,1),XD(6,2),XD(6,3),NOP_,NOP_,NOP_);
  STAGE(4,10,QD(0),QD(1),QD(2),QD(3),NOP_,NOP_,NOP_);
  STAGE(5,8,QD(4),QD(5),QD(6),NOP_,NOP_,NOP_,{ LDS(float,L+L_MISC+lane*16+w*4)=ck; LDS(float,L+L_MISC+1024+lane*16+w*4)=cv; });
  STAGE(6,7,NOP_,NOP_,NOP_,NOP_,NOP_,NOP_,NOP_);
  STAMP(3);
  float rstd,nmean;
  { f32x4 a=LDS(f32x4,L+L_MISC+lane*16),c=LDS(f32x4,L+L_MISC+1024+lane*16);
    nmean=swp_hi(cK[12])*(-1.f/448);
    cK=__builtin_amdgcn_mfma_f32_32x32x2f32((a[0]+a[1])+(a[2]+a[3]),nmean,cK,0,0,0);
    cV=__builtin_amdgcn_mfma_f32_32x32x2f32((c[0]+c[1])+(c[2]+c[3]),nmean,cV,0,0,0);
    float g8[8],g4[4];
#pragma unroll
    for(int e=0;e<8;e++){ float u0=cG[2*e],u1=cG[2*e+1]; asm("":"+v"(u0)); g8[e]=(t&1)?u1:u0; }
#pragma unroll
    for(int e=0;e<4;e++){ float u0=g8[2*e]; asm("":"+v"(u0)); g4[e]=(t&2)?g8[2*e+1]:u0; }
    float g2a=(t&8)?g4[1]:g4[0],g2b=(t&8)?g4[3]:g4[2],g1=(t&16)?g2b:g2a;
    auto r=__builtin_amdgcn_permlane32_swap(__float_as_uint(g1),__float_as_uint(g1),false,false);
    float ss=__uint_as_float((t&4)?r[1]:r[0]);
    rstd=__builtin_amdgcn_rsqf(__builtin_fmaxf(__builtin_fmaf(ss,1.f/448,-nmean*nmean),0.f)+1e-5f); }
  const int wro=(w*4096+t*64),c0=((2*hf)^((t>>2)&3))<<4;
  { f32x16 p=mfma(wp[0],pk8(cK,0),f32x16{}); p=mfma(wp[1],pk8(cK,8),p);
#pragma unroll
    for(int r=0;r<16;r++) cV[r]*=rstd;
    u32x4 v0=pk8(cV,0),v1=pk8(cV,8); if(hf){ v1[2]=0x3F80u; v1[3]=0u; }
    LDS(u32x4,L+L_VI+wro+c0)=v0; LDS(u32x4,L+L_VI+wro+(c0^16))=v1;
    u32x4 ph[4]; dsoftmax(p,rstd*LOG2E,ph);
    LDS(u32x4,L+L_PHI+wro+c0)=ph[0]; LDS(u32x4,L+L_PHI+wro+(c0^16))=ph[1];
    LDS(u32x4,L+L_PHI+2048+wro+c0)=ph[2]; LDS(u32x4,L+L_PHI+2048+wro+(c0^16))=ph[3]; }
  WAITV(0);
  STAMP(4);
  BAR();
  const bool act=j<14; const u32 rot=2*j;
  auto kap=[&](int n){ const u32 k=n+rot; return k>=28?k-28:k; };
  const rsrc_t rkv=mk(ws+WS_KV+j*65536,65536), r1=mk(w1,448*3584);
  const u32 v8=(lane>>3), p8=((lane&7)^(lane>>2&4))<<4, vw1=v8*3584+p8;
#define W1D(base,half,u) dma((base)+(u)*1024,vw1,r1,(kap((u)>>1)*2+((u)&1))*8*3584+j*256+(half)*128)
  if(w<2){
    const int ko=(8*hf+q4)*64+(sg&1)*8; const lptr k0=L+ko+(((sg>>1)^(2*hf))<<4),k1=L+ko+256+(((sg>>1)^(2*hf+1))<<4);
    f32x16 c={};
#pragma unroll
    for(int u=0;u<8;u++){ const int o=(u>>1)*4096+(u&1)*1024; c=mfma(tr2(k0+L_PHI+w*2048+o,k1+L_PHI+w*2048+o),tr2(k0+L_VI+o,k1+L_VI+o),c); }
    __builtin_amdgcn_raw_buffer_store_b128(pk8(c,0),rkv,i*4096+w*2048+lane*16,0,16);
    __builtin_amdgcn_raw_buffer_store_b128(pk8(c,8),rkv,i*4096+w*2048+1024+lane*16,0,16);
  } else if(act){
    const rsrc_t r0=mk(wo,448*896); const u32 vo=(lane>>2)*896+(lane&3)*16;
#pragma unroll 1
    for(int u=(w-2)*14;u<(w-1)*14;u++) dma(L_WO+u*1024,vo,r0,kap(u)*16*896+j*64);
  }
  { f32x4 cq={};
#pragma unroll
    for(int u=0;u<7;u++){ const lptr p=aQ+(u>>2)*16384+w*4096+(u&3)*1024; cq=m44(u32x2{0x3F803F80u,0x3F803F80u},tr(p),cq); cq=m44(u32x2{0x3F803F80u,0x3F803F80u},tr(p+256),cq); }
    LDS(float,L+L_MISC+2048+lane*16+w*4)=cq[0]; }
  STAMP(5);
  BAR();
  u32x4 phq[4];
  { f32x16 cQ={};
#pragma unroll
    for(int ks=0;ks<28;ks++){ const lptr p=aQ+((ks>>2)>>2)*16384+(ks&3)*4096+((ks>>2)&3)*1024; cQ=mfma(tr2(p,p+256),xf[ks],cQ);
      if(w>=2){ if(act) W1D(L_W1A,0,(w-2)*28+ks); }
      else if(ks==12){ WAITV(0); if(lane==0) setflag(ws,0,j,i,w,tag); STAMP(6); } }
    f32x4 a=LDS(f32x4,L+L_MISC+2048+lane*16);
    cQ=__builtin_amdgcn_mfma_f32_32x32x2f32((a[0]+a[1])+(a[2]+a[3]),nmean,cQ,0,0,0);
    f32x16 p=mfma(wp[0],pk8(cQ,0),f32x16{}); p=mfma(wp[1],pk8(cQ,8),p);
    dsoftmax(p,rstd*LOG2E,phq); }
  STAMP(7);
  if(w==0) poll(ws,0,j,(lane&2)==0,tag,lane);
  BAR();
  STAMP(8);
  const u32x2 id4={(lane&3)==0?0x3F80u:(lane&3)==1?0x3F800000u:0u,(lane&3)==2?0x3F80u:(lane&3)==3?0x3F800000u:0u};
  u32x4 xr0,xr1;
  { u32x4 g[16];
#pragma unroll
    for(int n=0;n<16;n++) g[n]=__builtin_amdgcn_raw_buffer_load_b128(rkv,w*1024+lane*16,((n+i)&15)*4096,16);
    xr0=__builtin_amdgcn_raw_buffer_load_b128(rx,t*896+hf*16+j*64,0,0); xr1=__builtin_amdgcn_raw_buffer_load_b128(rx,t*896+hf*16+32+j*64,0,0);
    f32x4 s[4]={};
#pragma unroll
    for(int n=0;n<16;n++){ s[2*(n&1)]=m44(id4,u32x2{g[n][0],g[n][1]},s[2*(n&1)]); s[2*(n&1)+1]=m44(id4,u32x2{g[n][2],g[n][3]},s[2*(n&1)+1]); }
    s[0]+=s[2]; s[1]+=s[3];
    LDS(u32x4,L+L_KVX+w*1024+lane*16)=u32x4{pk(s[0][0],s[0][1]),pk(s[0][2],s[0][3]),pk(s[1][0],s[1][1]),pk(s[1][2],s[1][3])}; }
  BAR();
  STAMP(9);
  const rsrc_t ro=mk(ws+WS_O+i*114688,114688);
  { f32x16 c={};
#pragma unroll
    for(int n=0;n<4;n++) c=mfma(LDS(u32x4,L+L_KVX+n*1024+lane*16),phq[n],c);
    const float rd=__builtin_amdgcn_rcpf(swp_hi(c[12])+1e-6f);
#pragma unroll
    for(int r=0;r<16;r++) c[r]*=rd;
    const u32x4 o0=pk8(c,0),o1=pk8(c,8); const u32 ub=w*28672+t*16+((7*j)>>1)*512;
    __builtin_amdgcn_raw_buffer_store_b128(o0,ro,ub+(odd+2*hf)*512,0,16);
    if(hf==0) __builtin_amdgcn_raw_buffer_store_b128(o1,ro,ub+(odd+1)*512,0,16);
    else __builtin_amdgcn_raw_buffer_store_b64(u32x2{o1[0],o1[1]},ro,ub+(odd?8:1536),0,16);
    WAITV(0); }
  if(lane==0) setflag(ws,1,i,j,w,tag);
  STAMP(10);
  const rsrc_t rh=mk(ws+WS_H+i*114688,114688), rp=mk(ws+WS_P+i*(14*114688),14*114688);
  if(act){
    if(w==0) poll(ws,1,i,true,tag,lane);
    else {
#pragma unroll 1
      for(int u=w-1;u<56;u+=3) W1D(L_W1B,1,u);
    }
    const int nq=(mu&4)|((mu&1)<<1)|((mu>>1)&1);
    if(w==0){ const int tq=t>>2, f=((tq&4)|((tq&1)<<1)|((tq>>1)&1))*4+(t&3)+hf*32;
      LDS(u32,L+L_MISC+2048+lane*4)=(u32)b1[j*128+f]<<16; LDS(u32,L+L_MISC+2048+256+lane*4)=(u32)b1[j*128+64+f]<<16; }
    BAR();
    STAMP(11);
    if(b==0&&tid==0) __hip_atomic_store((u32*)ws,tag,__ATOMIC_RELAXED,__HIP_MEMORY_SCOPE_AGENT);
    const rsrc_t rs=mk(ws+WS_HS+i*14336,14336);
    { u32x4 of[28];
#pragma unroll
      for(int n=0;n<28;n++) of[n]=__builtin_amdgcn_raw_buffer_load_b128(ro,w*28672+lane*16,kap(n)*1024,16);
      f32x16 c;
#pragma unroll
      for(int e=0;e<4;e++){ c[2*e]=__uint_as_float(xr0[e]<<16); c[2*e+1]=__uint_as_float(xr0[e]&0xffff0000u); c[8+2*e]=__uint_as_float(xr1[e]<<16); c[9+2*e]=__uint_as_float(xr1[e]&0xffff0000u); }
      const lptr aO=L+L_WO+rowo+nq*8;
#pragma unroll
      for(int n=0;n<28;n++) c=mfma(tr2(aO+n*1024,aO+n*1024+256),of[n],c);
      __builtin_amdgcn_raw_buffer_store_b128(pk8(c,0),rh,w*28672+j*2048+lane*16,0,16);
      __builtin_amdgcn_raw_buffer_store_b128(pk8(c,8),rh,w*28672+j*2048+1024+lane*16,0,16);
      float s1=0,s2=0;
#pragma unroll
      for(int r=0;r<16;r++){ s1+=c[r]; s2=__builtin_fmaf(c[r],c[r],s2); }
      s1=swp_add(s1); s2=swp_add(s2);
      if(hf==0) __builtin_amdgcn_raw_buffer_store_b64(u32x2{__float_as_uint(s1),__float_as_uint(s2)},rs,w*3584+j*256+t*8,0,16);
      WAITV(0); }
    if(lane==0) setflag(ws,2,i,j,w,tag);
    STAMP(13);
    BAR();
    const rsrc_t r2=mk(w2+(size_t)j*128*448,128*896); const u32 vw2=v8*896+p8;
#define W2D(c,u) dma(w2c(c)+(u)*1024,vw2,r2,(u)*7168+(c)*128)
    const lptr a0=L+(8*hf+q4)*128+(q4>>1)*64+nq*8; const lptr a1=L+(((8*hf+q4)*128+(q4>>1)*64+nq*8)^64);
    {
      const lptr p=((w&1)?a1:a0)+((w&2)?L_W1B:L_W1A); f32x4 cs={};
      if(w){
#pragma unroll 1
        for(int u=w-1;u<32;u+=3) dma(65536+u*1024,vw2,r2,(u&15)*7168+(u>>4)*128);
      }
#pragma unroll
      for(int n=0;n<28;n++){ cs=m44(u32x2{0x3F803F80u,0x3F803F80u},tr(p+n*2048),cs); cs=m44(u32x2{0x3F803F80u,0x3F803F80u},tr(p+n*2048+512),cs); }
      const float v=swp_add(cs[0]); if(hf==0) LDS(float,L+L_MISC+1024+w*128+t*4)=v; }
    STAMP(14);
    if(w==0) poll(ws,2,i,lane<56,tag,lane);
    BAR();
    STAMP(15);
    u32x4 hf_[28],fa[3][2],hb[8]; f32x16 c4[4],c5[8]; float sg_,K1,K2,Lg;
    { u32x2 st[7];
#pragma unroll
      for(int n=0;n<7;n++) st[n]=__builtin_amdgcn_raw_buffer_load_b64(rs,w*3584+n*512+lane*8,0,16);
#pragma unroll
      for(int n=0;n<28;n++) hf_[n]=__builtin_amdgcn_raw_buffer_load_b128(rh,w*28672+lane*16,kap(n)*1024,16);
      float cb[4];
#pragma unroll
      for(int T=0;T<4;T++) cb[T]=LDS(float,L+L_MISC+1024+hf*1024+T*128+t*4);
      float s1=0,s2=0;
#pragma unroll
      for(int n=0;n<7;n++){ s1+=__uint_as_float(st[n][0]); s2+=__uint_as_float(st[n][1]); }
      s1=swp_add(s1)*(-1.f/448); s2=swp_add(s2);
      const float v=__builtin_fmaxf(__builtin_fmaf(s2,1.f/448,-s1*s1),0.f), vv=__builtin_fmaf(v,1.00001f,1e-10f), rs_=__builtin_amdgcn_rsqf(vv);
      sg_=vv*rs_; K1=-2.302208198f*rs_; K2=-0.1029432397f*rs_*rs_*rs_; Lg=0.5f*__builtin_amdgcn_logf(vv);
      const float bb=hf?sg_:s1;
#pragma unroll
      for(int T=0;T<4;T++) c4[T]=__builtin_amdgcn_mfma_f32_32x32x2f32(cb[T],bb,f32x16{},0,0,0); }
    float t0,t1,e0,e1,g0;
#define GA(p) c4[(p)>>3][2*((p)&7)]
#define GB(p) c4[(p)>>3][2*((p)&7)+1]
#define G5(p) do{ float g1=GB(p)*e1; PINW(g0); PINW(g1); hb[(p)>>2][(p)&3]=pk(g0,g1); }while(0)
#define GS(q) do{ constexpr int p_=(q)>>2; \
      if constexpr(((q)&3)==0){ if constexpr(p_>0&&p_!=24) G5(p_>0?p_-1:0); t0=GA(p_)*GA(p_); PINW(t0); t1=GB(p_)*GB(p_); t0=__builtin_fmaf(t0,K2,K1); PINW(t0); t1=__builtin_fmaf(t1,K2,K1); PINW(t1);} \
      else if constexpr(((q)&3)==1){ t0=__builtin_fmaf(GA(p_),t0,Lg); PINW(t0); t1=__builtin_fmaf(GB(p_),t1,Lg); e0=__builtin_amdgcn_exp2f(t0); PINW(e0); PINW(t1);} \
      else if constexpr(((q)&3)==2){ e1=__builtin_amdgcn_exp2f(t1); e0+=sg_; e0=__builtin_amdgcn_rcpf(e0); PINW(e0); PINW(e1);} \
      else { e1+=sg_; e1=__builtin_amdgcn_rcpf(e1); g0=GA(p_)*e0; PINW(e1); PINW(g0);} }while(0)
lptr wb[3][2]={{a0,a1},{a0+65536,a1+65536},{a0+98304,a1+98304}};
#pragma unroll
    for(int q=1;q<3;q++){ PINW(wb[q][0]); PINW(wb[q][1]); }
#define RDA(s,h) do{ constexpr int o_=adr(s), wi_=o_>=98304?2:o_>=65536?1:0, r_=o_-(wi_==2?98304:wi_==1?65536:0); fa[(s)%3][h]=tr2(wb[wi_][h]+r_,wb[wi_][h]+r_+512); }while(0)
    CST(24); RDA(0,0); RDA(0,1); RDA(1,0); RDA(1,1); SB();
    sfor<0,56>([&](auto S){ constexpr int s=S.v,T=s<28?0:2,n=s<28?s:s-28;
      c4[T]=mfma(fa[s%3][0],hf_[n],c4[T]); PINW(c4[T]); SB();
      if constexpr(s==54){ WAITV(4); asm volatile("s_barrier":::"memory"); }
      RDA(s+2,0);
      if constexpr(s>=28) GS(2*(s-28)); SB();
      c4[T+1]=mfma(fa[s%3][1],hf_[n],c4[T+1]); PINW(c4[T+1]); SB();
      if constexpr(s==13) CST(25); if constexpr(s==27) CST(26); if constexpr(s==41) CST(28); if constexpr(s==55) CST(29);
      if constexpr(s==27||s==55) asm volatile("s_barrier":::"memory"); if constexpr(s==27) CST(27); if constexpr(s==55) CST(30);
      RDA(s+2,1);
      if constexpr(s>=28) GS(2*(s-28)+1);
      if constexpr(s>=27&&s<51&&(s&1)) W2D(2+((s-27)>>3),w*4+(((s-27)>>1)&3));
      SB(); });
    STAMP(16);
    const u32 vp=w*28672+lane*16, sp=j*114688;
#define PST(n,c) do{ __builtin_amdgcn_raw_buffer_store_b128(pk8(c,0),rp,vp+(n)*2048,sp,16); __builtin_amdgcn_raw_buffer_store_b128(pk8(c,8),rp,vp+(n)*2048+1024,sp,16); }while(0)
    sfor<56,112>([&](auto S){ constexpr int s=S.v,u=s-56, ks=u<32?(u>>3)*2+(u&1):(u&7), A=u<32?(u&6):((u>>3)&1)*2; constexpr bool first=u<32?ks==0:(u&7)==0;
      if constexpr(u==16) CST(31); if constexpr(u==24) CST(32); if constexpr(u==32) CST(33); if constexpr(u==40) CST(34); if constexpr(u==48) CST(35);
      if constexpr(u==16){ sfor<88,96>([&](auto Q){ GS(Q.v); }); G5(23); SB(); }
      if constexpr(u==24){ sfor<112,128>([&](auto Q){ GS(Q.v); }); G5(31); SB(); }
      if constexpr(first) c5[A]=mfma(fa[s%3][0],hb[ks],f32x16{}); else c5[A]=mfma(fa[s%3][0],hb[ks],c5[A]);
      PINW(c5[A]); SB();
      if constexpr(u==30){ WAITV(8); asm volatile("s_barrier":::"memory"); }
      if constexpr(s+2<112) RDA(s+2,0);
      if constexpr(u<16) GS(56+2*u); else if constexpr(u<24) GS(96+2*(u-16));
      if constexpr(u>=27&&u<=33&&(u&1)) PST(u-27,c5[u-27]);
      if constexpr(u==42||u==50) PST(8+((u-42)>>2),c5[((u-42)>>2)&2]);
      SB();
      if constexpr(first) c5[A+1]=mfma(fa[s%3][1],hb[ks],f32x16{}); else c5[A+1]=mfma(fa[s%3][1],hb[ks],c5[A+1]);
      PINW(c5[A+1]); SB();
      if constexpr(s+2<112) RDA(s+2,1);
      if constexpr(u<16) GS(57+2*u); else if constexpr(u<24) GS(97+2*(u-16));
      if constexpr(u<8) W2D(5+(u>>2),w*4+(u&3));
      if constexpr(u>=27&&u<=33&&(u&1)) PST(u-26,c5[u-26]);
      if constexpr(u==42||u==50) PST(9+((u-42)>>2),c5[(((u-42)>>2)&2)+1]);
      SB(); });
    CST(36); PST(12,c5[0]); PST(13,c5[1]); CST(37);
    STAMP(17);
    WAITV(0);
    if(lane==0) setflag(ws,3,i,j,w,tag);
    STAMP(19);
  }
  { const u32 U=w*14+(lane>>3), vb=lane<56?U*16:0x80000000u, vq=lane<56?(j>>2)*28672+U*512+((j&3)*8+(lane&7))*16:0x80000000u;
    const rsrc_t rb=mk(b2,896);
    const u32x4 q0=__builtin_amdgcn_raw_buffer_load_b128(rb,vb,0,0),q1=__builtin_amdgcn_raw_buffer_load_b128(rb,vb+112,0,0);
    if(w==0) poll(ws,3,i,lane<56&&(lane&3)==(j>>2),tag,lane);
    BAR();
    STAMP(20);
    u32x4 g[15][2];
#pragma unroll
    for(int n=0;n<14;n++){ const u32 k=n+j, so=(k>=28?k-28:k>=14?k-14:k)*114688; g[n][0]=__builtin_amdgcn_raw_buffer_load_b128(rp,vq,so,16); g[n][1]=__builtin_amdgcn_raw_buffer_load_b128(rp,vq+3584,so,16); }
    g[14][0]=__builtin_amdgcn_raw_buffer_load_b128(rh,vq,0,16); g[14][1]=__builtin_amdgcn_raw_buffer_load_b128(rh,vq+3584,0,16);
    f32x4 s[4]; s[0]=m44(id4,u32x2{q0[0],q0[1]},f32x4{}); s[1]=m44(id4,u32x2{q0[2],q0[3]},f32x4{}); s[2]=m44(id4,u32x2{q1[0],q1[1]},f32x4{}); s[3]=m44(id4,u32x2{q1[2],q1[3]},f32x4{});
#pragma unroll
    for(int n=0;n<15;n++){ s[0]=m44(id4,u32x2{g[n][0][0],g[n][0][1]},s[0]); s[1]=m44(id4,u32x2{g[n][0][2],g[n][0][3]},s[1]); s[2]=m44(id4,u32x2{g[n][1][0],g[n][1][1]},s[2]); s[3]=m44(id4,u32x2{g[n][1][2],g[n][1][3]},s[3]); }
    const rsrc_t rout=mk(out+(size_t)(i*128+j*8)*448,8*896); const u32 vo=lane<56?(lane&7)*896+U*16:0x80000000u;
    __builtin_amdgcn_raw_buffer_store_b128(u32x4{pk(s[0][0],s[0][1]),pk(s[0][2],s[0][3]),pk(s[1][0],s[1][1]),pk(s[1][2],s[1][3])},rout,vo,0,0);
    __builtin_amdgcn_raw_buffer_store_b128(u32x4{pk(s[2][0],s[2][1]),pk(s[2][2],s[2][3]),pk(s[3][0],s[3][1]),pk(s[3][2],s[3][3])},rout,vo+112,0,0); }
  { uint64_t t_; asm volatile("s_memrealtime %0\n\ts_waitcnt lgkmcnt(0)":"=s"(t_)::"memory"); if(lane==0) ((u32*)(ws+(1<<19)))[b*4+w]=(u32)t_; }
}
}
__global__ void emptyk(char* p){ uint64_t t0,t1; asm volatile("s_memrealtime %0\n\ts_waitcnt lgkmcnt(0)":"=s"(t0)::"memory");
  unsigned m=0xffffffffu; for(int q=threadIdx.x;q<1024;q+=64){ unsigned v=(unsigned)t0-((unsigned*)(p+(1<<19)))[q]; m=v<m?v:m; }
  for(int o=32;o;o>>=1){ unsigned v=__shfl_xor(m,o); m=v<m?v:m; }
  unsigned d=__builtin_amdgcn_readfirstlane(m)*PMUL; if(d>4000) d=4000;
  do{ asm volatile("s_memrealtime %0\n\ts_waitcnt lgkmcnt(0)":"=s"(t1)::"memory"); }while((unsigned)t1-(unsigned)t0<d); }
extern "C" void kernel_launch(void* const* d_in,const int* in_sizes,int n_in,void* d_out,int out_size,void* d_ws,size_t ws_size,hipStream_t stream){
  typedef const unsigned short* P;
  (void)hipFuncSetAttribute((const void*)fk::fused,hipFuncAttributeMaxDynamicSharedMemorySize,163840);
  fk::fused<<<256,256,163840,stream>>>((P)d_in[0],(P)d_in[1],(P)d_in[2],(P)d_in[3],(P)d_in[5],(P)d_in[6],(P)d_in[7],(P)d_in[8],(unsigned short*)d_out,(char*)d_ws);
  emptyk<<<1,64,0,stream>>>((char*)d_ws);
}
```

```cpp
#define NWAVE 4
#define PROBE 3
#define PA 0
#define PB 21
#define PMUL 0
#define PBLK 9999
#define RA 0
#define RB 1
#include <hip/hip_runtime.h>
#include <stdint.h>
namespace fk {
typedef unsigned short u16; typedef unsigned u32;
typedef __attribute__((ext_vector_type(4))) u32 u32x4;
typedef __attribute__((ext_vector_type(2))) u32 u32x2;
typedef __attribute__((ext_vector_type(8))) __bf16 bf16x8;
typedef __attribute__((ext_vector_type(2))) __bf16 bf16x2;
typedef __attribute__((ext_vector_type(2))) float f32x2;
typedef __attribute__((ext_vector_type(4))) float f32x4;
typedef __attribute__((ext_vector_type(16))) float f32x16;
typedef short v4i16_t __attribute__((ext_vector_type(4)));
typedef __attribute__((address_space(3))) char* lptr;
typedef __amdgpu_buffer_rsrc_t rsrc_t;
#define DI __device__ __forceinline__
#define LDS(T,a) (*(__attribute__((address_space(3))) T*)(a))
#define STAMP(n) do{ if((n)==PA||(n)==PB){ uint64_t t_; asm volatile("s_memrealtime %0\n\ts_waitcnt lgkmcnt(0)":"=s"(t_)::"memory"); if((n)==PA) pa_=(u32)t_; if((n)==PB) pb_=(u32)t_; } }while(0)
#define CST(n) do{ if((n)==46||(n)==47){ uint64_t t_; asm volatile("s_memtime %0\n\ts_waitcnt lgkmcnt(0)":"=s"(t_)::"memory"); if((n)==46) ca_=(u32)t_; else cb_=(u32)t_; } }while(0)
constexpr u32 WS_FLAG=4096, WS_KV=1u<<20, WS_O=2u<<20, WS_H=4u<<20, WS_HS=6u<<20, WS_P=8u<<20;
constexpr int L_WK=98304, L_WV=126976, L_MISC=155648, L_KVX=L_MISC+3072;
constexpr int L_WQ=0, L_PHI=32768, L_VI=49152, L_WO=65536, L_W1A=98304, L_W1B=0;
constexpr int xs(int c){ return (c%6==0?4:c%6==5?5:c%6-1)*16384; }
constexpr int w2c(int c){ return c<5?65536+c*16384:(c-5)*16384; }
constexpr float LOG2E=1.4426950408889634f;

template<int V> struct IC{ static constexpr int v=V; };
template<int I,int N,class F> DI void sfor(F&& f){ if constexpr(I<N){ f(IC<I>{}); sfor<I+1,N>(f); } }
constexpr int adr(int s){ return s<28?L_W1A+s*2048: s<56?L_W1B+(s-28)*2048: s<88?w2c(((s-56)>>1)&3)+(((s-56)>>3)*2+((s-56)&1))*2048: w2c(4+((s-88)>>3))+((s-88)&7)*2048; }
DI rsrc_t mk(const void* p,u32 bytes){ return __builtin_amdgcn_make_buffer_rsrc((void*)p,0,bytes,0x00020000); }
DI void dma(u32 l,u32 voff,rsrc_t r,u32 soff){ asm volatile("s_mov_b32 m0, %0\n\ts_nop 0\n\tbuffer_load_dwordx4 %1, %2, %3 offen lds"::"s"(l),"v"(voff),"s"(r),"s"(soff):"memory"); }
#define WAITV(n) asm volatile("s_waitcnt vmcnt(" #n ")":::"memory")
#define BAR() asm volatile("s_waitcnt lgkmcnt(0)\n\ts_barrier":::"memory")
#define PIN(x) asm volatile(""::"v"(x))
#define PINW(x) asm volatile("":"+v"(x))
#define SB() __builtin_amdgcn_sched_barrier(0)
DI f32x16 mfma(u32x4 a,u32x4 b,f32x16 c){ return __builtin_amdgcn_mfma_f32_32x32x16_bf16(__builtin_bit_cast(bf16x8,a),__builtin_bit_cast(bf16x8,b),c,0,0,0); }
DI f32x4 m44(u32x2 a,u32x2 b,f32x4 c){ return __builtin_amdgcn_mfma_f32_4x4x4bf16_1k(__builtin_bit_cast(v4i16_t,a),__builtin_bit_cast(v4i16_t,b),c,0,0,0); }
DI u32x2 tr(lptr p){ return __builtin_bit_cast(u32x2,__builtin_amdgcn_ds_read_tr16_b64_v4i16((__attribute__((address_space(3))) v4i16_t*)p)); }
DI u32x4 tr2(lptr p0,lptr p1){ u32x2 a=tr(p0),b=tr(p1); return u32x4{a[0],a[1],b[0],b[1]}; }
DI u32 pk(float lo,float hi){ return __builtin_bit_cast(u32,__builtin_convertvector(f32x2{lo,hi},bf16x2)); }
DI float dot2(u32 a,u32 b,float c){ return __builtin_amdgcn_fdot2_f32_bf16(__builtin_bit_cast(bf16x2,a),__builtin_bit_cast(bf16x2,b),c,false); }
DI float swp_lo(float v){ auto r=__builtin_amdgcn_permlane32_swap(__float_as_uint(v),__float_as_uint(v),false,false); return __uint_as_float(r[0]); }
DI float swp_hi(float v){ auto r=__builtin_amdgcn_permlane32_swap(__float_as_uint(v),__float_as_uint(v),false,false); return __uint_as_float(r[1]); }
DI float swp_add(float v){ auto r=__builtin_amdgcn_permlane32_swap(__float_as_uint(v),__float_as_uint(v),false,false); return __uint_as_float(r[0])+__uint_as_float(r[1]); }
DI float swp_max(float v){ auto r=__builtin_amdgcn_permlane32_swap(__float_as_uint(v),__float_as_uint(v),false,false); return __builtin_fmaxf(__uint_as_float(r[0]),__uint_as_float(r[1])); }
DI float swp_min(float v){ auto r=__builtin_amdgcn_permlane32_swap(__float_as_uint(v),__float_as_uint(v),false,false); return __builtin_fminf(__uint_as_float(r[0]),__uint_as_float(r[1])); }
DI u32x4 pk8(const f32x16& c,int b){ return u32x4{pk(c[b],c[b+1]),pk(c[b+2],c[b+3]),pk(c[b+4],c[b+5]),pk(c[b+6],c[b+7])}; }
DI void dsoftmax(const f32x16& p,float c,u32x4* ph){
  float mx=p[0],mn=p[0];
#pragma unroll
  for(int r=1;r<14;r++){ mx=__builtin_fmaxf(mx,p[r]); mn=__builtin_fminf(mn,p[r]); }
  mx=swp_max(mx); mn=swp_min(mn);
  const float a=-mx*c,b=mn*c; f32x16 e,g; float s=0,u=0;
#pragma unroll
  for(int r=0;r<14;r++){ e[r]=__builtin_amdgcn_exp2f(__builtin_fmaf(p[r],c,a)); g[r]=__builtin_amdgcn_exp2f(__builtin_fmaf(p[r],-c,b)); s+=e[r]; u+=g[r]; }
  s=__builtin_amdgcn_rcpf(swp_add(s)); u=__builtin_amdgcn_rcpf(swp_add(u));
#pragma unroll
  for(int r=0;r<14;r++){ e[r]*=s; g[r]*=u; }
  ph[0]=pk8(e,0); ph[1]=u32x4{pk(e[8],e[9]),pk(e[10],e[11]),pk(e[12],e[13]),0u};
  ph[2]=pk8(g,0); ph[3]=u32x4{pk(g[8],g[9]),pk(g[10],g[11]),pk(g[12],g[13]),0u};
}
DI void setflag(char* ws,int ph,int grp,int mem,int w,u32 tag){ __hip_atomic_store((u32*)(ws+WS_FLAG+((ph*16+grp)*16+mem)*128+w*4),tag,__ATOMIC_RELAXED,__HIP_MEMORY_SCOPE_AGENT); }
DI void poll(char* ws,int ph,int grp,bool on,u32 tag,int lane){
  u32* f=(u32*)(ws+WS_FLAG+((ph*16+grp)*16+(lane>>2))*128+(lane&3)*4);
  for(int it=0;it<40000;it++){ u32 v=tag; if(on) v=__hip_atomic_load(f,__ATOMIC_RELAXED,__HIP_MEMORY_SCOPE_AGENT); if(__all(v==tag)) break; __builtin_amdgcn_s_sleep(1); }
}

__global__ __launch_bounds__(256,2) void fused(const u16* __restrict__ x,const u16* __restrict__ wqkv,const u16* __restrict__ wphi,const u16* __restrict__ wo,
    const u16* __restrict__ w1,const u16* __restrict__ b1,const u16* __restrict__ w2,const u16* __restrict__ b2,u16* __restrict__ out,char* __restrict__ ws
    ){
  extern __shared__ __attribute__((aligned(16))) char smem[];
  const lptr L=(lptr)smem;
  const int tid=threadIdx.x,lane=tid&63,w=__builtin_amdgcn_readfirstlane(tid>>6),t=lane&31,hf=lane>>5;
  const int b=blockIdx.x,i=(b&7)*2+((b>>3)&1),j=b>>4,odd=j&1;
  u32 pa_=0,pb_=0,ca_=0,cb_=0;
  STAMP(0); CST(46);
  const u32 epoch=*(const __attribute__((address_space(4))) u32*)ws;
  const rsrc_t rw=mk(wqkv,448*2688), rx=mk(x+(size_t)(i*128+w*32)*448,32*896);
  const u32 vw=(lane>>2)*2688+(lane&3)*16, hoff=56*j-8*odd+w*16*2688;
  const u32 vx0=(lane>>3)*896+(((lane&7)^(lane>>4))<<4), vx1=vx0^64;
#define WKDMA(c) dma(L_WK+w*1024+(c)*4096,vw,rw,hoff+896+(c)*64*2688)
#define WVDMA(c) dma(L_WV+w*1024+(c)*4096,vw,rw,hoff+1792+(c)*64*2688)
#define XD(c,g) dma(xs(c)+w*4096+(g)*1024,((g)&1)?vx1:vx0,rx,(g)*7168+(c)*128)
#define QD(u) dma(L_WQ+((u)>>2)*16384+w*4096+((u)&3)*1024,vw,rw,hoff+(u)*64*2688)
#define XDMA(c) do{ XD(c,0); XD(c,1); XD(c,2); XD(c,3); }while(0)
  WKDMA(0); WVDMA(0); WKDMA(1); WVDMA(1); XDMA(0);
  dma(L_KVX+(w&1)*1024,(tid&127)*16,mk(wphi+j*784,1568),0);
  WKDMA(2); WVDMA(2); XDMA(1); WKDMA(3); WVDMA(3); XDMA(2);
  const int q4=(lane&15)>>2,mu=((lane>>4)&1)*4+(lane&3),sg=4*(mu&1)+(mu>>1);
  const int rowo=(8*hf+q4)*64;
  const lptr aK=L+L_WK+rowo+((mu+odd)&7)*8;
  const lptr aV=L+L_WV+rowo+(odd?(sg==7?0:((sg+1)%7)+1):sg)*8;
  const lptr aQ=L+L_WQ+rowo+((mu+odd)&7)*8;
  const lptr oK=aK+w*1024,oV=aV+w*1024,pp=L+L_WK+w*1024+(lane&15)*64+(odd?0:56);
  const lptr xb=L+(w*32+t)*128; const int sw=hf^((t>>1)&7);
  u32 tag=epoch*0x9E3779B1u+0x7F4A7C15u; asm volatile("":"+s"(tag));
  STAMP(12);
  float ck=0,cv=0;
const u32x2 pv={0x3F80u,0u};
#define PATCH(c) asm volatile("s_mov_b64 exec, 0xffff\n\tds_write_b64 %0, %1 offset:%2\n\ts_mov_b64 exec, -1"::"v"(pp),"v"(pv),"i"((c)*4096):"memory")
#define OWNRD(c) ok=tr2(oK+(c)*4096,oK+(c)*4096+256); ov=tr2(oV+(c)*4096,oV+(c)*4096+256)
#define OWNSUM() do{ _Pragma("unroll") for(int e=0;e<4;e++){ ck=dot2(ok[e],0x3F803F80u,ck); cv=dot2(ov[e],0x3F803F80u,cv); } PINW(ck); PINW(cv); }while(0)
  u32x4 ok,ov;
  WAITV(19); PATCH(0); OWNRD(0); OWNSUM();
  STAMP(1);
  BAR();
  STAMP(2);
  u32x4 xf[28],fk[4],fv[4],wp[2]; f32x16 cK={},cV={},cG={};
#define XRD(ks) xf[ks]=LDS(u32x4,xb+xs((ks)>>2)+((sw^(((ks)&3)*2))<<4))
#define ARD(ks) fk[(ks)&3]=tr2(aK+(ks)*1024,aK+(ks)*1024+256); fv[(ks)&3]=tr2(aV+(ks)*1024,aV+(ks)*1024+256)
#define MG(ks) cG=mfma(xf[ks],xf[ks],cG); PINW(cG)
#define MK(ks) cK=mfma(fk[(ks)&3],xf[ks],cK); PINW(cK)
#define MV(ks) cV=mfma(fv[(ks)&3],xf[ks],cV); PINW(cV)
#define STAGE(c,N,D0,D1,D2,D3,D4,D5,EXTRA) do{ \
    ARD(4*(c)); ARD(4*(c)+1); SB(); WAITV(N); XRD(4*(c)); XRD(4*(c)+1); XRD(4*(c)+2); XRD(4*(c)+3); SB(); \
    MG(4*(c)); SB(); ARD(4*(c)+2); SB(); MK(4*(c)); SB(); if((c)<6){ PATCH((c)+1); OWNRD((c)+1); } SB(); MV(4*(c)); SB(); \
    MG(4*(c)+1); SB(); ARD(4*(c)+3); SB(); MK(4*(c)+1); SB(); D0; SB(); MV(4*(c)+1); SB(); D1; SB(); \
    MG(4*(c)+2); SB(); if((c)<6) OWNSUM(); SB(); MK(4*(c)+2); SB(); D2; SB(); MV(4*(c)+2); SB(); D3; SB(); \
    MG(4*(c)+3); SB(); D4; SB(); MK(4*(c)+3); SB(); D5; EXTRA; SB(); MV(4*(c)+3); SB(); if((c)<6) BAR(); }while(0)
#define NOP_ do{}while(0)
  STAGE(0,12,WKDMA(4),WVDMA(4),XD(3,0),XD(3,1),XD(3,2),XD(3,3),NOP_);
  { const bool mv=(t&26)!=26; const lptr p=L+L_KVX+(t<26?t:t-2)*2+hf*224; u32 e[16];
#pragma unroll
    for(int q=0;q<16;q++){ const int d=16*(q>>3)+8*((q>>2)&1)+(q&3); e[q]=LDS(u16,p+d*56); }
#pragma unroll
    for(int q=0;q<8;q++){ u32 v=e[2*q]|(e[2*q+1]<<16); v=(mv&&(q<6||hf==0))?v:0u; asm volatile("":"+v"(v)); wp[q>>2][q&3]=v; } }
  STAGE(1,12,WKDMA(5),WVDMA(5),XD(4,0),XD(4,1),XD(4,2),XD(4,3),NOP_);
  STAGE(2,12,WKDMA(6),WVDMA(6),XD(5,0),XD(5,1),XD(5,2),XD(5,3),NOP_);
  STAGE(3,12,XD(6,0),XD(6,1),XD(6,2),XD(6,3),NOP_,NOP_,NOP_);
  STAGE(4,10,QD(0),QD(1),QD(2),QD(3),NOP_,NOP_,NOP_);
  STAGE(5,8,QD(4),QD(5),QD(6),NOP_,NOP_,NOP_,{ LDS(float,L+L_MISC+lane*16+w*4)=ck; LDS(float,L+L_MISC+1024+lane*16+w*4)=cv; });
  STAGE(6,7,NOP_,NOP_,NOP_,NOP_,NOP_,NOP_,NOP_);
  STAMP(3);
  float rstd,nmean;
  { f32x4 a=LDS(f32x4,L+L_MISC+lane*16),c=LDS(f32x4,L+L_MISC+1024+lane*16);
    nmean=swp_hi(cK[12])*(-1.f/448);
    cK=__builtin_amdgcn_mfma_f32_32x32x2f32((a[0]+a[1])+(a[2]+a[3]),nmean,cK,0,0,0);
    cV=__builtin_amdgcn_mfma_f32_32x32x2f32((c[0]+c[1])+(c[2]+c[3]),nmean,cV,0,0,0);
    float g8[8],g4[4];
#pragma unroll
    for(int e=0;e<8;e++){ float u0=cG[2*e],u1=cG[2*e+1]; asm("":"+v"(u0)); g8[e]=(t&1)?u1:u0; }
#pragma unroll
    for(int e=0;e<4;e++){ float u0=g8[2*e]; asm("":"+v"(u0)); g4[e]=(t&2)?g8[2*e+1]:u0; }
    float g2a=(t&8)?g4[1]:g4[0],g2b=(t&8)?g4[3]:g4[2],g1=(t&16)?g2b:g2a;
    auto r=__builtin_amdgcn_permlane32_swap(__float_as_uint(g1),__float_as_uint(g1),false,false);
    float ss=__uint_as_float((t&4)?r[1]:r[0]);
    rstd=__builtin_amdgcn_rsqf(__builtin_fmaxf(__builtin_fmaf(ss,1.f/448,-nmean*nmean),0.f)+1e-5f); }
  const int wro=(w*4096+t*64),c0=((2*hf)^((t>>2)&3))<<4;
  { f32x16 p=mfma(wp[0],pk8(cK,0),f32x16{}); p=mfma(wp[1],pk8(cK,8),p);
#pragma unroll
    for(int r=0;r<16;r++) cV[r]*=rstd;
    u32x4 v0=pk8(cV,0),v1=pk8(cV,8); if(hf){ v1[2]=0x3F80u; v1[3]=0u; }
    LDS(u32x4,L+L_VI+wro+c0)=v0; LDS(u32x4,L+L_VI+wro+(c0^16))=v1;
    u32x4 ph[4]; dsoftmax(p,rstd*LOG2E,ph);
    LDS(u32x4,L+L_PHI+wro+c0)=ph[0]; LDS(u32x4,L+L_PHI+wro+(c0^16))=ph[1];
    LDS(u32x4,L+L_PHI+2048+wro+c0)=ph[2]; LDS(u32x4,L+L_PHI+2048+wro+(c0^16))=ph[3]; }
  WAITV(0);
  STAMP(4);
  BAR();
  const bool act=j<14; const u32 rot=2*j;
  auto kap=[&](int n){ const u32 k=n+rot; return k>=28?k-28:k; };
  const rsrc_t rkv=mk(ws+WS_KV+j*65536,65536), r1=mk(w1,448*3584);
  const u32 v8=(lane>>3), p8=((lane&7)^(lane>>2&4))<<4, vw1=v8*3584+p8;
#define W1D(base,half,u) dma((base)+(u)*1024,vw1,r1,(kap((u)>>1)*2+((u)&1))*8*3584+j*256+(half)*128)
  if(w<2){
    const int ko=(8*hf+q4)*64+(sg&1)*8; const lptr k0=L+ko+(((sg>>1)^(2*hf))<<4),k1=L+ko+256+(((sg>>1)^(2*hf+1))<<4);
    f32x16 c={};
#pragma unroll
    for(int u=0;u<8;u++){ const int o=(u>>1)*4096+(u&1)*1024; c=mfma(tr2(k0+L_PHI+w*2048+o,k1+L_PHI+w*2048+o),tr2(k0+L_VI+o,k1+L_VI+o),c); }
    __builtin_amdgcn_raw_buffer_store_b128(pk8(c,0),rkv,i*4096+w*2048+lane*16,0,16);
    __builtin_amdgcn_raw_buffer_store_b128(pk8(c,8),rkv,i*4096+w*2048+1024+lane*16,0,16);
  } else if(act){
    const rsrc_t r0=mk(wo,448*896); const u32 vo=(lane>>2)*896+(lane&3)*16;
#pragma unroll 1
    for(int u=(w-2)*14;u<(w-1)*14;u++) dma(L_WO+u*1024,vo,r0,kap(u)*16*896+j*64);
  }
  { f32x4 cq={};
#pragma unroll
    for(int u=0;u<7;u++){ const lptr p=aQ+(u>>2)*16384+w*4096+(u&3)*1024; cq=m44(u32x2{0x3F803F80u,0x3F803F80u},tr(p),cq); cq=m44(u32x2{0x3F803F80u,0x3F803F80u},tr(p+256),cq); }
    LDS(float,L+L_MISC+2048+lane*16+w*4)=cq[0]; }
  STAMP(5);
  BAR();
  u32x4 phq[4];
  { f32x16 cQ={};
#pragma unroll
    for(int ks=0;ks<28;ks++){ const lptr p=aQ+((ks>>2)>>2)*16384+(ks&3)*4096+((ks>>2)&3)*1024; cQ=mfma(tr2(p,p+256),xf[ks],cQ);
      if(w>=2){ if(act) W1D(L_W1A,0,(w-2)*28+ks); }
      else if(ks==12){ WAITV(0); if(lane==0) setflag(ws,0,j,i,w,tag); STAMP(6); } }
    f32x4 a=LDS(f32x4,L+L_MISC+2048+lane*16);
    cQ=__builtin_amdgcn_mfma_f32_32x32x2f32((a[0]+a[1])+(a[2]+a[3]),nmean,cQ,0,0,0);
    f32x16 p=mfma(wp[0],pk8(cQ,0),f32x16{}); p=mfma(wp[1],pk8(cQ,8),p);
    dsoftmax(p,rstd*LOG2E,phq); }
  STAMP(7);
  if(w==0) poll(ws,0,j,(lane&2)==0,tag,lane);
  BAR();
  STAMP(8);
  const u32x2 id4={(lane&3)==0?0x3F80u:(lane&3)==1?0x3F800000u:0u,(lane&3)==2?0x3F80u:(lane&3)==3?0x3F800000u:0u};
  u32x4 xr0,xr1;
  { u32x4 g[16];
#pragma unroll
    for(int n=0;n<16;n++) g[n]=__builtin_amdgcn_raw_buffer_load_b128(rkv,w*1024+lane*16,((n+i)&15)*4096,16);
    xr0=__builtin_amdgcn_raw_buffer_load_b128(rx,t*896+hf*16+j*64,0,0); xr1=__builtin_amdgcn_raw_buffer_load_b128(rx,t*896+hf*16+32+j*64,0,0);
    f32x4 s[4]={};
#pragma unroll
    for(int n=0;n<16;n++){ s[2*(n&1)]=m44(id4,u32x2{g[n][0],g[n][1]},s[2*(n&1)]); s[2*(n&1)+1]=m44(id4,u32x2{g[n][2],g[n][3]},s[2*(n&1)+1]); }
    s[0]+=s[2]; s[1]+=s[3];
    LDS(u32x4,L+L_KVX+w*1024+lane*16)=u32x4{pk(s[0][0],s[0][1]),pk(s[0][2],s[0][3]),pk(s[1][0],s[1][1]),pk(s[1][2],s[1][3])}; }
  BAR();
  STAMP(9);
  const rsrc_t ro=mk(ws+WS_O+i*114688,114688);
  { f32x16 c={};
#pragma unroll
    for(int n=0;n<4;n++) c=mfma(LDS(u32x4,L+L_KVX+n*1024+lane*16),phq[n],c);
    const float rd=__builtin_amdgcn_rcpf(swp_hi(c[12])+1e-6f);
#pragma unroll
    for(int r=0;r<16;r++) c[r]*=rd;
    const u32x4 o0=pk8(c,0),o1=pk8(c,8); const u32 ub=w*28672+t*16+((7*j)>>1)*512;
    __builtin_amdgcn_raw_buffer_store_b128(o0,ro,ub+(odd+2*hf)*512,0,16);
    if(hf==0) __builtin_amdgcn_raw_buffer_store_b128(o1,ro,ub+(odd+1)*512,0,16);
    else __builtin_amdgcn_raw_buffer_store_b64(u32x2{o1[0],o1[1]},ro,ub+(odd?8:1536),0,16);
    WAITV(0); }
  if(lane==0) setflag(ws,1,i,j,w,tag);
  STAMP(10);
  const rsrc_t rh=mk(ws+WS_H+i*114688,114688), rp=mk(ws+WS_P+i*(14*114688),14*114688);
  if(act){
    if(w==0) poll(ws,1,i,true,tag,lane);
    else {
#pragma unroll 1
      for(int u=w-1;u<56;u+=3) W1D(L_W1B,1,u);
    }
    const int nq=(mu&4)|((mu&1)<<1)|((mu>>1)&1);
    if(w==0){ const int tq=t>>2, f=((tq&4)|((tq&1)<<1)|((tq>>1)&1))*4+(t&3)+hf*32;
      LDS(u32,L+L_MISC+2048+lane*4)=(u32)b1[j*128+f]<<16; LDS(u32,L+L_MISC+2048+256+lane*4)=(u32)b1[j*128+64+f]<<16; }
    BAR();
    STAMP(11);
    if(b==0&&tid==0) __hip_atomic_store((u32*)ws,tag,__ATOMIC_RELAXED,__HIP_MEMORY_SCOPE_AGENT);
    const rsrc_t rs=mk(ws+WS_HS+i*14336,14336);
    { u32x4 of[28];
#pragma unroll
      for(int n=0;n<28;n++) of[n]=__builtin_amdgcn_raw_buffer_load_b128(ro,w*28672+lane*16,kap(n)*1024,16);
      f32x16 c;
#pragma unroll
      for(int e=0;e<4;e++){ c[2*e]=__uint_as_float(xr0[e]<<16); c[2*e+1]=__uint_as_float(xr0[e]&0xffff0000u); c[8+2*e]=__uint_as_float(xr1[e]<<16); c[9+2*e]=__uint_as_float(xr1[e]&0xffff0000u); }
      const lptr aO=L+L_WO+rowo+nq*8;
#pragma unroll
      for(int n=0;n<28;n++) c=mfma(tr2(aO+n*1024,aO+n*1024+256),of[n],c);
      __builtin_amdgcn_raw_buffer_store_b128(pk8(c,0),rh,w*28672+j*2048+lane*16,0,16);
      __builtin_amdgcn_raw_buffer_store_b128(pk8(c,8),rh,w*28672+j*2048+1024+lane*16,0,16);
      float s1=0,s2=0;
#pragma unroll
      for(int r=0;r<16;r++){ s1+=c[r]; s2=__builtin_fmaf(c[r],c[r],s2); }
      s1=swp_add(s1); s2=swp_add(s2);
      if(hf==0) __builtin_amdgcn_raw_buffer_store_b64(u32x2{__float_as_uint(s1),__float_as_uint(s2)},rs,w*3584+j*256+t*8,0,16);
      WAITV(0); }
    if(lane==0) setflag(ws,2,i,j,w,tag);
    STAMP(13);
    BAR();
    const rsrc_t r2=mk(w2+(size_t)j*128*448,128*896); const u32 vw2=v8*896+p8;
#define W2D(c,u) dma(w2c(c)+(u)*1024,vw2,r2,(u)*7168+(c)*128)
    const lptr a0=L+(8*hf+q4)*128+(q4>>1)*64+nq*8; const lptr a1=L+(((8*hf+q4)*128+(q4>>1)*64+nq*8)^64);
    {
      const lptr p=((w&1)?a1:a0)+((w&2)?L_W1B:L_W1A); f32x4 cs={};
      if(w){
#pragma unroll 1
        for(int u=w-1;u<32;u+=3) dma(65536+u*1024,vw2,r2,(u&15)*7168+(u>>4)*128);
      }
#pragma unroll
      for(int n=0;n<28;n++){ cs=m44(u32x2{0x3F803F80u,0x3F803F80u},tr(p+n*2048),cs); cs=m44(u32x2{0x3F803F80u,0x3F803F80u},tr(p+n*2048+512),cs); }
      const float v=swp_add(cs[0]); if(hf==0) LDS(float,L+L_MISC+1024+w*128+t*4)=v; }
    STAMP(14);
    if(w==0) poll(ws,2,i,lane<56,tag,lane);
    BAR();
    STAMP(15);
    u32x4 hf_[28],fa[3][2],hb[8]; f32x16 c4[4],c5[8]; float sg_,K1,K2,Lg;
    { u32x2 st[7];
#pragma unroll
      for(int n=0;n<7;n++) st[n]=__builtin_amdgcn_raw_buffer_load_b64(rs,w*3584+n*512+lane*8,0,16);
#pragma unroll
      for(int n=0;n<28;n++) hf_[n]=__builtin_amdgcn_raw_buffer_load_b128(rh,w*28672+lane*16,kap(n)*1024,16);
      float cb[4];
#pragma unroll
      for(int T=0;T<4;T++) cb[T]=LDS(float,L+L_MISC+1024+hf*1024+T*128+t*4);
      float s1=0,s2=0;
#pragma unroll
      for(int n=0;n<7;n++){ s1+=__uint_as_float(st[n][0]); s2+=__uint_as_float(st[n][1]); }
      s1=swp_add(s1)*(-1.f/448); s2=swp_add(s2);
      const float v=__builtin_fmaxf(__builtin_fmaf(s2,1.f/448,-s1*s1),0.f), vv=__builtin_fmaf(v,1.00001f,1e-10f), rs_=__builtin_amdgcn_rsqf(vv);
      sg_=vv*rs_; K1=-2.302208198f*rs_; K2=-0.1029432397f*rs_*rs_*rs_; Lg=0.5f*__builtin_amdgcn_logf(vv);
      const float bb=hf?sg_:s1;
#pragma unroll
      for(int T=0;T<4;T++) c4[T]=__builtin_amdgcn_mfma_f32_32x32x2f32(cb[T],bb,f32x16{},0,0,0); }
    float t0,t1,e0,e1,g0;
#define GA(p) c4[(p)>>3][2*((p)&7)]
#define GB(p) c4[(p)>>3][2*((p)&7)+1]
#define G5(p) do{ float g1=GB(p)*e1; PINW(g0); PINW(g1); hb[(p)>>2][(p)&3]=pk(g0,g1); }while(0)
#define GS(q) do{ constexpr int p_=(q)>>2; \
      if constexpr(((q)&3)==0){ if constexpr(p_>0&&p_!=24) G5(p_>0?p_-1:0); t0=GA(p_)*GA(p_); PINW(t0); t1=GB(p_)*GB(p_); t0=__builtin_fmaf(t0,K2,K1); PINW(t0); t1=__builtin_fmaf(t1,K2,K1); PINW(t1);} \
      else if constexpr(((q)&3)==1){ t0=__builtin_fmaf(GA(p_),t0,Lg); PINW(t0); t1=__builtin_fmaf(GB(p_),t1,Lg); e0=__builtin_amdgcn_exp2f(t0); PINW(e0); PINW(t1);} \
      else if constexpr(((q)&3)==2){ e1=__builtin_amdgcn_exp2f(t1); e0+=sg_; e0=__builtin_amdgcn_rcpf(e0); PINW(e0); PINW(e1);} \
      else { e1+=sg_; e1=__builtin_amdgcn_rcpf(e1); g0=GA(p_)*e0; PINW(e1); PINW(g0);} }while(0)
lptr wb[3][2]={{a0,a1},{a0+65536,a1+65536},{a0+98304,a1+98304}};
#pragma unroll
    for(int q=1;q<3;q++){ PINW(wb[q][0]); PINW(wb[q][1]); }
#define RDA(s,h) do{ constexpr int o_=adr(s), wi_=o_>=98304?2:o_>=65536?1:0, r_=o_-(wi_==2?98304:wi_==1?65536:0); fa[(s)%3][h]=tr2(wb[wi_][h]+r_,wb[wi_][h]+r_+512); }while(0)
    CST(24); RDA(0,0); RDA(0,1); RDA(1,0); RDA(1,1); SB();
    sfor<0,56>([&](auto S){ constexpr int s=S.v,T=s<28?0:2,n=s<28?s:s-28;
      c4[T]=mfma(fa[s%3][0],hf_[n],c4[T]); PINW(c4[T]); SB();
      if constexpr(s==54){ WAITV(4); asm volatile("s_barrier":::"memory"); }
      RDA(s+2,0);
      if constexpr(s>=28) GS(2*(s-28)); SB();
      c4[T+1]=mfma(fa[s%3][1],hf_[n],c4[T+1]); PINW(c4[T+1]); SB();
      if constexpr(s==13) CST(25); if constexpr(s==27) CST(26); if constexpr(s==41) CST(28); if constexpr(s==55) CST(29);
      if constexpr(s==27||s==55) asm volatile("s_barrier":::"memory"); if constexpr(s==27) CST(27); if constexpr(s==55) CST(30);
      RDA(s+2,1);
      if constexpr(s>=28) GS(2*(s-28)+1);
      if constexpr(s>=27&&s<51&&(s&1)) W2D(2+((s-27)>>3),w*4+(((s-27)>>1)&3));
      SB(); });
    STAMP(16);
    const u32 vp=w*28672+lane*16, sp=j*114688;
#define PST(n,c) do{ __builtin_amdgcn_raw_buffer_store_b128(pk8(c,0),rp,vp+(n)*2048,sp,16); __builtin_amdgcn_raw_buffer_store_b128(pk8(c,8),rp,vp+(n)*2048+1024,sp,16); }while(0)
    sfor<56,112>([&](auto S){ constexpr int s=S.v,u=s-56, ks=u<32?(u>>3)*2+(u&1):(u&7), A=u<32?(u&6):((u>>3)&1)*2; constexpr bool first=u<32?ks==0:(u&7)==0;
      if constexpr(u==16) CST(31); if constexpr(u==24) CST(32); if constexpr(u==32) CST(33); if constexpr(u==40) CST(34); if constexpr(u==48) CST(35);
      if constexpr(u==16){ sfor<88,96>([&](auto Q){ GS(Q.v); }); G5(23); SB(); }
      if constexpr(u==24){ sfor<112,128>([&](auto Q){ GS(Q.v); }); G5(31); SB(); }
      if constexpr(first) c5[A]=mfma(fa[s%3][0],hb[ks],f32x16{}); else c5[A]=mfma(fa[s%3][0],hb[ks],c5[A]);
      PINW(c5[A]); SB();
      if constexpr(u==30){ WAITV(8); asm volatile("s_barrier":::"memory"); }
      if constexpr(s+2<112) RDA(s+2,0);
      if constexpr(u<16) GS(56+2*u); else if constexpr(u<24) GS(96+2*(u-16));
      if constexpr(u>=27&&u<=33&&(u&1)) PST(u-27,c5[u-27]);
      if constexpr(u==42||u==50) PST(8+((u-42)>>2),c5[((u-42)>>2)&2]);
      SB();
      if constexpr(first) c5[A+1]=mfma(fa[s%3][1],hb[ks],f32x16{}); else c5[A+1]=mfma(fa[s%3][1],hb[ks],c5[A+1]);
      PINW(c5[A+1]); SB();
      if constexpr(s+2<112) RDA(s+2,1);
      if constexpr(u<16) GS(57+2*u); else if constexpr(u<24) GS(97+2*(u-16));
      if constexpr(u<8) W2D(5+(u>>2),w*4+(u&3));
      if constexpr(u>=27&&u<=33&&(u&1)) PST(u-26,c5[u-26]);
      if constexpr(u==42||u==50) PST(9+((u-42)>>2),c5[(((u-42)>>2)&2)+1]);
      SB(); });
    CST(36); PST(12,c5[0]); PST(13,c5[1]); CST(37);
    STAMP(17);
    WAITV(0);
    if(lane==0) setflag(ws,3,i,j,w,tag);
    STAMP(19);
  }
  { const u32 U=w*14+(lane>>3), vb=lane<56?U*16:0x80000000u, vq=lane<56?(j>>2)*28672+U*512+((j&3)*8+(lane&7))*16:0x80000000u;
    const rsrc_t rb=mk(b2,896);
    const u32x4 q0=__builtin_amdgcn_raw_buffer_load_b128(rb,vb,0,0),q1=__builtin_amdgcn_raw_buffer_load_b128(rb,vb+112,0,0);
    if(w==0) poll(ws,3,i,lane<56&&(lane&3)==(j>>2),tag,lane);
    BAR();
    STAMP(20);
    u32x4 g[15][2];
#pragma unroll
    for(int n=0;n<14;n++){ const u32 k=n+j, so=(k>=28?k-28:k>=14?k-14:k)*114688; g[n][0]=__builtin_amdgcn_raw_buffer_load_b128(rp,vq,so,16); g[n][1]=__builtin_amdgcn_raw_buffer_load_b128(rp,vq+3584,so,16); }
    g[14][0]=__builtin_amdgcn_raw_buffer_load_b128(rh,vq,0,16); g[14][1]=__builtin_amdgcn_raw_buffer_load_b128(rh,vq+3584,0,16);
    f32x4 s[4]; s[0]=m44(id4,u32x2{q0[0],q0[1]},f32x4{}); s[1]=m44(id4,u32x2{q0[2],q0[3]},f32x4{}); s[2]=m44(id4,u32x2{q1[0],q1[1]},f32x4{}); s[3]=m44(id4,u32x2{q1[2],q1[3]},f32x4{});
#pragma unroll
    for(int n=0;n<15;n++){ s[0]=m44(id4,u32x2{g[n][0][0],g[n][0][1]},s[0]); s[1]=m44(id4,u32x2{g[n][0][2],g[n][0][3]},s[1]); s[2]=m44(id4,u32x2{g[n][1][0],g[n][1][1]},s[2]); s[3]=m44(id4,u32x2{g[n][1][2],g[n][1][3]},s[3]); }
    const rsrc_t rout=mk(out+(size_t)(i*128+j*8)*448,8*896); const u32 vo=lane<56?(lane&7)*896+U*16:0x80000000u;
    __builtin_amdgcn_raw_buffer_store_b128(u32x4{pk(s[0][0],s[0][1]),pk(s[0][2],s[0][3]),pk(s[1][0],s[1][1]),pk(s[1][2],s[1][3])},rout,vo,0,0);
    __builtin_amdgcn_raw_buffer_store_b128(u32x4{pk(s[2][0],s[2][1]),pk(s[2][2],s[2][3]),pk(s[3][0],s[3][1]),pk(s[3][2],s[3][3])},rout,vo+112,0,0); }
  STAMP(21); CST(47);
  if(lane==0){ ((u32*)(ws+(1<<19)))[(b*NWAVE+w)*2]=pa_; ((u32*)(ws+(1<<19)))[(b*NWAVE+w)*2+1]=pb_; }
  if(b==PBLK){ u32 d=PROBE==2?(cb_-ca_)/10:(pb_-pa_)*PMUL; if(d>40000) d=40000; uint64_t t0_,t1_; asm volatile("s_memrealtime %0\n\ts_waitcnt lgkmcnt(0)":"=s"(t0_)::"memory");
    do{ asm volatile("s_memrealtime %0\n\ts_waitcnt lgkmcnt(0)":"=s"(t1_)::"memory"); }while((u32)t1_-(u32)t0_<d); }
}
}
__global__ void emptyk(char* p){ uint64_t t0,t1; asm volatile("s_memrealtime %0\n\ts_waitcnt lgkmcnt(0)":"=s"(t0)::"memory");
  int r[2];
  for(int k=0;k<2;k++){ int mn=0x7fffffff,mx=-1,sm=0,c=0; for(int q=threadIdx.x;q<1024;q+=64){ unsigned s=((unsigned*)(p+(1<<19)))[q*2+k]; if(s){ int v=(int)((unsigned)t0-s); mn=v<mn?v:mn; mx=v>mx?v:mx; sm+=v; c++; } }
    for(int o=32;o;o>>=1){ int v=__shfl_xor(mn,o); mn=v<mn?v:mn; v=__shfl_xor(mx,o); mx=v>mx?v:mx; sm+=__shfl_xor(sm,o); c+=__shfl_xor(c,o); }
    int R=k?RB:RA; r[k]=R==0?mx:R==1?mn:sm/(c?c:1); }
  unsigned d=__builtin_amdgcn_readfirstlane(r[0]-r[1])*PMUL; if(d>6000) d=6000;
  asm volatile("s_memrealtime %0\n\ts_waitcnt lgkmcnt(0)":"=s"(t0)::"memory");
  do{ asm volatile("s_memrealtime %0\n\ts_waitcnt lgkmcnt(0)":"=s"(t1)::"memory"); }while((unsigned)t1-(unsigned)t0<d); }
extern "C" void kernel_launch(void* const* d_in,const int* in_sizes,int n_in,void* d_out,int out_size,void* d_ws,size_t ws_size,hipStream_t stream){
  typedef const unsigned short* P;
  (void)hipFuncSetAttribute((const void*)fk::fused,hipFuncAttributeMaxDynamicSharedMemorySize,163840);
  fk::fused<<<256,256,163840,stream>>>((P)d_in[0],(P)d_in[1],(P)d_in[2],(P)d_in[3],(P)d_in[5],(P)d_in[6],(P)d_in[7],(P)d_in[8],(unsigned short*)d_out,(char*)d_ws);
  emptyk<<<1,64,0,stream>>>((char*)d_ws);
}
```

```cpp
#define NWAVE 4
#define PROBE 3
#define PA 0
#define PB 6
#define PMUL 1
#define PBLK 9999
#define RA 0
#define RB 2
#include <hip/hip_runtime.h>
#include <stdint.h>
namespace fk {
typedef unsigned short u16; typedef unsigned u32;
typedef __attribute__((ext_vector_type(4))) u32 u32x4;
typedef __attribute__((ext_vector_type(2))) u32 u32x2;
typedef __attribute__((ext_vector_type(8))) __bf16 bf16x8;
typedef __attribute__((ext_vector_type(2))) __bf16 bf16x2;
typedef __attribute__((ext_vector_type(2))) float f32x2;
typedef __attribute__((ext_vector_type(4))) float f32x4;
typedef __attribute__((ext_vector_type(16))) float f32x16;
typedef short v4i16_t __attribute__((ext_vector_type(4)));
typedef __attribute__((address_space(3))) char* lptr;
typedef __amdgpu_buffer_rsrc_t rsrc_t;
#define DI __device__ __forceinline__
#define LDS(T,a) (*(__attribute__((address_space(3))) T*)(a))
#define STAMP(n) do{ if((n)==PA||(n)==PB){ uint64_t t_; asm volatile("s_memrealtime %0\n\ts_waitcnt lgkmcnt(0)":"=s"(t_)::"memory"); if((n)==PA) pa_=(u32)t_; if((n)==PB) pb_=(u32)t_; } }while(0)
#define CST(n) do{ if((n)==46||(n)==47){ uint64_t t_; asm volatile("s_memtime %0\n\ts_waitcnt lgkmcnt(0)":"=s"(t_)::"memory"); if((n)==46) ca_=(u32)t_; else cb_=(u32)t_; } }while(0)
constexpr u32 WS_FLAG=4096, WS_KV=1u<<20, WS_O=2u<<20, WS_H=4u<<20, WS_HS=6u<<20, WS_P=8u<<20;
constexpr int L_WK=98304, L_WV=126976, L_MISC=155648, L_KVX=L_MISC+3072;
constexpr int L_WQ=0, L_PHI=32768, L_VI=49152, L_WO=65536, L_W1A=98304, L_W1B=0;
constexpr int xs(int c){ return (c%6==0?4:c%6==5?5:c%6-1)*16384; }
constexpr int w2c(int c){ return c<5?65536+c*16384:(c-5)*16384; }
constexpr float LOG2E=1.4426950408889634f;

template<int V> struct IC{ static constexpr int v=V; };
template<int I,int N,class F> DI void sfor(F&& f){ if constexpr(I<N){ f(IC<I>{}); sfor<I+1,N>(f); } }
constexpr int adr(int s){ return s<28?L_W1A+s*2048: s<56?L_W1B+(s-28)*2048: s<88?w2c(((s-56)>>1)&3)+(((s-56)>>3)*2+((s-56)&1))*2048: w2c(4+((s-88)>>3))+((s-88)&7)*2048; }
DI rsrc_t mk(const void* p,u32 bytes){ return __builtin_amdgcn_make_buffer_rsrc((void*)p,0,bytes,0x00020000); }
DI void dma(u32 l,u32 voff,rsrc_t r,u32 soff){ asm volatile("s_mov_b32 m0, %0\n\ts_nop 0\n\tbuffer_load_dwordx4 %1, %2, %3 offen lds"::"s"(l),"v"(voff),"s"(r),"s"(soff):"memory"); }
#define WAITV(n) asm volatile("s_waitcnt vmcnt(" #n ")":::"memory")
#define BAR() asm volatile("s_waitcnt lgkmcnt(0)\n\ts_barrier":::"memory")
#define PIN(x) asm volatile(""::"v"(x))
#define PINW(x) asm volatile("":"+v"(x))
#define SB() __builtin_amdgcn_sched_barrier(0)
DI f32x16 mfma(u32x4 a,u32x4 b,f32x16 c){ return __builtin_amdgcn_mfma_f32_32x32x16_bf16(__builtin_bit_cast(bf16x8,a),__builtin_bit_cast(bf16x8,b),c,0,0,0); }
DI f32x4 m44(u32x2 a,u32x2 b,f32x4 c){ return __builtin_amdgcn_mfma_f32_4x4x4bf16_1k(__builtin_bit_cast(v4i16_t,a),__builtin_bit_cast(v4i16_t,b),c,0,0,0); }
DI u32x2 tr(lptr p){ return __builtin_bit_cast(u32x2,__builtin_amdgcn_ds_read_tr16_b64_v4i16((__attribute__((address_space(3))) v4i16_t*)p)); }
DI u32x4 tr2(lptr p0,lptr p1){ u32x2 a=tr(p0),b=tr(p1); return u32x4{a[0],a[1],b[0],b[1]}; }
DI u32 pk(float lo,float hi){ return __builtin_bit_cast(u32,__builtin_convertvector(f32x2{lo,hi},bf16x2)); }
DI float dot2(u32 a,u32 b,float c){ return __builtin_amdgcn_fdot2_f32_bf16(__builtin_bit_cast(bf16x2,a),__builtin_bit_cast(bf16x2,b),c,false); }
DI float swp_lo(float v){ auto r=__builtin_amdgcn_permlane32_swap(__float_as_uint(v),__float_as_uint(v),false,false); return __uint_as_float(r[0]); }
DI float swp_hi(float v){ auto r=__builtin_amdgcn_permlane32_swap(__float_as_uint(v),__float_as_uint(v),false,false); return __uint_as_float(r[1]); }
DI float swp_add(float v){ auto r=__builtin_amdgcn_permlane32_swap(__float_as_uint(v),__float_as_uint(v),false,false); return __uint_as_float(r[0])+__uint_as_float(r[1]); }
DI float swp_max(float v){ auto r=__builtin_amdgcn_permlane32_swap(__float_as_uint(v),__float_as_uint(v),false,false); return __builtin_fmaxf(__uint_as_float(r[0]),__uint_as_float(r[1])); }
DI float swp_min(float v){ auto r=__builtin_amdgcn_permlane32_swap(__float_as_uint(v),__float_as_uint(v),false,false); return __builtin_fminf(__uint_as_float(r[0]),__uint_as_float(r[1])); }
DI u32x4 pk8(const f32x16& c,int b){ return u32x4{pk(c[b],c[b+1]),pk(c[b+2],c[b+3]),pk(c[b+4],c[b+5]),pk(c[b+6],c[b+7])}; }
DI void dsoftmax(const f32x16& p,float c,u32x4* ph){
  float mx=p[0],mn=p[0];
#pragma unroll
  for(int r=1;r<14;r++){ mx=__builtin_fmaxf(mx,p[r]); mn=__builtin_fminf(mn,p[r]); }
  mx=swp_max(mx); mn=swp_min(mn);
  const float a=-mx*c,b=mn*c; f32x16 e,g; float s=0,u=0;
#pragma unroll
  for(int r=0;r<14;r++){ e[r]=__builtin_amdgcn_exp2f(__builtin_fmaf(p[r],c,a)); g[r]=__builtin_amdgcn_exp2f(__builtin_fmaf(p[r],-c,b)); s+=e[r]; u+=g[r]; }
  s=__builtin_amdgcn_rcpf(swp_add(s)); u=__builtin_amdgcn_rcpf(swp_add(u));
#pragma unroll
  for(int r=0;r<14;r++){ e[r]*=s; g[r]*=u; }
  ph[0]=pk8(e,0); ph[1]=u32x4{pk(e[8],e[9]),pk(e[10],e[11]),pk(e[12],e[13]),0u};
  ph[2]=pk8(g,0); ph[3]=u32x4{pk(g[8],g[9]),pk(g[10],g[11]),pk(g[12],g[13]),0u};
}
DI void setflag(char* ws,int ph,int grp,int mem,int w,u32 tag){ __hip_atomic_store((u32*)(ws+WS_FLAG+((ph*16+grp)*16+mem)*128+w*4),tag,__ATOMIC_RELAXED,__HIP_MEMORY_SCOPE_AGENT); }
DI void poll(char* ws,int ph,int grp,bool on,u32 tag,int lane){
  u32* f=(u32*)(ws+WS_FLAG+((ph*16+grp)*16+(lane>>2))*128+(lane&3)*4);
  for(int it=0;it<40000;it++){ u32 v=tag; if(on) v=__hip_atomic_load(f,__ATOMIC_RELAXED,__HIP_MEMORY_SCOPE_AGENT); if(__all(v==tag)) break; __builtin_amdgcn_s_sleep(1); }
}

__global__ __launch_bounds__(256,2) void fused(const u16* __restrict__ x,const u16* __restrict__ wqkv,const u16* __restrict__ wphi,const u16* __restrict__ wo,
    const u16* __restrict__ w1,const u16* __restrict__ b1,const u16* __restrict__ w2,const u16* __restrict__ b2,u16* __restrict__ out,char* __restrict__ ws
    ){
  extern __shared__ __attribute__((aligned(16))) char smem[];
  const lptr L=(lptr)smem;
  const int tid=threadIdx.x,lane=tid&63,w=__builtin_amdgcn_readfirstlane(tid>>6),t=lane&31,hf=lane>>5;
  const int b=blockIdx.x,i=(b&7)*2+((b>>3)&1),j=b>>4,odd=j&1;
  u32 pa_=0,pb_=0,ca_=0,cb_=0;
  STAMP(0); CST(46);
  const u32 epoch=*(const __attribute__((address_space(4))) u32*)ws;
  const rsrc_t rw=mk(wqkv,448*2688), rx=mk(x+(size_t)(i*128+w*32)*448,32*896);
  const u32 vw=(lane>>2)*2688+(lane&3)*16, hoff=56*j-8*odd+w*16*2688;
  const u32 vx0=(lane>>3)*896+(((lane&7)^(lane>>4))<<4), vx1=vx0^64;
#define WKDMA(c) dma(L_WK+w*1024+(c)*4096,vw,rw,hoff+896+(c)*64*2688)
#define WVDMA(c) dma(L_WV+w*1024+(c)*4096,vw,rw,hoff+1792+(c)*64*2688)
#define XD(c,g) dma(xs(c)+w*4096+(g)*1024,((g)&1)?vx1:vx0,rx,(g)*7168+(c)*128)
#define QD(u) dma(L_WQ+((u)>>2)*16384+w*4096+((u)&3)*1024,vw,rw,hoff+(u)*64*2688)
#define XDMA(c) do{ XD(c,0); XD(c,1); XD(c,2); XD(c,3); }while(0)
  WKDMA(0); WVDMA(0); WKDMA(1); WVDMA(1); XDMA(0);
  dma(L_KVX+(w&1)*1024,(tid&127)*16,mk(wphi+j*784,1568),0);
  WKDMA(2); WVDMA(2); XDMA(1); WKDMA(3); WVDMA(3); XDMA(2);
  const int q4=(lane&15)>>2,mu=((lane>>4)&1)*4+(lane&3),sg=4*(mu&1)+(mu>>1);
  const int rowo=(8*hf+q4)*64;
  const lptr aK=L+L_WK+rowo+((mu+odd)&7)*8;
  const lptr aV=L+L_WV+rowo+(odd?(sg==7?0:((sg+1)%7)+1):sg)*8;
  const lptr aQ=L+L_WQ+rowo+((mu+odd)&7)*8;
  const lptr oK=aK+w*1024,oV=aV+w*1024,pp=L+L_WK+w*1024+(lane&15)*64+(odd?0:56);
  const lptr xb=L+(w*32+t)*128; const int sw=hf^((t>>1)&7);
  u32 tag=epoch*0x9E3779B1u+0x7F4A7C15u; asm volatile("":"+s"(tag));
  STAMP(12);
  float ck=0,cv=0;
const u32x2 pv={0x3F80u,0u};
#define PATCH(c) asm volatile("s_mov_b64 exec, 0xffff\n\tds_write_b64 %0, %1 offset:%2\n\ts_mov_b64 exec, -1"::"v"(pp),"v"(pv),"i"((c)*4096):"memory")
#define OWNRD(c) ok=tr2(oK+(c)*4096,oK+(c)*4096+256); ov=tr2(oV+(c)*4096,oV+(c)*4096+256)
#define OWNSUM() do{ _Pragma("unroll") for(int e=0;e<4;e++){ ck=dot2(ok[e],0x3F803F80u,ck); cv=dot2(ov[e],0x3F803F80u,cv); } PINW(ck); PINW(cv); }while(0)
  u32x4 ok,ov;
  WAITV(19); PATCH(0); OWNRD(0); OWNSUM();
  STAMP(1);
  BAR();
  STAMP(2);
  u32x4 xf[28],fk[4],fv[4],wp[2]; f32x16 cK={},cV={},cG={};
#define XRD(ks) xf[ks]=LDS(u32x4,xb+xs((ks)>>2)+((sw^(((ks)&3)*2))<<4))
#define ARD(ks) fk[(ks)&3]=tr2(aK+(ks)*1024,aK+(ks)*1024+256); fv[(ks)&3]=tr2(aV+(ks)*1024,aV+(ks)*1024+256)
#define MG(ks) cG=mfma(xf[ks],xf[ks],cG); PINW(cG)
#define MK(ks) cK=mfma(fk[(ks)&3],xf[ks],cK); PINW(cK)
#define MV(ks) cV=mfma(fv[(ks)&3],xf[ks],cV); PINW(cV)
#define STAGE(c,N,D0,D1,D2,D3,D4,D5,EXTRA) do{ \
    ARD(4*(c)); ARD(4*(c)+1); SB(); WAITV(N); XRD(4*(c)); XRD(4*(c)+1); XRD(4*(c)+2); XRD(4*(c)+3); SB(); \
    MG(4*(c)); SB(); ARD(4*(c)+2); SB(); MK(4*(c)); SB(); if((c)<6){ PATCH((c)+1); OWNRD((c)+1); } SB(); MV(4*(c)); SB(); \
    MG(4*(c)+1); SB(); ARD(4*(c)+3); SB(); MK(4*(c)+1); SB(); D0; SB(); MV(4*(c)+1); SB(); D1; SB(); \
    MG(4*(c)+2); SB(); if((c)<6) OWNSUM(); SB(); MK(4*(c)+2); SB(); D2; SB(); MV(4*(c)+2); SB(); D3; SB(); \
    MG(4*(c)+3); SB(); D4; SB(); MK(4*(c)+3); SB(); D5; EXTRA; SB(); MV(4*(c)+3); SB(); if((c)<6) BAR(); }while(0)
#define NOP_ do{}while(0)
  STAGE(0,12,WKDMA(4),WVDMA(4),XD(3,0),XD(3,1),XD(3,2),XD(3,3),NOP_);
  { const bool mv=(t&26)!=26; const lptr p=L+L_KVX+(t<26?t:t-2)*2+hf*224; u32 e[16];
#pragma unroll
    for(int q=0;q<16;q++){ const int d=16*(q>>3)+8*((q>>2)&1)+(q&3); e[q]=LDS(u16,p+d*56); }
#pragma unroll
    for(int q=0;q<8;q++){ u32 v=e[2*q]|(e[2*q+1]<<16); v=(mv&&(q<6||hf==0))?v:0u; asm volatile("":"+v"(v)); wp[q>>2][q&3]=v; } }
  STAGE(1,12,WKDMA(5),WVDMA(5),XD(4,0),XD(4,1),XD(4,2),XD(4,3),NOP_);
  STAGE(2,12,WKDMA(6),WVDMA(6),XD(5,0),XD(5,1),XD(5,2),XD(5,3),NOP_);
  STAGE(3,12,XD(6,0),XD(6,1),XD(6,2),XD(6,3),NOP_,NOP_,NOP_);
  STAGE(4,10,QD(0),QD(1),QD(2),QD(3),NOP_,NOP_,NOP_);
  STAGE(5,8,QD(4),QD(5),QD(6),NOP_,NOP_,NOP_,{ LDS(float,L+L_MISC+lane*16+w*4)=ck; LDS(float,L+L_MISC+1024+lane*16+w*4)=cv; });
  STAGE(6,7,NOP_,NOP_,NOP_,NOP_,NOP_,NOP_,NOP_);
  STAMP(3);
  float rstd,nmean;
  { f32x4 a=LDS(f32x4,L+L_MISC+lane*16),c=LDS(f32x4,L+L_MISC+1024+lane*16);
    nmean=swp_hi(cK[12])*(-1.f/448);
    cK=__builtin_amdgcn_mfma_f32_32x32x2f32((a[0]+a[1])+(a[2]+a[3]),nmean,cK,0,0,0);
    cV=__builtin_amdgcn_mfma_f32_32x32x2f32((c[0]+c[1])+(c[2]+c[3]),nmean,cV,0,0,0);
    float g8[8],g4[4];
#pragma unroll
    for(int e=0;e<8;e++){ float u0=cG[2*e],u1=cG[2*e+1]; asm("":"+v"(u0)); g8[e]=(t&1)?u1:u0; }
#pragma unroll
    for(int e=0;e<4;e++){ float u0=g8[2*e]; asm("":"+v"(u0)); g4[e]=(t&2)?g8[2*e+1]:u0; }
    float g2a=(t&8)?g4[1]:g4[0],g2b=(t&8)?g4[3]:g4[2],g1=(t&16)?g2b:g2a;
    auto r=__builtin_amdgcn_permlane32_swap(__float_as_uint(g1),__float_as_uint(g1),false,false);
    float ss=__uint_as_float((t&4)?r[1]:r[0]);
    rstd=__builtin_amdgcn_rsqf(__builtin_fmaxf(__builtin_fmaf(ss,1.f/448,-nmean*nmean),0.f)+1e-5f); }
  const int wro=(w*4096+t*64),c0=((2*hf)^((t>>2)&3))<<4;
  { f32x16 p=mfma(wp[0],pk8(cK,0),f32x16{}); p=mfma(wp[1],pk8(cK,8),p);
#pragma unroll
    for(int r=0;r<16;r++) cV[r]*=rstd;
    u32x4 v0=pk8(cV,0),v1=pk8(cV,8); if(hf){ v1[2]=0x3F80u; v1[3]=0u; }
    LDS(u32x4,L+L_VI+wro+c0)=v0; LDS(u32x4,L+L_VI+wro+(c0^16))=v1;
    u32x4 ph[4]; dsoftmax(p,rstd*LOG2E,ph);
    LDS(u32x4,L+L_PHI+wro+c0)=ph[0]; LDS(u32x4,L+L_PHI+wro+(c0^16))=ph[1];
    LDS(u32x4,L+L_PHI+2048+wro+c0)=ph[2]; LDS(u32x4,L+L_PHI+2048+wro+(c0^16))=ph[3]; }
  WAITV(0);
  STAMP(4);
  BAR();
  const bool act=j<14; const u32 rot=2*j;
  auto kap=[&](int n){ const u32 k=n+rot; return k>=28?k-28:k; };
  const rsrc_t rkv=mk(ws+WS_KV+j*65536,65536), r1=mk(w1,448*3584);
  const u32 v8=(lane>>3), p8=((lane&7)^(lane>>2&4))<<4, vw1=v8*3584+p8;
#define W1D(base,half,u) dma((base)+(u)*1024,vw1,r1,(kap((u)>>1)*2+((u)&1))*8*3584+j*256+(half)*128)
  if(w<2){
    const int ko=(8*hf+q4)*64+(sg&1)*8; const lptr k0=L+ko+(((sg>>1)^(2*hf))<<4),k1=L+ko+256+(((sg>>1)^(2*hf+1))<<4);
    f32x16 c={};
#pragma unroll
    for(int u=0;u<8;u++){ const int o=(u>>1)*4096+(u&1)*1024; c=mfma(tr2(k0+L_PHI+w*2048+o,k1+L_PHI+w*2048+o),tr2(k0+L_VI+o,k1+L_VI+o),c); }
    __builtin_amdgcn_raw_buffer_store_b128(pk8(c,0),rkv,i*4096+w*2048+lane*16,0,16);
    __builtin_amdgcn_raw_buffer_store_b128(pk8(c,8),rkv,i*4096+w*2048+1024+lane*16,0,16);
  } else if(act){
    const rsrc_t r0=mk(wo,448*896); const u32 vo=(lane>>2)*896+(lane&3)*16;
#pragma unroll 1
    for(int u=(w-2)*14;u<(w-1)*14;u++) dma(L_WO+u*1024,vo,r0,kap(u)*16*896+j*64);
  }
  { f32x4 cq={};
#pragma unroll
    for(int u=0;u<7;u++){ const lptr p=aQ+(u>>2)*16384+w*4096+(u&3)*1024; cq=m44(u32x2{0x3F803F80u,0x3F803F80u},tr(p),cq); cq=m44(u32x2{0x3F803F80u,0x3F803F80u},tr(p+256),cq); }
    LDS(float,L+L_MISC+2048+lane*16+w*4)=cq[0]; }
  STAMP(5);
  BAR();
  u32x4 phq[4];
  { f32x16 cQ={};
#pragma unroll
    for(int ks=0;ks<28;ks++){ const lptr p=aQ+((ks>>2)>>2)*16384+(ks&3)*4096+((ks>>2)&3)*1024; cQ=mfma(tr2(p,p+256),xf[ks],cQ);
      if(w>=2){ if(act) W1D(L_W1A,0,(w-2)*28+ks); }
      else if(ks==12){ WAITV(0); if(lane==0) setflag(ws,0,j,i,w,tag); STAMP(6); } }
    f32x4 a=LDS(f32x4,L+L_MISC+2048+lane*16);
    cQ=__builtin_amdgcn_mfma_f32_32x32x2f32((a[0]+a[1])+(a[2]+a[3]),nmean,cQ,0,0,0);
    f32x16 p=mfma(wp[0],pk8(cQ,0),f32x16{}); p=mfma(wp[1],pk8(cQ,8),p);
    dsoftmax(p,rstd*LOG2E,phq); }
  STAMP(7);
  if(w==0) poll(ws,0,j,(lane&2)==0,tag,lane);
  BAR();
  STAMP(8);
  const u32x2 id4={(lane&3)==0?0x3F80u:(lane&3)==1?0x3F800000u:0u,(lane&3)==2?0x3F80u:(lane&3)==3?0x3F800000u:0u};
  u32x4 xr0,xr1;
  { u32x4 g[16];
#pragma unroll
    for(int n=0;n<16;n++) g[n]=__builtin_amdgcn_raw_buffer_load_b128(rkv,w*1024+lane*16,((n+i)&15)*4096,16);
    xr0=__builtin_amdgcn_raw_buffer_load_b128(rx,t*896+hf*16+j*64,0,0); xr1=__builtin_amdgcn_raw_buffer_load_b128(rx,t*896+hf*16+32+j*64,0,0);
    f32x4 s[4]={};
#pragma unroll
    for(int n=0;n<16;n++){ s[2*(n&1)]=m44(id4,u32x2{g[n][0],g[n][1]},s[2*(n&1)]); s[2*(n&1)+1]=m44(id4,u32x2{g[n][2],g[n][3]},s[2*(n&1)+1]); }
    s[0]+=s[2]; s[1]+=s[3];
    LDS(u32x4,L+L_KVX+w*1024+lane*16)=u32x4{pk(s[0][0],s[0][1]),pk(s[0][2],s[0][3]),pk(s[1][0],s[1][1]),pk(s[1][2],s[1][3])}; }
  BAR();
  STAMP(9);
  const rsrc_t ro=mk(ws+WS_O+i*114688,114688);
  { f32x16 c={};
#pragma unroll
    for(int n=0;n<4;n++) c=mfma(LDS(u32x4,L+L_KVX+n*1024+lane*16),phq[n],c);
    const float rd=__builtin_amdgcn_rcpf(swp_hi(c[12])+1e-6f);
#pragma unroll
    for(int r=0;r<16;r++) c[r]*=rd;
    const u32x4 o0=pk8(c,0),o1=pk8(c,8); const u32 ub=w*28672+t*16+((7*j)>>1)*512;
    __builtin_amdgcn_raw_buffer_store_b128(o0,ro,ub+(odd+2*hf)*512,0,16);
    if(hf==0) __builtin_amdgcn_raw_buffer_store_b128(o1,ro,ub+(odd+1)*512,0,16);
    else __builtin_amdgcn_raw_buffer_store_b64(u32x2{o1[0],o1[1]},ro,ub+(odd?8:1536),0,16);
    WAITV(0); }
  if(lane==0) setflag(ws,1,i,j,w,tag);
  STAMP(10);
  const rsrc_t rh=mk(ws+WS_H+i*114688,114688), rp=mk(ws+WS_P+i*(14*114688),14*114688);
  if(act){
    if(w==0) poll(ws,1,i,true,tag,lane);
    else {
#pragma unroll 1
      for(int u=w-1;u<56;u+=3) W1D(L_W1B,1,u);
    }
    const int nq=(mu&4)|((mu&1)<<1)|((mu>>1)&1);
    if(w==0){ const int tq=t>>2, f=((tq&4)|((tq&1)<<1)|((tq>>1)&1))*4+(t&3)+hf*32;
      LDS(u32,L+L_MISC+2048+lane*4)=(u32)b1[j*128+f]<<16; LDS(u32,L+L_MISC+2048+256+lane*4)=(u32)b1[j*128+64+f]<<16; }
    BAR();
    STAMP(11);
    if(b==0&&tid==0) __hip_atomic_store((u32*)ws,tag,__ATOMIC_RELAXED,__HIP_MEMORY_SCOPE_AGENT);
    const rsrc_t rs=mk(ws+WS_HS+i*14336,14336);
    { u32x4 of[28];
#pragma unroll
      for(int n=0;n<28;n++) of[n]=__builtin_amdgcn_raw_buffer_load_b128(ro,w*28672+lane*16,kap(n)*1024,16);
      f32x16 c;
#pragma unroll
      for(int e=0;e<4;e++){ c[2*e]=__uint_as_float(xr0[e]<<16); c[2*e+1]=__uint_as_float(xr0[e]&0xffff0000u); c[8+2*e]=__uint_as_float(xr1[e]<<16); c[9+2*e]=__uint_as_float(xr1[e]&0xffff0000u); }
      const lptr aO=L+L_WO+rowo+nq*8;
#pragma unroll
      for(int n=0;n<28;n++) c=mfma(tr2(aO+n*1024,aO+n*1024+256),of[n],c);
      __builtin_amdgcn_raw_buffer_store_b128(pk8(c,0),rh,w*28672+j*2048+lane*16,0,16);
      __builtin_amdgcn_raw_buffer_store_b128(pk8(c,8),rh,w*28672+j*2048+1024+lane*16,0,16);
      float s1=0,s2=0;
#pragma unroll
      for(int r=0;r<16;r++){ s1+=c[r]; s2=__builtin_fmaf(c[r],c[r],s2); }
      s1=swp_add(s1); s2=swp_add(s2);
      if(hf==0) __builtin_amdgcn_raw_buffer_store_b64(u32x2{__float_as_uint(s1),__float_as_uint(s2)},rs,w*3584+j*256+t*8,0,16);
      WAITV(0); }
    if(lane==0) setflag(ws,2,i,j,w,tag);
    STAMP(13);
    BAR();
    const rsrc_t r2=mk(w2+(size_t)j*128*448,128*896); const u32 vw2=v8*896+p8;
#define W2D(c,u) dma(w2c(c)+(u)*1024,vw2,r2,(u)*7168+(c)*128)
    const lptr a0=L+(8*hf+q4)*128+(q4>>1)*64+nq*8; const lptr a1=L+(((8*hf+q4)*128+(q4>>1)*64+nq*8)^64);
    {
      const lptr p=((w&1)?a1:a0)+((w&2)?L_W1B:L_W1A); f32x4 cs={};
      if(w){
#pragma unroll 1
        for(int u=w-1;u<32;u+=3) dma(65536+u*1024,vw2,r2,(u&15)*7168+(u>>4)*128);
      }
#pragma unroll
      for(int n=0;n<28;n++){ cs=m44(u32x2{0x3F803F80u,0x3F803F80u},tr(p+n*2048),cs); cs=m44(u32x2{0x3F803F80u,0x3F803F80u},tr(p+n*2048+512),cs); }
      const float v=swp_add(cs[0]); if(hf==0) LDS(float,L+L_MISC+1024+w*128+t*4)=v; }
    STAMP(14);
    if(w==0) poll(ws,2,i,lane<56,tag,lane);
    BAR();
    STAMP(15);
    u32x4 hf_[28],fa[3][2],hb[8]; f32x16 c4[4],c5[8]; float sg_,K1,K2,Lg;
    { u32x2 st[7];
#pragma unroll
      for(int n=0;n<7;n++) st[n]=__builtin_amdgcn_raw_buffer_load_b64(rs,w*3584+n*512+lane*8,0,16);
#pragma unroll
      for(int n=0;n<28;n++) hf_[n]=__builtin_amdgcn_raw_buffer_load_b128(rh,w*28672+lane*16,kap(n)*1024,16);
      float cb[4];
#pragma unroll
      for(int T=0;T<4;T++) cb[T]=LDS(float,L+L_MISC+1024+hf*1024+T*128+t*4);
      float s1=0,s2=0;
#pragma unroll
      for(int n=0;n<7;n++){ s1+=__uint_as_float(st[n][0]); s2+=__uint_as_float(st[n][1]); }
      s1=swp_add(s1)*(-1.f/448); s2=swp_add(s2);
      const float v=__builtin_fmaxf(__builtin_fmaf(s2,1.f/448,-s1*s1),0.f), vv=__builtin_fmaf(v,1.00001f,1e-10f), rs_=__builtin_amdgcn_rsqf(vv);
      sg_=vv*rs_; K1=-2.302208198f*rs_; K2=-0.1029432397f*rs_*rs_*rs_; Lg=0.5f*__builtin_amdgcn_logf(vv);
      const float bb=hf?sg_:s1;
#pragma unroll
      for(int T=0;T<4;T++) c4[T]=__builtin_amdgcn_mfma_f32_32x32x2f32(cb[T],bb,f32x16{},0,0,0); }
    float t0,t1,e0,e1,g0;
#define GA(p) c4[(p)>>3][2*((p)&7)]
#define GB(p) c4[(p)>>3][2*((p)&7)+1]
#define G5(p) do{ float g1=GB(p)*e1; PINW(g0); PINW(g1); hb[(p)>>2][(p)&3]=pk(g0,g1); }while(0)
#define GS(q) do{ constexpr int p_=(q)>>2; \
      if constexpr(((q)&3)==0){ if constexpr(p_>0&&p_!=24) G5(p_>0?p_-1:0); t0=GA(p_)*GA(p_); PINW(t0); t1=GB(p_)*GB(p_); t0=__builtin_fmaf(t0,K2,K1); PINW(t0); t1=__builtin_fmaf(t1,K2,K1); PINW(t1);} \
      else if constexpr(((q)&3)==1){ t0=__builtin_fmaf(GA(p_),t0,Lg); PINW(t0); t1=__builtin_fmaf(GB(p_),t1,Lg); e0=__builtin_amdgcn_exp2f(t0); PINW(e0); PINW(t1);} \
      else if constexpr(((q)&3)==2){ e1=__builtin_amdgcn_exp2f(t1); e0+=sg_; e0=__builtin_amdgcn_rcpf(e0); PINW(e0); PINW(e1);} \
      else { e1+=sg_; e1=__builtin_amdgcn_rcpf(e1); g0=GA(p_)*e0; PINW(e1); PINW(g0);} }while(0)
lptr wb[3][2]={{a0,a1},{a0+65536,a1+65536},{a0+98304,a1+98304}};
#pragma unroll
    for(int q=1;q<3;q++){ PINW(wb[q][0]); PINW(wb[q][1]); }
#define RDA(s,h) do{ constexpr int o_=adr(s), wi_=o_>=98304?2:o_>=65536?1:0, r_=o_-(wi_==2?98304:wi_==1?65536:0); fa[(s)%3][h]=tr2(wb[wi_][h]+r_,wb[wi_][h]+r_+512); }while(0)
    CST(24); RDA(0,0); RDA(0,1); RDA(1,0); RDA(1,1); SB();
    sfor<0,56>([&](auto S){ constexpr int s=S.v,T=s<28?0:2,n=s<28?s:s-28;
      c4[T]=mfma(fa[s%3][0],hf_[n],c4[T]); PINW(c4[T]); SB();
      if constexpr(s==54){ WAITV(4); asm volatile("s_barrier":::"memory"); }
      RDA(s+2,0);
      if constexpr(s>=28) GS(2*(s-28)); SB();
      c4[T+1]=mfma(fa[s%3][1],hf_[n],c4[T+1]); PINW(c4[T+1]); SB();
      if constexpr(s==13) CST(25); if constexpr(s==27) CST(26); if constexpr(s==41) CST(28); if constexpr(s==55) CST(29);
      if constexpr(s==27||s==55) asm volatile("s_barrier":::"memory"); if constexpr(s==27) CST(27); if constexpr(s==55) CST(30);
      RDA(s+2,1);
      if constexpr(s>=28) GS(2*(s-28)+1);
      if constexpr(s>=27&&s<51&&(s&1)) W2D(2+((s-27)>>3),w*4+(((s-27)>>1)&3));
      SB(); });
    STAMP(16);
    const u32 vp=w*28672+lane*16, sp=j*114688;
#define PST(n,c) do{ __builtin_amdgcn_raw_buffer_store_b128(pk8(c,0),rp,vp+(n)*2048,sp,16); __builtin_amdgcn_raw_buffer_store_b128(pk8(c,8),rp,vp+(n)*2048+1024,sp,16); }while(0)
    sfor<56,112>([&](auto S){ constexpr int s=S.v,u=s-56, ks=u<32?(u>>3)*2+(u&1):(u&7), A=u<32?(u&6):((u>>3)&1)*2; constexpr bool first=u<32?ks==0:(u&7)==0;
      if constexpr(u==16) CST(31); if constexpr(u==24) CST(32); if constexpr(u==32) CST(33); if constexpr(u==40) CST(34); if constexpr(u==48) CST(35);
      if constexpr(u==16){ sfor<88,96>([&](auto Q){ GS(Q.v); }); G5(23); SB(); }
      if constexpr(u==24){ sfor<112,128>([&](auto Q){ GS(Q.v); }); G5(31); SB(); }
      if constexpr(first) c5[A]=mfma(fa[s%3][0],hb[ks],f32x16{}); else c5[A]=mfma(fa[s%3][0],hb[ks],c5[A]);
      PINW(c5[A]); SB();
      if constexpr(u==30){ WAITV(8); asm volatile("s_barrier":::"memory"); }
      if constexpr(s+2<112) RDA(s+2,0);
      if constexpr(u<16) GS(56+2*u); else if constexpr(u<24) GS(96+2*(u-16));
      if constexpr(u>=27&&u<=33&&(u&1)) PST(u-27,c5[u-27]);
      if constexpr(u==42||u==50) PST(8+((u-42)>>2),c5[((u-42)>>2)&2]);
      SB();
      if constexpr(first) c5[A+1]=mfma(fa[s%3][1],hb[ks],f32x16{}); else c5[A+1]=mfma(fa[s%3][1],hb[ks],c5[A+1]);
      PINW(c5[A+1]); SB();
      if constexpr(s+2<112) RDA(s+2,1);
      if constexpr(u<16) GS(57+2*u); else if constexpr(u<24) GS(97+2*(u-16));
      if constexpr(u<8) W2D(5+(u>>2),w*4+(u&3));
      if constexpr(u>=27&&u<=33&&(u&1)) PST(u-26,c5[u-26]);
      if constexpr(u==42||u==50) PST(9+((u-42)>>2),c5[(((u-42)>>2)&2)+1]);
      SB(); });
    CST(36); PST(12,c5[0]); PST(13,c5[1]); CST(37);
    STAMP(17);
    WAITV(0);
    if(lane==0) setflag(ws,3,i,j,w,tag);
    STAMP(19);
  }
  { const u32 U=w*14+(lane>>3), vb=lane<56?U*16:0x80000000u, vq=lane<56?(j>>2)*28672+U*512+((j&3)*8+(lane&7))*16:0x80000000u;
    const rsrc_t rb=mk(b2,896);
    const u32x4 q0=__builtin_amdgcn_raw_buffer_load_b128(rb,vb,0,0),q1=__builtin_amdgcn_raw_buffer_load_b128(rb,vb+112,0,0);
    if(w==0) poll(ws,3,i,lane<56&&(lane&3)==(j>>2),tag,lane);
    BAR();
    STAMP(20);
    u32x4 g[15][2];
#pragma unroll
    for(int n=0;n<14;n++){ const u32 k=n+j, so=(k>=28?k-28:k>=14?k-14:k)*114688; g[n][0]=__builtin_amdgcn_raw_buffer_load_b128(rp,vq,so,16); g[n][1]=__builtin_amdgcn_raw_buffer_load_b128(rp,vq+3584,so,16); }
    g[14][0]=__builtin_amdgcn_raw_buffer_load_b128(rh,vq,0,16); g[14][1]=__builtin_amdgcn_raw_buffer_load_b128(rh,vq+3584,0,16);
    f32x4 s[4]; s[0]=m44(id4,u32x2{q0[0],q0[1]},f32x4{}); s[1]=m44(id4,u32x2{q0[2],q0[3]},f32x4{}); s[2]=m44(id4,u32x2{q1[0],q1[1]},f32x4{}); s[3]=m44(id4,u32x2{q1[2],q1[3]},f32x4{});
#pragma unroll
    for(int n=0;n<15;n++){ s[0]=m44(id4,u32x2{g[n][0][0],g[n][0][1]},s[0]); s[1]=m44(id4,u32x2{g[n][0][2],g[n][0][3]},s[1]); s[2]=m44(id4,u32x2{g[n][1][0],g[n][1][1]},s[2]); s[3]=m44(id4,u32x2{g[n][1][2],g[n][1][3]},s[3]); }
    const rsrc_t rout=mk(out+(size_t)(i*128+j*8)*448,8*896); const u32 vo=lane<56?(lane&7)*896+U*16:0x80000000u;
    __builtin_amdgcn_raw_buffer_store_b128(u32x4{pk(s[0][0],s[0][1]),pk(s[0][2],s[0][3]),pk(s[1][0],s[1][1]),pk(s[1][2],s[1][3])},rout,vo,0,0);
    __builtin_amdgcn_raw_buffer_store_b128(u32x4{pk(s[2][0],s[2][1]),pk(s[2][2],s[2][3]),pk(s[3][0],s[3][1]),pk(s[3][2],s[3][3])},rout,vo+112,0,0); }
  STAMP(21); CST(47);
  if(PA<0){ pb_=pa_+(cb_-ca_)/20; }
  if(lane==0){ ((u32*)(ws+(1<<19)))[(b*NWAVE+w)*2]=pa_; ((u32*)(ws+(1<<19)))[(b*NWAVE+w)*2+1]=pb_; }
  if(b==PBLK){ u32 d=PROBE==2?(cb_-ca_)/10:(pb_-pa_)*PMUL; if(d>40000) d=40000; uint64_t t0_,t1_; asm volatile("s_memrealtime %0\n\ts_waitcnt lgkmcnt(0)":"=s"(t0_)::"memory");
    do{ asm volatile("s_memrealtime %0\n\ts_waitcnt lgkmcnt(0)":"=s"(t1_)::"memory"); }while((u32)t1_-(u32)t0_<d); }
}
}
__global__ void emptyk(char* p){ uint64_t t0,t1; asm volatile("s_memrealtime %0\n\ts_waitcnt lgkmcnt(0)":"=s"(t0)::"memory");
  int r[2];
  for(int k=0;k<2;k++){ int mn=0x7fffffff,mx=-1,sm=0,c=0; for(int q=threadIdx.x;q<1024;q+=64){ unsigned s=((unsigned*)(p+(1<<19)))[q*2+k]; if(s){ int v=(int)((unsigned)t0-s); mn=v<mn?v:mn; mx=v>mx?v:mx; sm+=v; c++; } }
    for(int o=32;o;o>>=1){ int v=__shfl_xor(mn,o); mn=v<mn?v:mn; v=__shfl_xor(mx,o); mx=v>mx?v:mx; sm+=__shfl_xor(sm,o); c+=__shfl_xor(c,o); }
    int R=k?RB:RA; r[k]=R==0?mx:R==1?mn:sm/(c?c:1); }
  unsigned d=__builtin_amdgcn_readfirstlane(r[0]-r[1])*PMUL; if(d>6000) d=6000;
  asm volatile("s_memrealtime %0\n\ts_waitcnt lgkmcnt(0)":"=s"(t0)::"memory");
  do{ asm volatile("s_memrealtime %0\n\ts_waitcnt lgkmcnt(0)":"=s"(t1)::"memory"); }while((unsigned)t1-(unsigned)t0<d); }
extern "C" void kernel_launch(void* const* d_in,const int* in_sizes,int n_in,void* d_out,int out_size,void* d_ws,size_t ws_size,hipStream_t stream){
  typedef const unsigned short* P;
  (void)hipFuncSetAttribute((const void*)fk::fused,hipFuncAttributeMaxDynamicSharedMemorySize,163840);
  fk::fused<<<256,256,163840,stream>>>((P)d_in[0],(P)d_in[1],(P)d_in[2],(P)d_in[3],(P)d_in[5],(P)d_in[6],(P)d_in[7],(P)d_in[8],(unsigned short*)d_out,(char*)d_ws);
  emptyk<<<1,64,0,stream>>>((char*)d_ws);
}
```

```cpp
#define NWAVE 4
#define PROBE 3
#define PA 0
#define PB 21
#define PMUL 0
#define PBLK 9999
#define RA 0
#define RB 1
#include <hip/hip_runtime.h>
#include <stdint.h>
namespace fk {
typedef unsigned short u16; typedef unsigned u32;
typedef __attribute__((ext_vector_type(4))) u32 u32x4;
typedef __attribute__((ext_vector_type(2))) u32 u32x2;
typedef __attribute__((ext_vector_type(8))) __bf16 bf16x8;
typedef __attribute__((ext_vector_type(2))) __bf16 bf16x2;
typedef __attribute__((ext_vector_type(2))) float f32x2;
typedef __attribute__((ext_vector_type(4))) float f32x4;
typedef __attribute__((ext_vector_type(16))) float f32x16;
typedef short v4i16_t __attribute__((ext_vector_type(4)));
typedef __attribute__((address_space(3))) char* lptr;
typedef __amdgpu_buffer_rsrc_t rsrc_t;
#define DI __device__ __forceinline__
#define LDS(T,a) (*(__attribute__((address_space(3))) T*)(a))
#define STAMP(n) do{ if((n)==PA||(n)==PB){ uint64_t t_; asm volatile("s_memrealtime %0\n\ts_waitcnt lgkmcnt(0)":"=s"(t_)::"memory"); if((n)==PA) pa_=(u32)t_; if((n)==PB) pb_=(u32)t_; } }while(0)
#define CST(n) do{ if((n)==46||(n)==47){ uint64_t t_; asm volatile("s_memtime %0\n\ts_waitcnt lgkmcnt(0)":"=s"(t_)::"memory"); if((n)==46) ca_=(u32)t_; else cb_=(u32)t_; } }while(0)
constexpr u32 WS_FLAG=4096, WS_KV=1u<<20, WS_O=2u<<20, WS_H=4u<<20, WS_HS=6u<<20, WS_P=8u<<20;
constexpr int L_WK=98304, L_WV=126976, L_MISC=155648, L_KVX=L_MISC+3072;
constexpr int L_WQ=0, L_PHI=32768, L_VI=49152, L_WO=65536, L_W1A=98304, L_W1B=0;
constexpr int xs(int c){ return (c%6==0?4:c%6==5?5:c%6-1)*16384; }
constexpr int w2c(int c){ return c<5?65536+c*16384:(c-5)*16384; }
constexpr float LOG2E=1.4426950408889634f;

template<int V> struct IC{ static constexpr int v=V; };
template<int I,int N,class F> DI void sfor(F&& f){ if constexpr(I<N){ f(IC<I>{}); sfor<I+1,N>(f); } }
constexpr int adr(int s){ return s<28?L_W1A+s*2048: s<56?L_W1B+(s-28)*2048: s<88?w2c(((s-56)>>1)&3)+(((s-56)>>3)*2+((s-56)&1))*2048: w2c(4+((s-88)>>3))+((s-88)&7)*2048; }
DI rsrc_t mk(const void* p,u32 bytes){ return __builtin_amdgcn_make_buffer_rsrc((void*)p,0,bytes,0x00020000); }
DI void dma(u32 l,u32 voff,rsrc_t r,u32 soff){ asm volatile("s_mov_b32 m0, %0\n\ts_nop 0\n\tbuffer_load_dwordx4 %1, %2, %3 offen lds"::"s"(l),"v"(voff),"s"(r),"s"(soff):"memory"); }
#define WAITV(n) asm volatile("s_waitcnt vmcnt(" #n ")":::"memory")
#define BAR() asm volatile("s_waitcnt lgkmcnt(0)\n\ts_barrier":::"memory")
#define PIN(x) asm volatile(""::"v"(x))
#define PINW(x) asm volatile("":"+v"(x))
#define SB() __builtin_amdgcn_sched_barrier(0)
DI f32x16 mfma(u32x4 a,u32x4 b,f32x16 c){ return __builtin_amdgcn_mfma_f32_32x32x16_bf16(__builtin_bit_cast(bf16x8,a),__builtin_bit_cast(bf16x8,b),c,0,0,0); }
DI f32x4 m44(u32x2 a,u32x2 b,f32x4 c){ return __builtin_amdgcn_mfma_f32_4x4x4bf16_1k(__builtin_bit_cast(v4i16_t,a),__builtin_bit_cast(v4i16_t,b),c,0,0,0); }
DI u32x2 tr(lptr p){ return __builtin_bit_cast(u32x2,__builtin_amdgcn_ds_read_tr16_b64_v4i16((__attribute__((address_space(3))) v4i16_t*)p)); }
DI u32x4 tr2(lptr p0,lptr p1){ u32x2 a=tr(p0),b=tr(p1); return u32x4{a[0],a[1],b[0],b[1]}; }
DI u32 pk(float lo,float hi){ return __builtin_bit_cast(u32,__builtin_convertvector(f32x2{lo,hi},bf16x2)); }
DI float dot2(u32 a,u32 b,float c){ return __builtin_amdgcn_fdot2_f32_bf16(__builtin_bit_cast(bf16x2,a),__builtin_bit_cast(bf16x2,b),c,false); }
DI float swp_lo(float v){ auto r=__builtin_amdgcn_permlane32_swap(__float_as_uint(v),__float_as_uint(v),false,false); return __uint_as_float(r[0]); }
DI float swp_hi(float v){ auto r=__builtin_amdgcn_permlane32_swap(__float_as_uint(v),__float_as_uint(v),false,false); return __uint_as_float(r[1]); }
DI float swp_add(float v){ auto r=__builtin_amdgcn_permlane32_swap(__float_as_uint(v),__float_as_uint(v),false,false); return __uint_as_float(r[0])+__uint_as_float(r[1]); }
DI float swp_max(float v){ auto r=__builtin_amdgcn_permlane32_swap(__float_as_uint(v),__float_as_uint(v),false,false); return __builtin_fmaxf(__uint_as_float(r[0]),__uint_as_float(r[1])); }
DI float swp_min(float v){ auto r=__builtin_amdgcn_permlane32_swap(__float_as_uint(v),__float_as_uint(v),false,false); return __builtin_fminf(__uint_as_float(r[0]),__uint_as_float(r[1])); }
DI u32x4 pk8(const f32x16& c,int b){ return u32x4{pk(c[b],c[b+1]),pk(c[b+2],c[b+3]),pk(c[b+4],c[b+5]),pk(c[b+6],c[b+7])}; }
DI void dsoftmax(const f32x16& p,float c,u32x4* ph){
  float mx=p[0],mn=p[0];
#pragma unroll
  for(int r=1;r<14;r++){ mx=__builtin_fmaxf(mx,p[r]); mn=__builtin_fminf(mn,p[r]); }
  mx=swp_max(mx); mn=swp_min(mn);
  const float a=-mx*c,b=mn*c; f32x16 e,g; float s=0,u=0;
#pragma unroll
  for(int r=0;r<14;r++){ e[r]=__builtin_amdgcn_exp2f(__builtin_fmaf(p[r],c,a)); g[r]=__builtin_amdgcn_exp2f(__builtin_fmaf(p[r],-c,b)); s+=e[r]; u+=g[r]; }
  s=__builtin_amdgcn_rcpf(swp_add(s)); u=__builtin_amdgcn_rcpf(swp_add(u));
#pragma unroll
  for(int r=0;r<14;r++){ e[r]*=s; g[r]*=u; }
  ph[0]=pk8(e,0); ph[1]=u32x4{pk(e[8],e[9]),pk(e[10],e[11]),pk(e[12],e[13]),0u};
  ph[2]=pk8(g,0); ph[3]=u32x4{pk(g[8],g[9]),pk(g[10],g[11]),pk(g[12],g[13]),0u};
}
DI void setflag(char* ws,int ph,int grp,int mem,int w,u32 tag){ __hip_atomic_store((u32*)(ws+WS_FLAG+((ph*16+grp)*16+mem)*128+w*4),tag,__ATOMIC_RELAXED,__HIP_MEMORY_SCOPE_AGENT); }
DI void poll(char* ws,int ph,int grp,bool on,u32 tag,int lane){
  u32* f=(u32*)(ws+WS_FLAG+((ph*16+grp)*16+(lane>>2))*128+(lane&3)*4);
  for(int it=0;it<40000;it++){ u32 v=tag; if(on) v=__hip_atomic_load(f,__ATOMIC_RELAXED,__HIP_MEMORY_SCOPE_AGENT); if(__all(v==tag)) break; __builtin_amdgcn_s_sleep(1); }
}

__global__ __launch_bounds__(256,2) void fused(const u16* __restrict__ x,const u16* __restrict__ wqkv,const u16* __restrict__ wphi,const u16* __restrict__ wo,
    const u16* __restrict__ w1,const u16* __restrict__ b1,const u16* __restrict__ w2,const u16* __restrict__ b2,u16* __restrict__ out,char* __restrict__ ws
    ){
  extern __shared__ __attribute__((aligned(16))) char smem[];
  const lptr L=(lptr)smem;
  const int tid=threadIdx.x,lane=tid&63,w=__builtin_amdgcn_readfirstlane(tid>>6),t=lane&31,hf=lane>>5;
  const int b=blockIdx.x,i=(b&7)*2+((b>>3)&1),j=b>>4,odd=j&1;
  u32 pa_=0,pb_=0,ca_=0,cb_=0;
  STAMP(0); CST(46);
  const u32 epoch=*(const __attribute__((address_space(4))) u32*)ws;
  const rsrc_t rw=mk(wqkv,448*2688), rx=mk(x+(size_t)(i*128+w*32)*448,32*896);
  const u32 vw=(lane>>2)*2688+(lane&3)*16, hoff=56*j-8*odd+w*16*2688;
  const u32 vx0=(lane>>3)*896+(((lane&7)^(lane>>4))<<4), vx1=vx0^64;
#define WKDMA(c) dma(L_WK+w*1024+(c)*4096,vw,rw,hoff+896+(c)*64*2688)
#define WVDMA(c) dma(L_WV+w*1024+(c)*4096,vw,rw,hoff+1792+(c)*64*2688)
#define XD(c,g) dma(xs(c)+w*4096+(g)*1024,((g)&1)?vx1:vx0,rx,(g)*7168+(c)*128)
#define QD(u) dma(L_WQ+((u)>>2)*16384+w*4096+((u)&3)*1024,vw,rw,hoff+(u)*64*2688)
#define XDMA(c) do{ XD(c,0); XD(c,1); XD(c,2); XD(c,3); }while(0)
  WKDMA(0); WVDMA(0); WKDMA(1); WVDMA(1); XDMA(0);
  dma(L_KVX+(w&1)*1024,(tid&127)*16,mk(wphi+j*784,1568),0);
  WKDMA(2); WVDMA(2); XDMA(1); WKDMA(3); WVDMA(3); XDMA(2);
  const int q4=(lane&15)>>2,mu=((lane>>4)&1)*4+(lane&3),sg=4*(mu&1)+(mu>>1);
  const int rowo=(8*hf+q4)*64;
  const lptr aK=L+L_WK+rowo+((mu+odd)&7)*8;
  const lptr aV=L+L_WV+rowo+(odd?(sg==7?0:((sg+1)%7)+1):sg)*8;
  const lptr aQ=L+L_WQ+rowo+((mu+odd)&7)*8;
  const lptr oK=aK+w*1024,oV=aV+w*1024,pp=L+L_WK+w*1024+(lane&15)*64+(odd?0:56);
  const lptr xb=L+(w*32+t)*128; const int sw=hf^((t>>1)&7);
  u32 tag=epoch*0x9E3779B1u+0x7F4A7C15u; asm volatile("":"+s"(tag));
  STAMP(12);
  float ck=0,cv=0;
const u32x2 pv={0x3F80u,0u};
#define PATCH(c) asm volatile("s_mov_b64 exec, 0xffff\n\tds_write_b64 %0, %1 offset:%2\n\ts_mov_b64 exec, -1"::"v"(pp),"v"(pv),"i"((c)*4096):"memory")
#define OWNRD(c) ok=tr2(oK+(c)*4096,oK+(c)*4096+256); ov=tr2(oV+(c)*4096,oV+(c)*4096+256)
#define OWNSUM() do{ _Pragma("unroll") for(int e=0;e<4;e++){ ck=dot2(ok[e],0x3F803F80u,ck); cv=dot2(ov[e],0x3F803F80u,cv); } PINW(ck); PINW(cv); }while(0)
  u32x4 ok,ov;
  WAITV(19); PATCH(0); OWNRD(0); OWNSUM();
  STAMP(1);
  BAR();
  STAMP(2);
  u32x4 xf[28],fk[4],fv[4],wp[2]; f32x16 cK={},cV={},cG={};
#define XRD(ks) xf[ks]=LDS(u32x4,xb+xs((ks)>>2)+((sw^(((ks)&3)*2))<<4))
#define ARD(ks) fk[(ks)&3]=tr2(aK+(ks)*1024,aK+(ks)*1024+256); fv[(ks)&3]=tr2(aV+(ks)*1024,aV+(ks)*1024+256)
#define MG(ks) cG=mfma(xf[ks],xf[ks],cG); PINW(cG)
#define MK(ks) cK=mfma(fk[(ks)&3],xf[ks],cK); PINW(cK)
#define MV(ks) cV=mfma(fv[(ks)&3],xf[ks],cV); PINW(cV)
#define STAGE(c,N,D0,D1,D2,D3,D4,D5,EXTRA) do{ \
    ARD(4*(c)); ARD(4*(c)+1); SB(); WAITV(N); XRD(4*(c)); XRD(4*(c)+1); XRD(4*(c)+2); XRD(4*(c)+3); SB(); \
    MG(4*(c)); SB(); ARD(4*(c)+2); SB(); MK(4*(c)); SB(); if((c)<6){ PATCH((c)+1); OWNRD((c)+1); } SB(); MV(4*(c)); SB(); \
    MG(4*(c)+1); SB(); ARD(4*(c)+3); SB(); MK(4*(c)+1); SB(); D0; SB(); MV(4*(c)+1); SB(); D1; SB(); \
    MG(4*(c)+2); SB(); if((c)<6) OWNSUM(); SB(); MK(4*(c)+2); SB(); D2; SB(); MV(4*(c)+2); SB(); D3; SB(); \
    MG(4*(c)+3); SB(); D4; SB(); MK(4*(c)+3); SB(); D5; EXTRA; SB(); MV(4*(c)+3); SB(); if((c)<6) BAR(); }while(0)
#define NOP_ do{}while(0)
  STAGE(0,12,WKDMA(4),WVDMA(4),XD(3,0),XD(3,1),XD(3,2),XD(3,3),NOP_);
  { const bool mv=(t&26)!=26; const lptr p=L+L_KVX+(t<26?t:t-2)*2+hf*224; u32 e[16];
#pragma unroll
    for(int q=0;q<16;q++){ const int d=16*(q>>3)+8*((q>>2)&1)+(q&3); e[q]=LDS(u16,p+d*56); }
#pragma unroll
    for(int q=0;q<8;q++){ u32 v=e[2*q]|(e[2*q+1]<<16); v=(mv&&(q<6||hf==0))?v:0u; asm volatile("":"+v"(v)); wp[q>>2][q&3]=v; } }
  STAGE(1,12,WKDMA(5),WVDMA(5),XD(4,0),XD(4,1),XD(4,2),XD(4,3),NOP_);
  STAGE(2,12,WKDMA(6),WVDMA(6),XD(5,0),XD(5,1),XD(5,2),XD(5,3),NOP_);
  STAGE(3,12,XD(6,0),XD(6,1),XD(6,2),XD(6,3),NOP_,NOP_,NOP_);
  STAGE(4,10,QD(0),QD(1),QD(2),QD(3),NOP_,NOP_,NOP_);
  STAGE(5,8,QD(4),QD(5),QD(6),NOP_,NOP_,NOP_,{ LDS(float,L+L_MISC+lane*16+w*4)=ck; LDS(float,L+L_MISC+1024+lane*16+w*4)=cv; });
  STAGE(6,7,NOP_,NOP_,NOP_,NOP_,NOP_,NOP_,NOP_);
  STAMP(3);
  float rstd,nmean;
  { f32x4 a=LDS(f32x4,L+L_MISC+lane*16),c=LDS(f32x4,L+L_MISC+1024+lane*16);
    nmean=swp_hi(cK[12])*(-1.f/448);
    cK=__builtin_amdgcn_mfma_f32_32x32x2f32((a[0]+a[1])+(a[2]+a[3]),nmean,cK,0,0,0);
    cV=__builtin_amdgcn_mfma_f32_32x32x2f32((c[0]+c[1])+(c[2]+c[3]),nmean,cV,0,0,0);
    float g8[8],g4[4];
#pragma unroll
    for(int e=0;e<8;e++){ float u0=cG[2*e],u1=cG[2*e+1]; asm("":"+v"(u0)); g8[e]=(t&1)?u1:u0; }
#pragma unroll
    for(int e=0;e<4;e++){ float u0=g8[2*e]; asm("":"+v"(u0)); g4[e]=(t&2)?g8[2*e+1]:u0; }
    float g2a=(t&8)?g4[1]:g4[0],g2b=(t&8)?g4[3]:g4[2],g1=(t&16)?g2b:g2a;
    auto r=__builtin_amdgcn_permlane32_swap(__float_as_uint(g1),__float_as_uint(g1),false,false);
    float ss=__uint_as_float((t&4)?r[1]:r[0]);
    rstd=__builtin_amdgcn_rsqf(__builtin_fmaxf(__builtin_fmaf(ss,1.f/448,-nmean*nmean),0.f)+1e-5f); }
  const int wro=(w*4096+t*64),c0=((2*hf)^((t>>2)&3))<<4;
  { f32x16 p=mfma(wp[0],pk8(cK,0),f32x16{}); p=mfma(wp[1],pk8(cK,8),p);
#pragma unroll
    for(int r=0;r<16;r++) cV[r]*=rstd;
    u32x4 v0=pk8(cV,0),v1=pk8(cV,8); if(hf){ v1[2]=0x3F80u; v1[3]=0u; }
    LDS(u32x4,L+L_VI+wro+c0)=v0; LDS(u32x4,L+L_VI+wro+(c0^16))=v1;
    u32x4 ph[4]; dsoftmax(p,rstd*LOG2E,ph);
    LDS(u32x4,L+L_PHI+wro+c0)=ph[0]; LDS(u32x4,L+L_PHI+wro+(c0^16))=ph[1];
    LDS(u32x4,L+L_PHI+2048+wro+c0)=ph[2]; LDS(u32x4,L+L_PHI+2048+wro+(c0^16))=ph[3]; }
  WAITV(0);
  STAMP(4);
  BAR();
  const bool act=j<14; const u32 rot=2*j;
  auto kap=[&](int n){ const u32 k=n+rot; return k>=28?k-28:k; };
  const rsrc_t rkv=mk(ws+WS_KV+j*65536,65536), r1=mk(w1,448*3584);
  const u32 v8=(lane>>3), p8=((lane&7)^(lane>>2&4))<<4, vw1=v8*3584+p8;
#define W1D(base,half,u) dma((base)+(u)*1024,vw1,r1,(kap((u)>>1)*2+((u)&1))*8*3584+j*256+(half)*128)
  if(w<2){
    const int ko=(8*hf+q4)*64+(sg&1)*8; const lptr k0=L+ko+(((sg>>1)^(2*hf))<<4),k1=L+ko+256+(((sg>>1)^(2*hf+1))<<4);
    f32x16 c={};
#pragma unroll
    for(int u=0;u<8;u++){ const int o=(u>>1)*4096+(u&1)*1024; c=mfma(tr2(k0+L_PHI+w*2048+o,k1+L_PHI+w*2048+o),tr2(k0+L_VI+o,k1+L_VI+o),c); }
    __builtin_amdgcn_raw_buffer_store_b128(pk8(c,0),rkv,i*4096+w*2048+lane*16,0,16);
    __builtin_amdgcn_raw_buffer_store_b128(pk8(c,8),rkv,i*4096+w*2048+1024+lane*16,0,16);
  } else if(act){
    const rsrc_t r0=mk(wo,448*896); const u32 vo=(lane>>2)*896+(lane&3)*16;
#pragma unroll 1
    for(int u=(w-2)*14;u<(w-1)*14;u++) dma(L_WO+u*1024,vo,r0,kap(u)*16*896+j*64);
  }
  { f32x4 cq={};
#pragma unroll
    for(int u=0;u<7;u++){ const lptr p=aQ+(u>>2)*16384+w*4096+(u&3)*1024; cq=m44(u32x2{0x3F803F80u,0x3F803F80u},tr(p),cq); cq=m44(u32x2{0x3F803F80u,0x3F803F80u},tr(p+256),cq); }
    LDS(float,L+L_MISC+2048+lane*16+w*4)=cq[0]; }
  STAMP(5);
  BAR();
  u32x4 phq[4];
  { f32x16 cQ={};
#pragma unroll
    for(int ks=0;ks<28;ks++){ const lptr p=aQ+((ks>>2)>>2)*16384+(ks&3)*4096+((ks>>2)&3)*1024; cQ=mfma(tr2(p,p+256),xf[ks],cQ);
      if(w>=2){ if(act) W1D(L_W1A,0,(w-2)*28+ks); }
      else if(ks==12){ WAITV(0); if(lane==0) setflag(ws,0,j,i,w,tag); STAMP(6); } }
    f32x4 a=LDS(f32x4,L+L_MISC+2048+lane*16);
    cQ=__builtin_amdgcn_mfma_f32_32x32x2f32((a[0]+a[1])+(a[2]+a[3]),nmean,cQ,0,0,0);
    f32x16 p=mfma(wp[0],pk8(cQ,0),f32x16{}); p=mfma(wp[1],pk8(cQ,8),p);
    dsoftmax(p,rstd*LOG2E,phq); }
  STAMP(7);
  if(w==0) poll(ws,0,j,(lane&2)==0,tag,lane);
  BAR();
  STAMP(8);
  const u32x2 id4={(lane&3)==0?0x3F80u:(lane&3)==1?0x3F800000u:0u,(lane&3)==2?0x3F80u:(lane&3)==3?0x3F800000u:0u};
  u32x4 xr0,xr1;
  { u32x4 g[16];
#pragma unroll
    for(int n=0;n<16;n++) g[n]=__builtin_amdgcn_raw_buffer_load_b128(rkv,w*1024+lane*16,((n+i)&15)*4096,16);
    xr0=__builtin_amdgcn_raw_buffer_load_b128(rx,t*896+hf*16+j*64,0,0); xr1=__builtin_amdgcn_raw_buffer_load_b128(rx,t*896+hf*16+32+j*64,0,0);
    f32x4 s[4]={};
#pragma unroll
    for(int n=0;n<16;n++){ s[2*(n&1)]=m44(id4,u32x2{g[n][0],g[n][1]},s[2*(n&1)]); s[2*(n&1)+1]=m44(id4,u32x2{g[n][2],g[n][3]},s[2*(n&1)+1]); }
    s[0]+=s[2]; s[1]+=s[3];
    LDS(u32x4,L+L_KVX+w*1024+lane*16)=u32x4{pk(s[0][0],s[0][1]),pk(s[0][2],s[0][3]),pk(s[1][0],s[1][1]),pk(s[1][2],s[1][3])}; }
  BAR();
  STAMP(9);
  const rsrc_t ro=mk(ws+WS_O+i*114688,114688);
  { f32x16 c={};
#pragma unroll
    for(int n=0;n<4;n++) c=mfma(LDS(u32x4,L+L_KVX+n*1024+lane*16),phq[n],c);
    const float rd=__builtin_amdgcn_rcpf(swp_hi(c[12])+1e-6f);
#pragma unroll
    for(int r=0;r<16;r++) c[r]*=rd;
    const u32x4 o0=pk8(c,0),o1=pk8(c,8); const u32 ub=w*28672+t*16+((7*j)>>1)*512;
    __builtin_amdgcn_raw_buffer_store_b128(o0,ro,ub+(odd+2*hf)*512,0,16);
    if(hf==0) __builtin_amdgcn_raw_buffer_store_b128(o1,ro,ub+(odd+1)*512,0,16);
    else __builtin_amdgcn_raw_buffer_store_b64(u32x2{o1[0],o1[1]},ro,ub+(odd?8:1536),0,16);
    WAITV(0); }
  if(lane==0) setflag(ws,1,i,j,w,tag);
  STAMP(10);
  const rsrc_t rh=mk(ws+WS_H+i*114688,114688), rp=mk(ws+WS_P+i*(14*114688),14*114688);
  if(act){
    if(w==0) poll(ws,1,i,true,tag,lane);
    else {
#pragma unroll 1
      for(int u=w-1;u<56;u+=3) W1D(L_W1B,1,u);
    }
    const int nq=(mu&4)|((mu&1)<<1)|((mu>>1)&1);
    if(w==0){ const int tq=t>>2, f=((tq&4)|((tq&1)<<1)|((tq>>1)&1))*4+(t&3)+hf*32;
      LDS(u32,L+L_MISC+2048+lane*4)=(u32)b1[j*128+f]<<16; LDS(u32,L+L_MISC+2048+256+lane*4)=(u32)b1[j*128+64+f]<<16; }
    BAR();
    STAMP(11);
    if(b==0&&tid==0) __hip_atomic_store((u32*)ws,tag,__ATOMIC_RELAXED,__HIP_MEMORY_SCOPE_AGENT);
    const rsrc_t rs=mk(ws+WS_HS+i*14336,14336);
    { u32x4 of[28];
#pragma unroll
      for(int n=0;n<28;n++) of[n]=__builtin_amdgcn_raw_buffer_load_b128(ro,w*28672+lane*16,kap(n)*1024,16);
      f32x16 c;
#pragma unroll
      for(int e=0;e<4;e++){ c[2*e]=__uint_as_float(xr0[e]<<16); c[2*e+1]=__uint_as_float(xr0[e]&0xffff0000u); c[8+2*e]=__uint_as_float(xr1[e]<<16); c[9+2*e]=__uint_as_float(xr1[e]&0xffff0000u); }
      const lptr aO=L+L_WO+rowo+nq*8;
#pragma unroll
      for(int n=0;n<28;n++) c=mfma(tr2(aO+n*1024,aO+n*1024+256),of[n],c);
      __builtin_amdgcn_raw_buffer_store_b128(pk8(c,0),rh,w*28672+j*2048+lane*16,0,16);
      __builtin_amdgcn_raw_buffer_store_b128(pk8(c,8),rh,w*28672+j*2048+1024+lane*16,0,16);
      float s1=0,s2=0;
#pragma unroll
      for(int r=0;r<16;r++){ s1+=c[r]; s2=__builtin_fmaf(c[r],c[r],s2); }
      s1=swp_add(s1); s2=swp_add(s2);
      if(hf==0) __builtin_amdgcn_raw_buffer_store_b64(u32x2{__float_as_uint(s1),__float_as_uint(s2)},rs,w*3584+j*256+t*8,0,16);
      WAITV(0); }
    if(lane==0) setflag(ws,2,i,j,w,tag);
    STAMP(13);
    BAR();
    const rsrc_t r2=mk(w2+(size_t)j*128*448,128*896); const u32 vw2=v8*896+p8;
#define W2D(c,u) dma(w2c(c)+(u)*1024,vw2,r2,(u)*7168+(c)*128)
    const lptr a0=L+(8*hf+q4)*128+(q4>>1)*64+nq*8; const lptr a1=L+(((8*hf+q4)*128+(q4>>1)*64+nq*8)^64);
    {
      const lptr p=((w&1)?a1:a0)+((w&2)?L_W1B:L_W1A); f32x4 cs={};
      if(w){
#pragma unroll 1
        for(int u=w-1;u<32;u+=3) dma(65536+u*1024,vw2,r2,(u&15)*7168+(u>>4)*128);
      }
#pragma unroll
      for(int n=0;n<28;n++){ cs=m44(u32x2{0x3F803F80u,0x3F803F80u},tr(p+n*2048),cs); cs=m44(u32x2{0x3F803F80u,0x3F803F80u},tr(p+n*2048+512),cs); }
      const float v=swp_add(cs[0]); if(hf==0) LDS(float,L+L_MISC+1024+w*128+t*4)=v; }
    STAMP(14);
    if(w==0) poll(ws,2,i,lane<56,tag,lane);
    BAR();
    STAMP(15);
    u32x4 hf_[28],fa[3][2],hb[8]; f32x16 c4[4],c5[8]; float sg_,K1,K2,Lg;
    { u32x2 st[7];
#pragma unroll
      for(int n=0;n<7;n++) st[n]=__builtin_amdgcn_raw_buffer_load_b64(rs,w*3584+n*512+lane*8,0,16);
#pragma unroll
      for(int n=0;n<28;n++) hf_[n]=__builtin_amdgcn_raw_buffer_load_b128(rh,w*28672+lane*16,kap(n)*1024,16);
      float cb[4];
#pragma unroll
      for(int T=0;T<4;T++) cb[T]=LDS(float,L+L_MISC+1024+hf*1024+T*128+t*4);
      float s1=0,s2=0;
#pragma unroll
      for(int n=0;n<7;n++){ s1+=__uint_as_float(st[n][0]); s2+=__uint_as_float(st[n][1]); }
      s1=swp_add(s1)*(-1.f/448); s2=swp_add(s2);
      const float v=__builtin_fmaxf(__builtin_fmaf(s2,1.f/448,-s1*s1),0.f), vv=__builtin_fmaf(v,1.00001f,1e-10f), rs_=__builtin_amdgcn_rsqf(vv);
      sg_=vv*rs_; K1=-2.302208198f*rs_; K2=-0.1029432397f*rs_*rs_*rs_; Lg=0.5f*__builtin_amdgcn_logf(vv);
      const float bb=hf?sg_:s1;
#pragma unroll
      for(int T=0;T<4;T++) c4[T]=__builtin_amdgcn_mfma_f32_32x32x2f32(cb[T],bb,f32x16{},0,0,0); }
    float t0,t1,e0,e1,g0;
#define GA(p) c4[(p)>>3][2*((p)&7)]
#define GB(p) c4[(p)>>3][2*((p)&7)+1]
#define G5(p) do{ float g1=GB(p)*e1; PINW(g0); PINW(g1); hb[(p)>>2][(p)&3]=pk(g0,g1); }while(0)
#define GS(q) do{ constexpr int p_=(q)>>2; \
      if constexpr(((q)&3)==0){ if constexpr(p_>0&&p_!=24) G5(p_>0?p_-1:0); t0=GA(p_)*GA(p_); PINW(t0); t1=GB(p_)*GB(p_); t0=__builtin_fmaf(t0,K2,K1); PINW(t0); t1=__builtin_fmaf(t1,K2,K1); PINW(t1);} \
      else if constexpr(((q)&3)==1){ t0=__builtin_fmaf(GA(p_),t0,Lg); PINW(t0); t1=__builtin_fmaf(GB(p_),t1,Lg); e0=__builtin_amdgcn_exp2f(t0); PINW(e0); PINW(t1);} \
      else if constexpr(((q)&3)==2){ e1=__builtin_amdgcn_exp2f(t1); e0+=sg_; e0=__builtin_amdgcn_rcpf(e0); PINW(e0); PINW(e1);} \
      else { e1+=sg_; e1=__builtin_amdgcn_rcpf(e1); g0=GA(p_)*e0; PINW(e1); PINW(g0);} }while(0)
lptr wb[3][2]={{a0,a1},{a0+65536,a1+65536},{a0+98304,a1+98304}};
#pragma unroll
    for(int q=1;q<3;q++){ PINW(wb[q][0]); PINW(wb[q][1]); }
#define RDA(s,h) do{ constexpr int o_=adr(s), wi_=o_>=98304?2:o_>=65536?1:0, r_=o_-(wi_==2?98304:wi_==1?65536:0); fa[(s)%3][h]=tr2(wb[wi_][h]+r_,wb[wi_][h]+r_+512); }while(0)
    CST(24); RDA(0,0); RDA(0,1); RDA(1,0); RDA(1,1); SB();
    sfor<0,56>([&](auto S){ constexpr int s=S.v,T=s<28?0:2,n=s<28?s:s-28;
      c4[T]=mfma(fa[s%3][0],hf_[n],c4[T]); PINW(c4[T]); SB();
      if constexpr(s==54){ WAITV(4); asm volatile("s_barrier":::"memory"); }
      RDA(s+2,0);
      if constexpr(s>=28) GS(2*(s-28)); SB();
      c4[T+1]=mfma(fa[s%3][1],hf_[n],c4[T+1]); PINW(c4[T+1]); SB();
      if constexpr(s==13) CST(25); if constexpr(s==27) CST(26); if constexpr(s==41) CST(28); if constexpr(s==55) CST(29);
      if constexpr(s==27||s==55) asm volatile("s_barrier":::"memory"); if constexpr(s==27) CST(27); if constexpr(s==55) CST(30);
      RDA(s+2,1);
      if constexpr(s>=28) GS(2*(s-28)+1);
      if constexpr(s>=27&&s<51&&(s&1)) W2D(2+((s-27)>>3),w*4+(((s-27)>>1)&3));
      SB(); });
    STAMP(16);
    const u32 vp=w*28672+lane*16, sp=j*114688;
#define PST(n,c) do{ __builtin_amdgcn_raw_buffer_store_b128(pk8(c,0),rp,vp+(n)*2048,sp,16); __builtin_amdgcn_raw_buffer_store_b128(pk8(c,8),rp,vp+(n)*2048+1024,sp,16); }while(0)
    sfor<56,112>([&](auto S){ constexpr int s=S.v,u=s-56, ks=u<32?(u>>3)*2+(u&1):(u&7), A=u<32?(u&6):((u>>3)&1)*2; constexpr bool first=u<32?ks==0:(u&7)==0;
      if constexpr(u==16) CST(31); if constexpr(u==24) CST(32); if constexpr(u==32) CST(33); if constexpr(u==40) CST(34); if constexpr(u==48) CST(35);
      if constexpr(u==16){ sfor<88,96>([&](auto Q){ GS(Q.v); }); G5(23); SB(); }
      if constexpr(u==24){ sfor<112,128>([&](auto Q){ GS(Q.v); }); G5(31); SB(); }
      if constexpr(first) c5[A]=mfma(fa[s%3][0],hb[ks],f32x16{}); else c5[A]=mfma(fa[s%3][0],hb[ks],c5[A]);
      PINW(c5[A]); SB();
      if constexpr(u==30){ WAITV(8); asm volatile("s_barrier":::"memory"); }
      if constexpr(s+2<112) RDA(s+2,0);
      if constexpr(u<16) GS(56+2*u); else if constexpr(u<24) GS(96+2*(u-16));
      if constexpr(u>=27&&u<=33&&(u&1)) PST(u-27,c5[u-27]);
      if constexpr(u==42||u==50) PST(8+((u-42)>>2),c5[((u-42)>>2)&2]);
      SB();
      if constexpr(first) c5[A+1]=mfma(fa[s%3][1],hb[ks],f32x16{}); else c5[A+1]=mfma(fa[s%3][1],hb[ks],c5[A+1]);
      PINW(c5[A+1]); SB();
      if constexpr(s+2<112) RDA(s+2,1);
      if constexpr(u<16) GS(57+2*u); else if constexpr(u<24) GS(97+2*(u-16));
      if constexpr(u<8) W2D(5+(u>>2),w*4+(u&3));
      if constexpr(u>=27&&u<=33&&(u&1)) PST(u-26,c5[u-26]);
      if constexpr(u==42||u==50) PST(9+((u-42)>>2),c5[(((u-42)>>2)&2)+1]);
      SB(); });
    CST(36); PST(12,c5[0]); PST(13,c5[1]); CST(37);
    STAMP(17);
    WAITV(0);
    if(lane==0) setflag(ws,3,i,j,w,tag);
    STAMP(19);
  }
  { const u32 U=w*14+(lane>>3), vb=lane<56?U*16:0x80000000u, vq=lane<56?(j>>2)*28672+U*512+((j&3)*8+(lane&7))*16:0x80000000u;
    const rsrc_t rb=mk(b2,896);
    const u32x4 q0=__builtin_amdgcn_raw_buffer_load_b128(rb,vb,0,0),q1=__builtin_amdgcn_raw_buffer_load_b128(rb,vb+112,0,0);
    if(w==0) poll(ws,3,i,lane<56&&(lane&3)==(j>>2),tag,lane);
    BAR();
    STAMP(20);
    u32x4 g[15][2];
#pragma unroll
    for(int n=0;n<14;n++){ const u32 k=n+j, so=(k>=28?k-28:k>=14?k-14:k)*114688; g[n][0]=__builtin_amdgcn_raw_buffer_load_b128(rp,vq,so,16); g[n][1]=__builtin_amdgcn_raw_buffer_load_b128(rp,vq+3584,so,16); }
    g[14][0]=__builtin_amdgcn_raw_buffer_load_b128(rh,vq,0,16); g[14][1]=__builtin_amdgcn_raw_buffer_load_b128(rh,vq+3584,0,16);
    f32x4 s[4]; s[0]=m44(id4,u32x2{q0[0],q0[1]},f32x4{}); s[1]=m44(id4,u32x2{q0[2],q0[3]},f32x4{}); s[2]=m44(id4,u32x2{q1[0],q1[1]},f32x4{}); s[3]=m44(id4,u32x2{q1[2],q1[3]},f32x4{});
#pragma unroll
    for(int n=0;n<15;n++){ s[0]=m44(id4,u32x2{g[n][0][0],g[n][0][1]},s[0]); s[1]=m44(id4,u32x2{g[n][0][2],g[n][0][3]},s[1]); s[2]=m44(id4,u32x2{g[n][1][0],g[n][1][1]},s[2]); s[3]=m44(id4,u32x2{g[n][1][2],g[n][1][3]},s[3]); }
    const rsrc_t rout=mk(out+(size_t)(i*128+j*8)*448,8*896); const u32 vo=lane<56?(lane&7)*896+U*16:0x80000000u;
    __builtin_amdgcn_raw_buffer_store_b128(u32x4{pk(s[0][0],s[0][1]),pk(s[0][2],s[0][3]),pk(s[1][0],s[1][1]),pk(s[1][2],s[1][3])},rout,vo,0,0);
    __builtin_amdgcn_raw_buffer_store_b128(u32x4{pk(s[2][0],s[2][1]),pk(s[2][2],s[2][3]),pk(s[3][0],s[3][1]),pk(s[3][2],s[3][3])},rout,vo+112,0,0); }
  STAMP(21); CST(47);
  if(lane==0){ ((u32*)(ws+(1<<19)))[(b*NWAVE+w)*2]=pa_; ((u32*)(ws+(1<<19)))[(b*NWAVE+w)*2+1]=pb_; }
  if(b==PBLK){ u32 d=PROBE==2?(cb_-ca_)/10:(pb_-pa_)*PMUL; if(d>40000) d=40000; uint64_t t0_,t1_; asm volatile("s_memrealtime %0\n\ts_waitcnt lgkmcnt(0)":"=s"(t0_)::"memory");
    do{ asm volatile("s_memrealtime %0\n\ts_waitcnt lgkmcnt(0)":"=s"(t1_)::"memory"); }while((u32)t1_-(u32)t0_<d); }
}
}
__global__ void emptyk(char* p){ uint64_t t0,t1; asm volatile("s_memrealtime %0\n\ts_waitcnt lgkmcnt(0)":"=s"(t0)::"memory");
  int r[2];
  for(int k=0;k<2;k++){ int mn=0x7fffffff,mx=-1,sm=0,c=0; for(int q=threadIdx.x;q<1024;q+=64){ unsigned s=((unsigned*)(p+(1<<19)))[q*2+k]; if(s){ int v=(int)((unsigned)t0-s); mn=v<mn?v:mn; mx=v>mx?v:mx; sm+=v; c++; } }
    for(int o=32;o;o>>=1){ int v=__shfl_xor(mn,o); mn=v<mn?v:mn; v=__shfl_xor(mx,o); mx=v>mx?v:mx; sm+=__shfl_xor(sm,o); c+=__shfl_xor(c,o); }
    int R=k?RB:RA; r[k]=R==0?mx:R==1?mn:sm/(c?c:1); }
  unsigned d=__builtin_amdgcn_readfirstlane(r[0]-r[1])*PMUL; d+=0; if(d>6000) d=6000;
  asm volatile("s_memrealtime %0\n\ts_waitcnt lgkmcnt(0)":"=s"(t0)::"memory");
  do{ asm volatile("s_memrealtime %0\n\ts_waitcnt lgkmcnt(0)":"=s"(t1)::"memory"); }while((unsigned)t1-(unsigned)t0<d); }
extern "C" void kernel_launch(void* const* d_in,const int* in_sizes,int n_in,void* d_out,int out_size,void* d_ws,size_t ws_size,hipStream_t stream){
  typedef const unsigned short* P;
  (void)hipFuncSetAttribute((const void*)fk::fused,hipFuncAttributeMaxDynamicSharedMemorySize,163840);
  fk::fused<<<256,256,163840,stream>>>((P)d_in[0],(P)d_in[1],(P)d_in[2],(P)d_in[3],(P)d_in[5],(P)d_in[6],(P)d_in[7],(P)d_in[8],(unsigned short*)d_out,(char*)d_ws);
  emptyk<<<1,64,0,stream>>>((char*)d_ws);
}
```

```cpp
#include <hip/hip_runtime.h>
#include <stdint.h>
#ifdef __HIP_DEVICE_COMPILE__
#pragma clang attribute push(__attribute__((target("no-packed-fp32-ops"))),apply_to=function)
#endif
namespace fk {
typedef unsigned short u16; typedef unsigned u32;
typedef __attribute__((ext_vector_type(4))) u32 u32x4;
typedef __attribute__((ext_vector_type(2))) u32 u32x2;
typedef __attribute__((ext_vector_type(8))) __bf16 bf16x8;
typedef __attribute__((ext_vector_type(2))) __bf16 bf16x2;
typedef __attribute__((ext_vector_type(2))) float f32x2;
typedef __attribute__((ext_vector_type(4))) float f32x4;
typedef __attribute__((ext_vector_type(16))) float f32x16;
typedef short v4i16_t __attribute__((ext_vector_type(4)));
typedef __attribute__((address_space(3))) char* lptr;
typedef __amdgpu_buffer_rsrc_t rsrc_t;
#define DI __device__ __forceinline__
#define LDS(T,a) (*(__attribute__((address_space(3))) T*)(a))
#define STAMP(n)
#define CST(n)
constexpr u32 WS_FLAG=4096, WS_ST=512u<<10, WS_KV=1u<<20, WS_O=2u<<20, WS_H=4u<<20, WS_HH=8u<<20, OOB=0x80000000u;
constexpr int L_WK=98304, L_MISC=155648, L_CNT=L_MISC+2048, L_PF=L_CNT+64, L_KVX=159744, L_ST=159744;
constexpr int L_PHI=32768, L_VI=49152, L_WO=98304, L_R2=32768, L_R3=98304;
constexpr int w1l(int ks){ return ks<12?98304+ks*4096:ks<20?65536+(ks-12)*4096:32768+(ks-20)*4096; }
constexpr int xs(int c){ return (c%6==0?4:c%6==5?5:c%6-1)*16384; }
constexpr float LOG2E=1.4426950408889634f;
#define MAP 1
#define PF1C 9
#define PF2C 9
#define PZC 3
#define DEP 3
constexpr int xtra(int c){ return (c==PF1C?2:0)+(c==PZC?7:0)+(c==PF2C?3:0); }
constexpr int after(int c){ int n=c<DEP?(DEP-1-c)*3:xtra(c-DEP); for(int q=(c<DEP?0:c-DEP+1);q<c;q++) n+=(q+DEP<=6?3:0)+xtra(q); return n; }
constexpr int afterz(){ int n=(PF1C==PZC?2:0)+(PF2C==PZC?3:0); for(int q=PZC+1;q<7;q++) n+=(q+DEP<=6?3:0)+xtra(q); return n; }
#define NOUNR _Pragma("clang loop unroll(disable)")
DI float u2f(u32 v){ return __builtin_bit_cast(float,v); }
DI u32 f2u(float v){ return __builtin_bit_cast(u32,v); }
template<int V> struct IC{ static constexpr int v=V; };
template<int I,int N,class F> DI void sfor(F&& f){ if constexpr(I<N){ f(IC<I>{}); sfor<I+1,N>(f); } }
DI rsrc_t mk(const void* p,u32 bytes){ return __builtin_amdgcn_make_buffer_rsrc((void*)p,0,bytes,0x00020000); }
DI void dma(u32 l,u32 voff,rsrc_t r,u32 soff){ asm volatile("s_mov_b32 m0, %0\n\ts_nop 0\n\tbuffer_load_dwordx4 %1, %2, %3 offen lds"::"s"(l),"v"(voff),"s"(r),"s"(soff):"memory"); }
#define WAITV(n) asm volatile("s_waitcnt vmcnt(" #n ")":::"memory")
#define BAR() asm volatile("s_waitcnt lgkmcnt(0)\n\ts_barrier":::"memory")
#define PIN(x) asm volatile(""::"v"(x))
#define PINW(x) asm volatile("":"+v"(x))
#define SB() __builtin_amdgcn_sched_barrier(0)
DI f32x16 mfma(u32x4 a,u32x4 b,f32x16 c){ return __builtin_amdgcn_mfma_f32_32x32x16_bf16(__builtin_bit_cast(bf16x8,a),__builtin_bit_cast(bf16x8,b),c,0,0,0); }
DI f32x4 m44(u32x2 a,u32x2 b,f32x4 c){ return __builtin_amdgcn_mfma_f32_4x4x4bf16_1k(__builtin_bit_cast(v4i16_t,a),__builtin_bit_cast(v4i16_t,b),c,0,0,0); }
DI u32x2 tr(lptr p){ return __builtin_bit_cast(u32x2,__builtin_amdgcn_ds_read_tr16_b64_v4i16((__attribute__((address_space(3))) v4i16_t*)p)); }
DI u32x4 tr2(lptr p0,lptr p1){ u32x2 a=tr(p0),b=tr(p1); return u32x4{a[0],a[1],b[0],b[1]}; }
DI u32 pk(float lo,float hi){ return __builtin_bit_cast(u32,__builtin_convertvector(f32x2{lo,hi},bf16x2)); }
DI float dot2(u32 a,u32 b,float c){ return __builtin_amdgcn_fdot2_f32_bf16(__builtin_bit_cast(bf16x2,a),__builtin_bit_cast(bf16x2,b),c,false); }
DI float swp_lo(float v){ auto r=__builtin_amdgcn_permlane32_swap(f2u(v),f2u(v),false,false); return u2f(r[0]); }
DI float swp_hi(float v){ auto r=__builtin_amdgcn_permlane32_swap(f2u(v),f2u(v),false,false); return u2f(r[1]); }
DI float swp_add(float v){ auto r=__builtin_amdgcn_permlane32_swap(f2u(v),f2u(v),false,false); return u2f(r[0])+u2f(r[1]); }
DI float swp_max(float v){ auto r=__builtin_amdgcn_permlane32_swap(f2u(v),f2u(v),false,false); return __builtin_fmaxf(u2f(r[0]),u2f(r[1])); }
DI float swp_min(float v){ auto r=__builtin_amdgcn_permlane32_swap(f2u(v),f2u(v),false,false); return __builtin_fminf(u2f(r[0]),u2f(r[1])); }
DI float red8(float v){ v+=u2f(__builtin_amdgcn_update_dpp(0u,f2u(v),0x128,0xf,0xf,false)); auto r=__builtin_amdgcn_permlane16_swap(f2u(v),f2u(v),false,false); return swp_add(u2f(r[0])+u2f(r[1])); }
DI u32x4 pk8(const f32x16& c,int b){ return u32x4{pk(c[b],c[b+1]),pk(c[b+2],c[b+3]),pk(c[b+4],c[b+5]),pk(c[b+6],c[b+7])}; }
DI void dsoftmax(const f32x16& p,float c,u32x4* ph){
  float mx=p[0],mn=p[0];
#pragma unroll
  for(int r=1;r<14;r++){ mx=__builtin_fmaxf(mx,p[r]); mn=__builtin_fminf(mn,p[r]); }
  mx=swp_max(mx); mn=swp_min(mn);
  const float a=-mx*c,b=mn*c; f32x16 e,g; float s=0,u=0;
#pragma unroll
  for(int r=0;r<14;r++){ e[r]=__builtin_amdgcn_exp2f(__builtin_fmaf(p[r],c,a)); g[r]=__builtin_amdgcn_exp2f(__builtin_fmaf(p[r],-c,b)); s+=e[r]; u+=g[r]; }
  s=__builtin_amdgcn_rcpf(swp_add(s)); u=__builtin_amdgcn_rcpf(swp_add(u));
#pragma unroll
  for(int r=0;r<14;r++){ e[r]*=s; g[r]*=u; }
  ph[0]=pk8(e,0); ph[1]=u32x4{pk(e[8],e[9]),pk(e[10],e[11]),pk(e[12],e[13]),0u};
  ph[2]=pk8(g,0); ph[3]=u32x4{pk(g[8],g[9]),pk(g[10],g[11]),pk(g[12],g[13]),0u};
}
DI void dmac(u32 l,u32 voff,rsrc_t r,u32 soff){ asm volatile("s_mov_b32 m0, %0\n\ts_nop 0\n\tbuffer_load_dwordx4 %1, %2, %3 offen sc1 lds"::"s"(l),"v"(voff),"s"(r),"s"(soff):"memory"); }
DI void pfl(u32 voff,rsrc_t r,u32 soff){ asm volatile("s_mov_b32 m0, %0\n\ts_nop 0\n\tbuffer_load_dword %1, %2, %3 offen lds"::"s"(L_PF),"v"(voff),"s"(r),"s"(soff):"memory"); }
template<int O> DI void rld(u32x4& v,u32 voff,rsrc_t r,u32 soff){ asm volatile("buffer_load_dwordx4 %0, %1, %2, %3 offen offset:%4 sc1":"+v"(v):"v"(voff),"s"(r),"s"(soff),"n"(O):"memory"); }
template<int O> DI u32x4 ldc(u32 voff,rsrc_t r,u32 soff){ u32x4 v; asm volatile("buffer_load_dwordx4 %0, %1, %2, %3 offen offset:%4 sc1":"=v"(v):"v"(voff),"s"(r),"s"(soff),"n"(O):"memory"); return v; }
DI u32x4 ldp(u32 voff,rsrc_t r,u32 soff){ u32x4 v; asm volatile("buffer_load_dwordx4 %0, %1, %2, %3 offen":"=v"(v):"v"(voff),"s"(r),"s"(soff):"memory"); return v; }
template<int N> DI void waitv(){ asm volatile("s_waitcnt vmcnt(%0)"::"n"(N):"memory"); }
template<int O=0> DI void st16(u32x4 v,rsrc_t r,u32 voff,u32 soff){ asm volatile("buffer_store_dwordx4 %0, %1, %2, %3 offen offset:%4 sc1\n\ts_nop 1"::"v"(v),"v"(voff),"s"(r),"s"(soff),"n"(O):"memory"); }
DI u32 mx3(u32 a,u32 b,u32 c){ u32 r; asm("v_max3_u32 %0, %1, %2, %3":"=v"(r):"v"(a),"v"(b),"v"(c)); return r; }
DI u32 mx4(u32 m,u32x4 g){ return mx3(mx3(m,g[0],g[1]),g[2],g[3]); }
DI bool bad(u32 m){ return __builtin_amdgcn_ballot_w64(m==0xFFFFFFFFu)!=0; }
constexpr int SPIN=30000;
DI void await(u32 voff,rsrc_t r,u32 soff){ NOUNR for(int it=0;it<SPIN;it++){ u32x4 v=ldc<0>(voff,r,soff); asm volatile("s_waitcnt vmcnt(0)":"+v"(v)::"memory"); if(!bad(mx3(mx3(v[0],v[1],v[2]),v[3],v[3]))) break; } }
DI void lsig(lptr p){ asm volatile("s_mov_b64 exec, 1\n\tds_add_u32 %0, %1\n\ts_mov_b64 exec, -1"::"v"(p),"v"(1u):"memory"); }
DI void lwait(lptr p,u32 n){ NOUNR for(int it=0;it<SPIN*8;it++){ u32 v; asm volatile("ds_read_b32 %0, %1\n\ts_waitcnt lgkmcnt(0)":"=v"(v):"v"(p):"memory"); if(__builtin_amdgcn_readfirstlane(v)>=n) break; } }
DI float sum8(u32x4 f,float c){ c=dot2(f[0],0x3F803F80u,c); c=dot2(f[1],0x3F803F80u,c); c=dot2(f[2],0x3F803F80u,c); return dot2(f[3],0x3F803F80u,c); }
DI float sq8(u32x4 f,float c){ c=dot2(f[0],f[0],c); c=dot2(f[1],f[1],c); c=dot2(f[2],f[2],c); return dot2(f[3],f[3],c); }
DI void acc8(u32x2 id4,u32x4 g,f32x4& a,f32x4& b){ a=m44(id4,u32x2{g[0],g[1]},a); b=m44(id4,u32x2{g[2],g[3]},b); }
DI u32x4 pk44(f32x4 a,f32x4 b){ return u32x4{pk(a[0],a[1]),pk(a[2],a[3]),pk(b[0],b[1]),pk(b[2],b[3])}; }
#define PRIO(n) __builtin_amdgcn_s_setprio(n)

__global__ __launch_bounds__(512,2) void fused(const u16* __restrict__ x,const u16* __restrict__ wqkv,const u16* __restrict__ wphi,const u16* __restrict__ wo,
    const u16* __restrict__ w1,const u16* __restrict__ b1,const u16* __restrict__ w2,const u16* __restrict__ b2,u16* __restrict__ out,char* __restrict__ ws
    ){
  extern __shared__ __attribute__((aligned(16))) char smem[];
  const lptr L=(lptr)smem;
  const int tid=__builtin_amdgcn_workitem_id_x(),lane=tid&63,w=__builtin_amdgcn_readfirstlane(tid>>6),tt=w&3,ty=w>>2,t=lane&31,hf=lane>>5;
  const int b=__builtin_amdgcn_workgroup_id_x(),i=(b&7)*2+((b>>3)&1),j=b>>4;
  const int i1=MAP?j:i,j1=MAP?i:j,odd=j1&1;
  STAMP(0);
  asm volatile(""::"s"(x),"s"(wqkv),"s"(wphi),"s"(wo),"s"(w1),"s"(b1),"s"(w2),"s"(b2),"s"(out),"s"(ws));
  const u32 epoch=*(const __attribute__((address_space(4))) u32*)ws;
  const rsrc_t rw=mk(wqkv,448*2688), rx=mk(x+(size_t)i1*128*448,128*896);
  const u32 vw=(lane>>2)*2688+(lane&3)*16, hoff=56*j1-8*odd+tt*16*2688, sW=hoff+(ty+1)*896, lW=L_WK+ty*28672+tt*1024;
  const u32 vx0=(lane>>3)*896+(((lane&7)^(lane>>4))<<4), vx1=vx0^64, sX=(tt*32+ty*16)*896, lX=tt*4096+ty*2048;
#define CH(c) do{ dma(lW+(c)*4096,vw,rw,sW+(c)*64*2688); dma(xs(c)+lX,vx0,rx,sX+(c)*128); dma(xs(c)+lX+1024,vx1,rx,sX+7168+(c)*128); }while(0)
#define SBAR() asm volatile("s_barrier":::"memory")
  if(w<2) dma(L_MISC+w*1024,tid*16,mk(wphi+j1*784,1568),0);
  CH(0); sfor<1,DEP>([&](auto C){ SBAR(); CH(C.v); });
  if(tid<2) LDS(u32x4,L+L_CNT+tid*16)=u32x4{0,0,0,0};
  const bool act=j<14; const int th=j>=7, cs=j-7*th, tt2=w&1, kq=w>>1;
  const int q4=(lane&15)>>2,mu=((lane>>4)&1)*4+(lane&3),sg=4*(mu&1)+(mu>>1);
  const int rowo=(8*hf+q4)*64, kslot=(mu+odd)&7;
  lptr aW[4],aQ[4],xb[4];
  { const int sw=hf^((t>>1)&7), sl=ty?(odd?(sg==7?0:((sg+1)%7)+1):sg):kslot;
#pragma unroll
    for(int u=0;u<4;u++){ const int r=(u+tt)&3; aW[u]=L+L_WK+ty*28672+rowo+sl*8+r*1024; aQ[u]=L+rowo+kslot*8+r*1024; xb[u]=L+(tt*32+t)*128+((sw^(r*2))<<4); PINW(aW[u]); PINW(xb[u]); } }
  const lptr pp=L+lW+(lane&15)*64+(odd?0:56);
  u32 tag=epoch*0x9E3779B1u+0x7F4A7C15u; asm volatile("":"+s"(tag));
  const rsrc_t ro1=mk(ws+WS_O+i1*114688,114688), r1=mk(w1,act?448*3584:0), r2=mk(w2,act?1792*896:0), r0=mk(wo,act?448*896:0);
  const u32 l16=lane*16;
  const rsrc_t rkv=mk(ws+WS_KV+j1*65536,65536);
  const bool duty=((b>>3)&1)==(((b&7)-1u)<3u); const u32 dq=(b>>4)*8+w;
  auto ost=[&](u32x4 o0,u32x4 o1){ const u32 ub=t*16+((7*j1)>>1)*512, so=tt*28672;
    st16(o0,ro1,ub+(odd+2*hf)*512,so); st16(o1,ro1,hf?OOB:ub+(odd+1)*512,so);
    __builtin_amdgcn_raw_buffer_store_b64(u32x2{o1[0],o1[1]},ro1,hf?ub+(odd?8:1536):OOB,so,16); };
  u32x4 PZ={~0u,~0u,~0u,~0u}; PINW(PZ);
  const u32x2 pv={0x3F80u,0u};
  const lptr cnt=L+L_CNT;
  u32x4 xf[28],wp[2],fw[2][4]; f32x16 c1={}; float ss=0,cz=0;
  auto mm=[&](auto C){ constexpr int c=C.v;
#pragma unroll
    for(int u=0;u<4;u++){ c1=mfma(fw[c&1][u],xf[4*c+u],c1); PINW(c1); }
    SB(); };
  auto dd=[&](auto C){ constexpr int c=C.v; cz=sum8(fw[c&1][0],cz);
    if(ty){ ss=sq8(xf[4*c+2],ss); ss=sq8(xf[4*c+3],ss); } else { ss=sq8(xf[4*c],ss); ss=sq8(xf[4*c+1],ss); }
    PINW(ss); PINW(cz); SB(); };
  sfor<0,7>([&](auto C){ constexpr int c=C.v;
    waitv<after(c)>();
    asm volatile("s_mov_b64 exec, 0xffff\n\tds_write_b64 %0, %1 offset:%2\n\ts_mov_b64 exec, -1"::"v"(pp),"v"(pv),"i"(c*4096):"memory");
    BAR();
    if constexpr(c==0) STAMP(1); else STAMP(25+c);
    if constexpr(c==6){ if(!ty) PRIO(2); }
#pragma unroll
    for(int u=0;u<4;u++){ fw[c&1][u]=tr2(aW[u]+c*4096,aW[u]+c*4096+256); xf[4*c+u]=LDS(u32x4,xb[u]+xs(c)); }
    SB();
    if constexpr(c+DEP<=6) CH(c+DEP);
    if constexpr(c==PZC){
      if(ty){ sfor<0,7>([&](auto U){ dma(tt*1024+U.v*4096,vw,rw,hoff+U.v*64*2688); }); }
      else { st16(PZ,rkv,l16,i1*4096+tt*1024); ost(PZ,PZ); const int t1=j1>=7; const rsrc_t rp=mk(ws+WS_H+i1*114688,j1<14?114688:0); const u32 so=(2*t1+(tt&1))*28672+(4*(j1-7*t1)+(tt>>1))*1024; st16(PZ,rp,l16,so); st16<2048>(PZ,rp,l16,so);
        st16(PZ,mk(ws+WS_ST+i1*8192,8192),lane<8?l16:OOB,(j1*4+tt)*128); } }
    if constexpr(c==PF1C){ const rsrc_t r=mk(w1,duty?448*3584:0); pfl(lane*128,r,dq*12544); pfl(lane<34?lane*128+8192:OOB,r,dq*12544); }
    if constexpr(c==PF2C){ pfl(lane<25?lane*128:OOB,mk(wo,duty?448*896:0),dq*3136); const rsrc_t r=mk(w2,duty?1792*896:0); pfl(lane*128,r,dq*12544); pfl(lane<34?lane*128+8192:OOB,r,dq*12544); }
    SB();
    if constexpr(c>0){ mm(IC<c-1>{}); dd(IC<c-1>{}); }
    if constexpr(c==0){
      const bool mv=(t&26)!=26; const lptr p=L+L_MISC+(t<26?t:t-2)*2+hf*224; u32 e[16];
#pragma unroll
      for(int q=0;q<16;q++){ const int d=16*(q>>3)+8*((q>>2)&1)+(q&3); e[q]=LDS(u16,p+d*56); }
#pragma unroll
      for(int q=0;q<8;q++){ u32 v=e[2*q]|(e[2*q+1]<<16); v=(mv&&(q<6||hf==0))?v:0u; asm volatile("":"+v"(v)); wp[q>>2][q&3]=v; } }
  });
  dd(IC<6>{});
  LDS(float,L+L_KVX+w*256+lane*4)=ss; LDS(float,L+L_KVX+2048+ty*1024+l16+tt*4)=cz;
  SB(); mm(IC<6>{});
  BAR();
  { f32x4 z=LDS(f32x4,L+L_KVX+2048+ty*1024+l16); ss+=LDS(float,L+L_KVX+(w^4)*256+lane*4); cz=(z[0]+z[1])+(z[2]+z[3]); }
  STAMP(2);
  const float nmean=swp_hi(c1[12])*(-1.f/448);
  c1=__builtin_amdgcn_mfma_f32_32x32x2f32(cz,nmean,c1,0,0,0);
  const float rstd=__builtin_amdgcn_rsqf(__builtin_fmaxf(__builtin_fmaf(swp_add(ss),1.f/448,-nmean*nmean),0.f)+1e-5f);
  const int wro=(tt*4096+t*64),c0=((2*hf)^((t>>2)&3))<<4;
  const rsrc_t rhh=mk(ws+WS_HH+i*458752,act?458752:0);
  const u32 v8=(lane>>3), p8=((lane&7)^(lane>>2&4))<<4, vw2=v8*896+p8, sW2=w*224*896+cs*128;
#define W2D(l,n) do{ dma((l),vw2,r2,sW2+(n)*14336); dma((l)+1024,vw2,r2,sW2+(n)*14336+7168); }while(0)
  const int nq=(mu&4)|((mu&1)<<1)|((mu>>1)&1);
  const int ao=(8*hf+q4)*128+(q4>>1)*64+nq*8; const lptr a0=L+ao,a1=L+(ao^64);
  const u32x2 id4={(lane&3)==0?0x3F80u:(lane&3)==1?0x3F800000u:0u,(lane&3)==2?0x3F80u:(lane&3)==3?0x3F800000u:0u};
  const u32 vo=(lane&7)*896+v8*16, so_=((2*th+tt2)*32+kq*8)*896+cs*128, vf=v8*512+(lane&7)*16+kq*128;
  const u32 vw1=v8*3584+p8, sW1=(tt&1)*8*3584+j*256+(tt>>1)*128;
  u32x4 phq[4],w1f[28],w2f[14][2],xr; u32 b1v;
  auto misc=[&]{ const u32 sh=ty*229376+(8*j+2*tt)*1024;
    st16(PZ,rhh,l16,sh); st16<1024>(PZ,rhh,l16,sh); st16(PZ,rhh,l16,sh+114688); st16<1024>(PZ,rhh,l16,sh+114688);
    xr=ldp(vo,mk(x+(size_t)i*128*448,128*896),so_);
    const int tq=t>>2; asm volatile("buffer_load_ushort %0, %1, %2, %3 offen":"=v"(b1v):"v"((((tq&4)|((tq&1)<<1)|((tq>>1)&1))*4+(t&3))*2),"s"(mk(b1,act?3584:0)),"s"(j*256+tt*64):"memory"); };
  auto rdw1=[&]{ lwait(cnt+20,8); lptr q[4];
#pragma unroll
    for(int u=0;u<4;u++){ q[u]=L+65536+((tt&1)?ao^64:ao)+(tt>>1)*2048+((u+tt)&3)*4096; PINW(q[u]); }
#pragma unroll
    for(int n=12;n<20;n++) w1f[n]=tr2(q[n&3]+(w1l(n&~3)-65536),q[n&3]+(w1l(n&~3)-65536)+512);
#pragma unroll
    for(int n=0;n<8;n++) w1f[n]=tr2(q[n&3]+(w1l(n&~3)-65536),q[n&3]+(w1l(n&~3)-65536)+512);
#pragma unroll
    for(int u=0;u<4;u++){ q[u]+=65536; PINW(q[u]); }
#pragma unroll
    for(int n=8;n<12;n++) w1f[n]=tr2(q[n&3],q[n&3]+512);
#pragma unroll
    for(int u=0;u<4;u++){ q[u]-=98304; PINW(q[u]); }
#pragma unroll
    for(int n=20;n<28;n++) w1f[n]=tr2(q[n&3]+(w1l(n&~3)-32768),q[n&3]+(w1l(n&~3)-32768)+512); };
  if(ty==0){
    f32x16 p=mfma(wp[0],pk8(c1,0),f32x16{}); p=mfma(wp[1],pk8(c1,8),p);
    u32x4 ph[4]; dsoftmax(p,rstd*LOG2E,ph);
    LDS(u32x4,L+L_PHI+wro+c0)=ph[0]; LDS(u32x4,L+L_PHI+wro+(c0^16))=ph[1];
    LDS(u32x4,L+L_PHI+2048+wro+c0)=ph[2]; LDS(u32x4,L+L_PHI+2048+wro+(c0^16))=ph[3];
    waitv<afterz()>(); lsig(cnt);
    STAMP(3);
    lwait(cnt,8);
    if(tt<2){
      const int ko=(8*hf+q4)*64+(sg&1)*8; const lptr k0=L+ko+(((sg>>1)^(2*hf))<<4),k1=L+ko+256+(((sg>>1)^(2*hf+1))<<4);
      f32x16 c={};
#pragma unroll
      for(int u=0;u<8;u++){ const int o=(u>>1)*4096+(u&1)*1024; c=mfma(tr2(k0+L_PHI+tt*2048+o,k1+L_PHI+tt*2048+o),tr2(k0+L_VI+o,k1+L_VI+o),c); }
      st16(pk8(c,0),rkv,l16,i1*4096+tt*2048); st16<1024>(pk8(c,8),rkv,l16,i1*4096+tt*2048);
      lsig(cnt+8);
      STAMP(4);
    } else if(tid==128) __hip_atomic_store((u32*)(ws+WS_FLAG+(j1*16+i1)*128),tag,__ATOMIC_RELAXED,__HIP_MEMORY_SCOPE_AGENT);
    lwait(cnt+8,2);
    STAMP(5);
    sfor<20,28>([&](auto K){ dma(w1l(K.v)+tt*1024,vw1,r1,sW1+K.v*16*3584); });
    STAMP(6);
    { const rsrc_t rf=mk(ws,2u<<20); const u32 n=lane&15, vp=lane<16?WS_FLAG+(j1*16+n)*128:lane<32?WS_FLAG+(n*16+i)*128:lane<48?WS_KV+j1*65536+n*4096+tt*1024+1020:OOB;
      NOUNR for(int it=0;it<SPIN;it++){ u32 v; asm volatile("buffer_load_dword %0, %1, %2, 0 offen sc1\n\ts_waitcnt vmcnt(0)":"=v"(v):"v"(vp),"s"(rf):"memory");
        if(__builtin_amdgcn_ballot_w64(lane<32?v!=tag:v==~0u)==0) break; }
      lsig(cnt+20); }
    STAMP(8);
    u32x4 g[16];
    sfor<0,16>([&](auto N){ g[N.v]=ldc<0>(l16,rkv,((N.v+i1)&15)*4096+tt*1024); });
    misc();
    rdw1(); SB();
    STAMP(7);
    f32x4 s[4];
    NOUNR for(int it=0;it<SPIN;it++){ u32 m=0; s[0]=s[1]=s[2]=s[3]=f32x4{};
      sfor<0,8>([&](auto N){ constexpr int n=N.v; asm volatile("s_waitcnt vmcnt(%2)":"+v"(g[2*n]),"+v"(g[2*n+1]):"n"(20-2*n):"memory"); acc8(id4,g[2*n],s[0],s[1]); acc8(id4,g[2*n+1],s[2],s[3]); m=mx4(mx4(m,g[2*n]),g[2*n+1]); });
      if(!bad(m)) break;
      sfor<0,16>([&](auto N){ rld<0>(g[N.v],l16,rkv,((N.v+i1)&15)*4096+tt*1024); }); WAITV(0); }
    LDS(u32x4,L+L_KVX+tt*1024+l16)=pk44(s[0]+s[2],s[1]+s[3]);
    PRIO(0);
  } else {
#pragma unroll
    for(int r=0;r<16;r++) c1[r]*=rstd;
    u32x4 v0=pk8(c1,0),v1=pk8(c1,8); if(hf){ v1[2]=0x3F80u; v1[3]=0u; }
    LDS(u32x4,L+L_VI+wro+c0)=v0; LDS(u32x4,L+L_VI+wro+(c0^16))=v1;
    lsig(cnt);
    STAMP(3);
    sfor<0,20>([&](auto K){ dma(w1l(K.v)+tt*1024,vw1,r1,sW1+K.v*16*3584); });
    waitv<afterz()+20>();
    u32x4 fq[28]; float cq=0;
#pragma unroll
    for(int u=0;u<7;u++) fq[4*u]=tr2(aQ[0]+u*4096,aQ[0]+u*4096+256);
#pragma unroll
    for(int u=0;u<7;u++) cq=sum8(fq[4*u],cq);
    LDS(float,L+L_MISC+l16+tt*4)=cq;
    lsig(cnt+4); lwait(cnt+4,4);
    STAMP(4);
    auto ord=[](int s){ return s<7?4*s:((s-7)/3)*4+1+(s-7)%3; };
    f32x16 cQ={}; f32x4 z=LDS(f32x4,L+L_MISC+l16);
    sfor<0,28>([&](auto S){ constexpr int s=S.v;
      if constexpr(s<21){ constexpr int n=ord(s+7); fq[n]=tr2(aQ[n&3]+(n>>2)*4096,aQ[n&3]+(n>>2)*4096+256); }
      if constexpr(s==21) lsig(cnt+16);
      SB(); cQ=mfma(fq[ord(s)],xf[ord(s)],cQ); PINW(cQ); SB(); });
    WAITV(0); lsig(cnt+20);
    cQ=__builtin_amdgcn_mfma_f32_32x32x2f32((z[0]+z[1])+(z[2]+z[3]),nmean,cQ,0,0,0);
    f32x16 p=mfma(wp[0],pk8(cQ,0),f32x16{}); p=mfma(wp[1],pk8(cQ,8),p);
    dsoftmax(p,rstd*LOG2E,phq);
    PINW(phq[0]); PINW(phq[1]); PINW(phq[2]); PINW(phq[3]);
    STAMP(5);
    lwait(cnt+16,4);
    W2D(w*4096,0); W2D(w*4096+2048,1); W2D(tt*4096,-56); W2D(tt*4096+2048,-55);
    misc();
    STAMP(6);
    rdw1();
    STAMP(7);
  }
  BAR();
  STAMP(9);
  if(ty){ f32x16 c={};
#pragma unroll
    for(int n=0;n<4;n++) c=mfma(LDS(u32x4,L+L_KVX+n*1024+l16),phq[n],c);
    const float rd=__builtin_amdgcn_rcpf(swp_hi(c[12])+1e-6f);
#pragma unroll
    for(int r=0;r<16;r++) c[r]*=rd;
    ost(pk8(c,0),pk8(c,8));
  }
  STAMP(10);
  if(act){
  sfor<0,7>([&](auto U){ dma(L_WO+(w*7+U.v)*1024,vw2,r0,(w*7+U.v)*7168+cs*128); });
  W2D(L_R2+w*6144,2); W2D(L_R2+w*6144+2048,3); W2D(L_R2+w*6144+4096,4);
  float cb=0;
  if(ty){
#pragma unroll
    for(int n=14;n<28;n++) cb=sum8(w1f[n],cb);
  } else {
#pragma unroll
    for(int n=0;n<14;n++) cb=sum8(w1f[n],cb); }
  LDS(float,L+L_MISC+w*256+lane*4)=cb;
  STAMP(11);
  const rsrc_t rh=mk(ws+WS_H+i*114688,114688), rst=mk(ws+WS_ST+i*8192,8192);
  u32x4 hp;
  { const rsrc_t ro=mk(ws+WS_O+i*114688,114688); const u32 so=(2*th+tt2)*28672+kq*7168;
    asm volatile("s_waitcnt vmcnt(6)":"+v"(xr),"+v"(b1v)::"memory");
    BAR();
    lptr p0=a0+L_WO+kq*14336,p1=a1+L_WO+kq*14336; PINW(p0); PINW(p1);
    u32x4 fa[7][2]; f32x16 c[2];
#pragma unroll
    for(int n=0;n<7;n++){ fa[n][0]=tr2(p0+n*2048,p0+n*2048+512); fa[n][1]=tr2(p1+n*2048,p1+n*2048+512); }
    SB();
    await(lane<14?lane*512+496:OOB,ro,so);
    STAMP(12);
    u32x4 of[7];
    sfor<0,7>([&](auto N){ of[N.v]=ldc<(N.v&3)*1024>(l16,ro,so+(N.v>>2)*4096); });
    NOUNR for(int it=0;it<SPIN;it++){ u32 m=0; c[0]=f32x16{}; c[1]=f32x16{};
      sfor<0,7>([&](auto N){ constexpr int n=N.v; asm volatile("s_waitcnt vmcnt(%1)":"+v"(of[n]):"n"(6-n):"memory");
        c[0]=mfma(fa[n][0],of[n],c[0]); PINW(c[0]); c[1]=mfma(fa[n][1],of[n],c[1]); PINW(c[1]); m=mx4(m,of[n]); SB(); });
      if(!bad(m)) break;
      sfor<0,7>([&](auto N){ rld<(N.v&3)*1024>(of[N.v],l16,ro,so+(N.v>>2)*4096); }); WAITV(0); }
    SB();
    p0=a0+w*4096; p1=a1+w*4096;
#pragma unroll
    for(int n=0;n<2;n++){ w2f[n][0]=tr2(p0+n*2048,p0+n*2048+512); w2f[n][1]=tr2(p1+n*2048,p1+n*2048+512); }
    asm volatile("":::"memory");
    lptr e=L+w*4096+l16;
    const float cbo=LDS(float,L+L_MISC+(w^4)*256+lane*4);
    LDS(u32x4,e)=pk8(c[0],0); LDS(u32x4,e+1024)=pk8(c[0],8); LDS(u32x4,e+2048)=pk8(c[1],0); LDS(u32x4,e+3072)=pk8(c[1],8);
    cb=swp_add(cb+cbo); cb=hf?u2f(b1v<<16):cb; PINW(cb);
    STAMP(13);
    BAR();
    e=L+tt2*4096+vf;
    f32x4 s[4]={}; acc8(id4,xr,s[0],s[1]);
#pragma unroll
    for(int q=0;q<4;q++) acc8(id4,LDS(u32x4,e+q*8192),s[2*(q&1)],s[2*(q&1)+1]);
    s[0]+=s[2]; s[1]+=s[3];
    hp=pk44(s[0],s[1]);
    st16(hp,rh,vf,(2*th+tt2)*28672+cs*4096);
    float s1=((s[0][0]+s[0][1])+(s[0][2]+s[0][3]))+((s[1][0]+s[1][1])+(s[1][2]+s[1][3])), s2=s[0][0]*s[0][0], s3=s[1][0]*s[1][0];
#pragma unroll
    for(int q=1;q<4;q++){ s2=__builtin_fmaf(s[0][q],s[0][q],s2); s3=__builtin_fmaf(s[1][q],s[1][q],s3); }
    s1=red8(s1); s2=red8(s2+s3);
    asm volatile("buffer_store_dwordx2 %0, %1, %2, %3 offen sc1"::"v"(f32x2{s1,s2}),"v"(lane<8?lane*64:OOB),"s"(rst),"s"(((2*th+tt2)*32+kq*8)*64+cs*8):"memory");
    SB(); }
  STAMP(14);
  if(b==0&&tid==0) __hip_atomic_store((u32*)ws,tag,__ATOMIC_RELAXED,__HIP_MEMORY_SCOPE_AGENT);
  const u32 bX=ty?24576+tt*8192:118784+tt*10240, bY=ty?86016+tt*8192:bX+8192, bZ=ty?bY+2048:tt*6144;
  W2D(L_R3+w*2048,5);
  if(!ty){ W2D(bX,6); W2D(bX+2048,7); W2D(bX+4096,8); W2D(bX+6144,9); W2D(bX+8192,10); }
  { lptr p0=a0+L_R2+w*6144,p1=a1+L_R2+w*6144;
#pragma unroll
    for(int n=0;n<3;n++){ w2f[2+n][0]=tr2(p0+n*2048,p0+n*2048+512); w2f[2+n][1]=tr2(p1+n*2048,p1+n*2048+512); } }
  BAR();
  const u32 sH=ty*57344+tt*1024, lH0=ty*28672+tt*1024, lH1=(ty?57344:86016)+tt*1024;
  await(lane<14?lane*4096+1008:OOB,rh,sH);
  STAMP(15);
  { lptr p0=a0+L_R3+w*2048,p1=a1+L_R3+w*2048; PINW(p0); PINW(p1); w2f[5][0]=tr2(p0,p0+512); w2f[5][1]=tr2(p1,p1+512); }
  if(ty) __builtin_amdgcn_s_sleep(4);
  u32x4 hb[28],sa,sb; float sg_,K1,K2,Lg,bb; const u32 vst=t*64+hf*32;
  auto dmas=[&](u32 l,int k){ sfor<0,7>([&](auto U){ dmac(l+U.v*4096,l16,rh,sH+k*28672+U.v*4096); }); };
  dmas(lH0,0); sa=ldc<0>(vst,rst,ty*4096); sb=ldc<16>(vst,rst,ty*4096);
  auto valid=[&](u32 l,int k){ lptr p=L+l+l16; PINW(p);
    NOUNR for(int it=0;it<SPIN;it++){
#pragma unroll
      for(int n=0;n<7;n++) hb[4*n]=LDS(u32x4,p+n*4096);
      if(hf){ sb[2]=0; sb[3]=0; }
      u32 m=mx4(mx4(0,sa),sb);
#pragma unroll
      for(int n=0;n<7;n++) m=mx4(m,hb[4*n]);
      if(!bad(m)) break;
      dmas(l,k); rld<0>(sa,vst,rst,ty*4096+k*2048); rld<16>(sb,vst,rst,ty*4096+k*2048); asm volatile("s_waitcnt vmcnt(0)":"+v"(sa),"+v"(sb)::"memory"); }
    const float s1=swp_add((u2f(sa[0])+u2f(sa[2]))+(u2f(sb[0])+u2f(sb[2])))*(-1.f/448), s2=swp_add((u2f(sa[1])+u2f(sa[3]))+(u2f(sb[1])+u2f(sb[3])));
    sg_=__builtin_fmaf(__builtin_fmaxf(__builtin_fmaf(s2,1.f/448,-s1*s1),0.f),1.00001f,1e-10f);
    const float rs_=__builtin_amdgcn_rsqf(sg_); Lg=0.5f*__builtin_amdgcn_logf(sg_);
    sg_*=rs_; K1=-2.302208198f*rs_; K2=-0.1029432397f*rs_*rs_*rs_; bb=hf?sg_:s1; PINW(sg_); PINW(K1); PINW(K2); PINW(Lg); PINW(bb); };
  if(ty) BAR();
  asm volatile("s_waitcnt vmcnt(0)":"+v"(sa),"+v"(sb)::"memory"); valid(lH0,0);
  if(!ty) BAR();
  STAMP(16);
  dmas(lH1,1); sa=ldc<0>(vst,rst,ty*4096+2048); sb=ldc<16>(vst,rst,ty*4096+2048);
  if(ty) BAR();
  sfor<0,2>([&](auto KK){ constexpr int k=KK.v;
    lptr q[4];
#pragma unroll
    for(int u=1;u<4;u++){ q[u]=L+(k?lH1:lH0)+(((u+tt)&3)-tt)*1024+l16; PINW(q[u]); }
    f32x16 c={};
    auto ord=[](int s){ return s<7?4*s:((s-7)/3)*4+1+(s-7)%3; };
#define RDH(s) hb[ord(s)]=LDS(u32x4,q[ord(s)&3]+(ord(s)>>2)*4096)
    RDH(7); RDH(8); SB();
    sfor<0,28>([&](auto S){ constexpr int s=S.v;
      c=mfma(w1f[ord(s)],hb[ord(s)],c); PINW(c); SB();
      if constexpr(s+9<28) RDH(s+9);
      if constexpr(k==1&&s>=2&&s<10&&(s&1)==0){ if(ty) W2D(bY+(s-2)*1024,10+((s-2)>>1)); }
      SB(); });
    c=__builtin_amdgcn_mfma_f32_32x32x2f32(cb,bb,c,0,0,0); PINW(c);
    BAR();
    STAMP(17+2*k);
    u32 g[8];
#pragma unroll
    for(int p=0;p<8;p++){ float a=c[2*p],d=c[2*p+1];
      float u=__builtin_fmaf(a,__builtin_fmaf(a*a,K2,K1),Lg), v=__builtin_fmaf(d,__builtin_fmaf(d*d,K2,K1),Lg);
      u=__builtin_amdgcn_rcpf(__builtin_amdgcn_exp2f(u)+sg_); v=__builtin_amdgcn_rcpf(__builtin_amdgcn_exp2f(v)+sg_);
      g[p]=pk(a*u,d*v);
      if(p==3) st16(u32x4{g[0],g[1],g[2],g[3]},rhh,l16,(2*ty+k)*114688+(8*j+2*tt)*1024); }
    st16<1024>(u32x4{g[4],g[5],g[6],g[7]},rhh,l16,(2*ty+k)*114688+(8*j+2*tt)*1024);
    SB();
    if constexpr(k==0){ asm volatile("s_waitcnt vmcnt(2)":"+v"(sa),"+v"(sb)::"memory"); valid(lH1,1);
      if(ty){ W2D(bX,6); W2D(bX+2048,7); W2D(bX+4096,8); W2D(bX+6144,9); } else { W2D(bZ,11); W2D(bZ+2048,12); W2D(bZ+4096,13); }
      BAR(); STAMP(18); }
  });
  { const u32 s5=th*229376+w*14336, vs=lane<14?lane*1024+1008:OOB;
    u32x4 sn=ldc<0>(vs,rhh,s5);
    WAITV(3);
    { lptr p0=a0+bX,p1=a1+bX; PINW(p0); PINW(p1);
#pragma unroll
      for(int n=0;n<4;n++){ w2f[6+n][0]=tr2(p0+n*2048,p0+n*2048+512); w2f[6+n][1]=tr2(p1+n*2048,p1+n*2048+512); }
      p0=a0+bY; p1=a1+bY; PINW(p0); PINW(p1); w2f[10][0]=tr2(p0,p0+512); w2f[10][1]=tr2(p1,p1+512);
      p0=a0+bZ; p1=a1+bZ; PINW(p0); PINW(p1);
#pragma unroll
      for(int n=0;n<3;n++){ w2f[11+n][0]=tr2(p0+n*2048,p0+n*2048+512); w2f[11+n][1]=tr2(p1+n*2048,p1+n*2048+512); } }
    if(!ty) BAR();
    STAMP(20);
    u32x4 hh[14],q2; lptr e=L+bX+l16; PINW(e);
    sfor<0,2>([&](auto PP){ constexpr int p=PP.v; const u32 so=s5+p*114688;
      asm volatile("s_waitcnt vmcnt(0)":"+v"(sn)::"memory");
      if(bad(mx4(0,sn))) await(vs,rhh,so);
      STAMP(21+2*p);
      sfor<0,14>([&](auto N){ hh[N.v]=ldc<(N.v&3)*1024>(l16,rhh,so+(N.v>>2)*4096); });
      if constexpr(p==0) sn=ldc<0>(vs,rhh,so+114688); else q2=ldp(v8*16,mk(b2,896),cs*128);
      f32x16 c[2];
      NOUNR for(int it=0;it<SPIN;it++){ u32 m=0; c[0]=f32x16{}; c[1]=f32x16{};
        sfor<0,14>([&](auto N){ constexpr int n=N.v; asm volatile("s_waitcnt vmcnt(%1)":"+v"(hh[n]):"n"(14-n):"memory");
          c[0]=mfma(w2f[n][0],hh[n],c[0]); PINW(c[0]); c[1]=mfma(w2f[n][1],hh[n],c[1]); PINW(c[1]); m=mx4(m,hh[n]); SB(); });
        if(!bad(m)) break;
        sfor<0,14>([&](auto N){ rld<(N.v&3)*1024>(hh[N.v],l16,rhh,so+(N.v>>2)*4096); }); WAITV(0); }
      STAMP(22+2*p);
      LDS(u32x4,e+p*4096)=pk8(c[0],0); LDS(u32x4,e+p*4096+1024)=pk8(c[0],8); LDS(u32x4,e+p*4096+2048)=pk8(c[1],0); LDS(u32x4,e+p*4096+3072)=pk8(c[1],8); });
    asm volatile("s_waitcnt vmcnt(0)":"+v"(q2)::"memory");
    BAR();
    lptr e0=L+24576+tt2*4096+vf,e1=e0+(118784-24576); PINW(e1);
    f32x4 s[4]={}; acc8(id4,hp,s[0],s[1]); acc8(id4,q2,s[2],s[3]);
#pragma unroll
    for(int q=0;q<4;q++){ acc8(id4,LDS(u32x4,e0+q*8192),s[0],s[1]); acc8(id4,LDS(u32x4,e1+q*10240),s[2],s[3]); }
    __builtin_amdgcn_raw_buffer_store_b128(pk44(s[0]+s[2],s[1]+s[3]),mk(out+(size_t)i*128*448,128*896),vo,so_,0); }
  }
  STAMP(25);
}
}
#ifdef __HIP_DEVICE_COMPILE__
#pragma clang attribute pop
#endif
extern "C" void kernel_launch(void* const* d_in,const int* in_sizes,int n_in,void* d_out,int out_size,void* d_ws,size_t ws_size,hipStream_t stream){
  typedef const unsigned short* P;
  (void)hipFuncSetAttribute((const void*)fk::fused,hipFuncAttributeMaxDynamicSharedMemorySize,163840);
  fk::fused<<<256,512,163840,stream>>>((P)d_in[0],(P)d_in[1],(P)d_in[2],(P)d_in[3],(P)d_in[5],(P)d_in[6],(P)d_in[7],(P)d_in[8],(unsigned short*)d_out,(char*)d_ws);
}
```

```cpp
#define PROBE 1
#define PA 0
#define PB 1
#define PMUL 4
#define PBLK 37
#define PW 0
#include <hip/hip_runtime.h>
#include <stdint.h>
#ifdef __HIP_DEVICE_COMPILE__
#pragma clang attribute push(__attribute__((target("no-packed-fp32-ops"))),apply_to=function)
#endif
namespace fk {
typedef unsigned short u16; typedef unsigned u32;
typedef __attribute__((ext_vector_type(4))) u32 u32x4;
typedef __attribute__((ext_vector_type(2))) u32 u32x2;
typedef __attribute__((ext_vector_type(8))) __bf16 bf16x8;
typedef __attribute__((ext_vector_type(2))) __bf16 bf16x2;
typedef __attribute__((ext_vector_type(2))) float f32x2;
typedef __attribute__((ext_vector_type(4))) float f32x4;
typedef __attribute__((ext_vector_type(16))) float f32x16;
typedef short v4i16_t __attribute__((ext_vector_type(4)));
typedef __attribute__((address_space(3))) char* lptr;
typedef __amdgpu_buffer_rsrc_t rsrc_t;
#define DI __device__ __forceinline__
#define LDS(T,a) (*(__attribute__((address_space(3))) T*)(a))
#define STAMP(n) do{ if((n)==PA||(n)==PB){ uint64_t t_; asm volatile("s_memrealtime %0\n\ts_waitcnt lgkmcnt(0)":"=s"(t_)::"memory"); if((n)==PA) pa_=(u32)t_; if((n)==PB) pb_=(u32)t_; } }while(0)
#define CST(n) do{ if((n)==46||(n)==47){ uint64_t t_; asm volatile("s_memtime %0\n\ts_waitcnt lgkmcnt(0)":"=s"(t_)::"memory"); if((n)==46) ca_=(u32)t_; else cb_=(u32)t_; } }while(0)
constexpr u32 WS_FLAG=4096, WS_ST=512u<<10, WS_KV=1u<<20, WS_O=2u<<20, WS_H=4u<<20, WS_HH=8u<<20, OOB=0x80000000u;
constexpr int L_WK=98304, L_MISC=155648, L_CNT=L_MISC+2048, L_PF=L_CNT+64, L_KVX=159744, L_ST=159744;
constexpr int L_PHI=32768, L_VI=49152, L_WO=98304, L_R2=32768, L_R3=98304;
constexpr int w1l(int ks){ return ks<12?98304+ks*4096:ks<20?65536+(ks-12)*4096:32768+(ks-20)*4096; }
constexpr int xs(int c){ return (c%6==0?4:c%6==5?5:c%6-1)*16384; }
constexpr float LOG2E=1.4426950408889634f;
#define MAP 1
#define PF1C 9
#define PF2C 9
#define PZC 3
#define DEP 3
constexpr int xtra(int c){ return (c==PF1C?2:0)+(c==PZC?7:0)+(c==PF2C?3:0); }
constexpr int after(int c){ int n=c<DEP?(DEP-1-c)*3:xtra(c-DEP); for(int q=(c<DEP?0:c-DEP+1);q<c;q++) n+=(q+DEP<=6?3:0)+xtra(q); return n; }
constexpr int afterz(){ int n=(PF1C==PZC?2:0)+(PF2C==PZC?3:0); for(int q=PZC+1;q<7;q++) n+=(q+DEP<=6?3:0)+xtra(q); return n; }
#define NOUNR _Pragma("clang loop unroll(disable)")
DI float u2f(u32 v){ return __builtin_bit_cast(float,v); }
DI u32 f2u(float v){ return __builtin_bit_cast(u32,v); }
template<int V> struct IC{ static constexpr int v=V; };
template<int I,int N,class F> DI void sfor(F&& f){ if constexpr(I<N){ f(IC<I>{}); sfor<I+1,N>(f); } }
DI rsrc_t mk(const void* p,u32 bytes){ return __builtin_amdgcn_make_buffer_rsrc((void*)p,0,bytes,0x00020000); }
DI void dma(u32 l,u32 voff,rsrc_t r,u32 soff){ asm volatile("s_mov_b32 m0, %0\n\ts_nop 0\n\tbuffer_load_dwordx4 %1, %2, %3 offen lds"::"s"(l),"v"(voff),"s"(r),"s"(soff):"memory"); }
#define WAITV(n) asm volatile("s_waitcnt vmcnt(" #n ")":::"memory")
#define BAR() asm volatile("s_waitcnt lgkmcnt(0)\n\ts_barrier":::"memory")
#define PIN(x) asm volatile(""::"v"(x))
#define PINW(x) asm volatile("":"+v"(x))
#define SB() __builtin_amdgcn_sched_barrier(0)
DI f32x16 mfma(u32x4 a,u32x4 b,f32x16 c){ return __builtin_amdgcn_mfma_f32_32x32x16_bf16(__builtin_bit_cast(bf16x8,a),__builtin_bit_cast(bf16x8,b),c,0,0,0); }
DI f32x4 m44(u32x2 a,u32x2 b,f32x4 c){ return __builtin_amdgcn_mfma_f32_4x4x4bf16_1k(__builtin_bit_cast(v4i16_t,a),__builtin_bit_cast(v4i16_t,b),c,0,0,0); }
DI u32x2 tr(lptr p){ return __builtin_bit_cast(u32x2,__builtin_amdgcn_ds_read_tr16_b64_v4i16((__attribute__((address_space(3))) v4i16_t*)p)); }
DI u32x4 tr2(lptr p0,lptr p1){ u32x2 a=tr(p0),b=tr(p1); return u32x4{a[0],a[1],b[0],b[1]}; }
DI u32 pk(float lo,float hi){ return __builtin_bit_cast(u32,__builtin_convertvector(f32x2{lo,hi},bf16x2)); }
DI float dot2(u32 a,u32 b,float c){ return __builtin_amdgcn_fdot2_f32_bf16(__builtin_bit_cast(bf16x2,a),__builtin_bit_cast(bf16x2,b),c,false); }
DI float swp_lo(float v){ auto r=__builtin_amdgcn_permlane32_swap(f2u(v),f2u(v),false,false); return u2f(r[0]); }
DI float swp_hi(float v){ auto r=__builtin_amdgcn_permlane32_swap(f2u(v),f2u(v),false,false); return u2f(r[1]); }
DI float swp_add(float v){ auto r=__builtin_amdgcn_permlane32_swap(f2u(v),f2u(v),false,false); return u2f(r[0])+u2f(r[1]); }
DI float swp_max(float v){ auto r=__builtin_amdgcn_permlane32_swap(f2u(v),f2u(v),false,false); return __builtin_fmaxf(u2f(r[0]),u2f(r[1])); }
DI float swp_min(float v){ auto r=__builtin_amdgcn_permlane32_swap(f2u(v),f2u(v),false,false); return __builtin_fminf(u2f(r[0]),u2f(r[1])); }
DI float red8(float v){ v+=u2f(__builtin_amdgcn_update_dpp(0u,f2u(v),0x128,0xf,0xf,false)); auto r=__builtin_amdgcn_permlane16_swap(f2u(v),f2u(v),false,false); return swp_add(u2f(r[0])+u2f(r[1])); }
DI u32x4 pk8(const f32x16& c,int b){ return u32x4{pk(c[b],c[b+1]),pk(c[b+2],c[b+3]),pk(c[b+4],c[b+5]),pk(c[b+6],c[b+7])}; }
DI void dsoftmax(const f32x16& p,float c,u32x4* ph){
  float mx=p[0],mn=p[0];
#pragma unroll
  for(int r=1;r<14;r++){ mx=__builtin_fmaxf(mx,p[r]); mn=__builtin_fminf(mn,p[r]); }
  mx=swp_max(mx); mn=swp_min(mn);
  const float a=-mx*c,b=mn*c; f32x16 e,g; float s=0,u=0;
#pragma unroll
  for(int r=0;r<14;r++){ e[r]=__builtin_amdgcn_exp2f(__builtin_fmaf(p[r],c,a)); g[r]=__builtin_amdgcn_exp2f(__builtin_fmaf(p[r],-c,b)); s+=e[r]; u+=g[r]; }
  s=__builtin_amdgcn_rcpf(swp_add(s)); u=__builtin_amdgcn_rcpf(swp_add(u));
#pragma unroll
  for(int r=0;r<14;r++){ e[r]*=s; g[r]*=u; }
  ph[0]=pk8(e,0); ph[1]=u32x4{pk(e[8],e[9]),pk(e[10],e[11]),pk(e[12],e[13]),0u};
  ph[2]=pk8(g,0); ph[3]=u32x4{pk(g[8],g[9]),pk(g[10],g[11]),pk(g[12],g[13]),0u};
}
DI void dmac(u32 l,u32 voff,rsrc_t r,u32 soff){ asm volatile("s_mov_b32 m0, %0\n\ts_nop 0\n\tbuffer_load_dwordx4 %1, %2, %3 offen sc1 lds"::"s"(l),"v"(voff),"s"(r),"s"(soff):"memory"); }
DI void pfl(u32 voff,rsrc_t r,u32 soff){ asm volatile("s_mov_b32 m0, %0\n\ts_nop 0\n\tbuffer_load_dword %1, %2, %3 offen lds"::"s"(L_PF),"v"(voff),"s"(r),"s"(soff):"memory"); }
template<int O> DI void rld(u32x4& v,u32 voff,rsrc_t r,u32 soff){ asm volatile("buffer_load_dwordx4 %0, %1, %2, %3 offen offset:%4 sc1":"+v"(v):"v"(voff),"s"(r),"s"(soff),"n"(O):"memory"); }
template<int O> DI u32x4 ldc(u32 voff,rsrc_t r,u32 soff){ u32x4 v; asm volatile("buffer_load_dwordx4 %0, %1, %2, %3 offen offset:%4 sc1":"=v"(v):"v"(voff),"s"(r),"s"(soff),"n"(O):"memory"); return v; }
DI u32x4 ldp(u32 voff,rsrc_t r,u32 soff){ u32x4 v; asm volatile("buffer_load_dwordx4 %0, %1, %2, %3 offen":"=v"(v):"v"(voff),"s"(r),"s"(soff):"memory"); return v; }
template<int N> DI void waitv(){ asm volatile("s_waitcnt vmcnt(%0)"::"n"(N):"memory"); }
template<int O=0> DI void st16(u32x4 v,rsrc_t r,u32 voff,u32 soff){ asm volatile("buffer_store_dwordx4 %0, %1, %2, %3 offen offset:%4 sc1\n\ts_nop 1"::"v"(v),"v"(voff),"s"(r),"s"(soff),"n"(O):"memory"); }
DI u32 mx3(u32 a,u32 b,u32 c){ u32 r; asm("v_max3_u32 %0, %1, %2, %3":"=v"(r):"v"(a),"v"(b),"v"(c)); return r; }
DI u32 mx4(u32 m,u32x4 g){ return mx3(mx3(m,g[0],g[1]),g[2],g[3]); }
DI bool bad(u32 m){ return __builtin_amdgcn_ballot_w64(m==0xFFFFFFFFu)!=0; }
constexpr int SPIN=30000;
DI void await(u32 voff,rsrc_t r,u32 soff){ NOUNR for(int it=0;it<SPIN;it++){ u32x4 v=ldc<0>(voff,r,soff); asm volatile("s_waitcnt vmcnt(0)":"+v"(v)::"memory"); if(!bad(mx3(mx3(v[0],v[1],v[2]),v[3],v[3]))) break; } }
DI void lsig(lptr p){ asm volatile("s_mov_b64 exec, 1\n\tds_add_u32 %0, %1\n\ts_mov_b64 exec, -1"::"v"(p),"v"(1u):"memory"); }
DI void lwait(lptr p,u32 n){ NOUNR for(int it=0;it<SPIN*8;it++){ u32 v; asm volatile("ds_read_b32 %0, %1\n\ts_waitcnt lgkmcnt(0)":"=v"(v):"v"(p):"memory"); if(__builtin_amdgcn_readfirstlane(v)>=n) break; } }
DI float sum8(u32x4 f,float c){ c=dot2(f[0],0x3F803F80u,c); c=dot2(f[1],0x3F803F80u,c); c=dot2(f[2],0x3F803F80u,c); return dot2(f[3],0x3F803F80u,c); }
DI float sq8(u32x4 f,float c){ c=dot2(f[0],f[0],c); c=dot2(f[1],f[1],c); c=dot2(f[2],f[2],c); return dot2(f[3],f[3],c); }
DI void acc8(u32x2 id4,u32x4 g,f32x4& a,f32x4& b){ a=m44(id4,u32x2{g[0],g[1]},a); b=m44(id4,u32x2{g[2],g[3]},b); }
DI u32x4 pk44(f32x4 a,f32x4 b){ return u32x4{pk(a[0],a[1]),pk(a[2],a[3]),pk(b[0],b[1]),pk(b[2],b[3])}; }
#define PRIO(n) __builtin_amdgcn_s_setprio(n)

__global__ __launch_bounds__(512,2) void fused(const u16* __restrict__ x,const u16* __restrict__ wqkv,const u16* __restrict__ wphi,const u16* __restrict__ wo,
    const u16* __restrict__ w1,const u16* __restrict__ b1,const u16* __restrict__ w2,const u16* __restrict__ b2,u16* __restrict__ out,char* __restrict__ ws
    ){
  extern __shared__ __attribute__((aligned(16))) char smem[];
  const lptr L=(lptr)smem;
  const int tid=__builtin_amdgcn_workitem_id_x(),lane=tid&63,w=__builtin_amdgcn_readfirstlane(tid>>6),tt=w&3,ty=w>>2,t=lane&31,hf=lane>>5;
  const int b=__builtin_amdgcn_workgroup_id_x(),i=(b&7)*2+((b>>3)&1),j=b>>4;
  const int i1=MAP?j:i,j1=MAP?i:j,odd=j1&1;
  u32 pa_=0,pb_=0;
  STAMP(0);
  asm volatile(""::"s"(x),"s"(wqkv),"s"(wphi),"s"(wo),"s"(w1),"s"(b1),"s"(w2),"s"(b2),"s"(out),"s"(ws));
  const u32 epoch=*(const __attribute__((address_space(4))) u32*)ws;
  const rsrc_t rw=mk(wqkv,448*2688), rx=mk(x+(size_t)i1*128*448,128*896);
  const u32 vw=(lane>>2)*2688+(lane&3)*16, hoff=56*j1-8*odd+tt*16*2688, sW=hoff+(ty+1)*896, lW=L_WK+ty*28672+tt*1024;
  const u32 vx0=(lane>>3)*896+(((lane&7)^(lane>>4))<<4), vx1=vx0^64, sX=(tt*32+ty*16)*896, lX=tt*4096+ty*2048;
#define CH(c) do{ dma(lW+(c)*4096,vw,rw,sW+(c)*64*2688); dma(xs(c)+lX,vx0,rx,sX+(c)*128); dma(xs(c)+lX+1024,vx1,rx,sX+7168+(c)*128); }while(0)
#define SBAR() asm volatile("s_barrier":::"memory")
  if(w<2) dma(L_MISC+w*1024,tid*16,mk(wphi+j1*784,1568),0);
  CH(0); sfor<1,DEP>([&](auto C){ SBAR(); CH(C.v); });
  if(tid<2) LDS(u32x4,L+L_CNT+tid*16)=u32x4{0,0,0,0};
  const bool act=j<14; const int th=j>=7, cs=j-7*th, tt2=w&1, kq=w>>1;
  const int q4=(lane&15)>>2,mu=((lane>>4)&1)*4+(lane&3),sg=4*(mu&1)+(mu>>1);
  const int rowo=(8*hf+q4)*64, kslot=(mu+odd)&7;
  lptr aW[4],aQ[4],xb[4];
  { const int sw=hf^((t>>1)&7), sl=ty?(odd?(sg==7?0:((sg+1)%7)+1):sg):kslot;
#pragma unroll
    for(int u=0;u<4;u++){ const int r=(u+tt)&3; aW[u]=L+L_WK+ty*28672+rowo+sl*8+r*1024; aQ[u]=L+rowo+kslot*8+r*1024; xb[u]=L+(tt*32+t)*128+((sw^(r*2))<<4); PINW(aW[u]); PINW(xb[u]); } }
  const lptr pp=L+lW+(lane&15)*64+(odd?0:56);
  u32 tag=epoch*0x9E3779B1u+0x7F4A7C15u; asm volatile("":"+s"(tag));
  const rsrc_t ro1=mk(ws+WS_O+i1*114688,114688), r1=mk(w1,act?448*3584:0), r2=mk(w2,act?1792*896:0), r0=mk(wo,act?448*896:0);
  const u32 l16=lane*16;
  const rsrc_t rkv=mk(ws+WS_KV+j1*65536,65536);
  const bool duty=((b>>3)&1)==(((b&7)-1u)<3u); const u32 dq=(b>>4)*8+w;
  auto ost=[&](u32x4 o0,u32x4 o1){ const u32 ub=t*16+((7*j1)>>1)*512, so=tt*28672;
    st16(o0,ro1,ub+(odd+2*hf)*512,so); st16(o1,ro1,hf?OOB:ub+(odd+1)*512,so);
    __builtin_amdgcn_raw_buffer_store_b64(u32x2{o1[0],o1[1]},ro1,hf?ub+(odd?8:1536):OOB,so,16); };
  u32x4 PZ={~0u,~0u,~0u,~0u}; PINW(PZ);
  const u32x2 pv={0x3F80u,0u};
  const lptr cnt=L+L_CNT;
  u32x4 xf[28],wp[2],fw[2][4]; f32x16 c1={}; float ss=0,cz=0;
  auto mm=[&](auto C){ constexpr int c=C.v;
#pragma unroll
    for(int u=0;u<4;u++){ c1=mfma(fw[c&1][u],xf[4*c+u],c1); PINW(c1); }
    SB(); };
  auto dd=[&](auto C){ constexpr int c=C.v; cz=sum8(fw[c&1][0],cz);
    if(ty){ ss=sq8(xf[4*c+2],ss); ss=sq8(xf[4*c+3],ss); } else { ss=sq8(xf[4*c],ss); ss=sq8(xf[4*c+1],ss); }
    PINW(ss); PINW(cz); SB(); };
  sfor<0,7>([&](auto C){ constexpr int c=C.v;
    waitv<after(c)>();
    asm volatile("s_mov_b64 exec, 0xffff\n\tds_write_b64 %0, %1 offset:%2\n\ts_mov_b64 exec, -1"::"v"(pp),"v"(pv),"i"(c*4096):"memory");
    BAR();
    if constexpr(c==0) STAMP(1); else STAMP(25+c);
    if constexpr(c==6){ if(!ty) PRIO(2); }
#pragma unroll
    for(int u=0;u<4;u++){ fw[c&1][u]=tr2(aW[u]+c*4096,aW[u]+c*4096+256); xf[4*c+u]=LDS(u32x4,xb[u]+xs(c)); }
    SB();
    if constexpr(c+DEP<=6) CH(c+DEP);
    if constexpr(c==PZC){
      if(ty){ sfor<0,7>([&](auto U){ dma(tt*1024+U.v*4096,vw,rw,hoff+U.v*64*2688); }); }
      else { st16(PZ,rkv,l16,i1*4096+tt*1024); ost(PZ,PZ); const int t1=j1>=7; const rsrc_t rp=mk(ws+WS_H+i1*114688,j1<14?114688:0); const u32 so=(2*t1+(tt&1))*28672+(4*(j1-7*t1)+(tt>>1))*1024; st16(PZ,rp,l16,so); st16<2048>(PZ,rp,l16,so);
        st16(PZ,mk(ws+WS_ST+i1*8192,8192),lane<8?l16:OOB,(j1*4+tt)*128); } }
    if constexpr(c==PF1C){ const rsrc_t r=mk(w1,duty?448*3584:0); pfl(lane*128,r,dq*12544); pfl(lane<34?lane*128+8192:OOB,r,dq*12544); }
    if constexpr(c==PF2C){ pfl(lane<25?lane*128:OOB,mk(wo,duty?448*896:0),dq*3136); const rsrc_t r=mk(w2,duty?1792*896:0); pfl(lane*128,r,dq*12544); pfl(lane<34?lane*128+8192:OOB,r,dq*12544); }
    SB();
    if constexpr(c>0){ mm(IC<c-1>{}); dd(IC<c-1>{}); }
    if constexpr(c==0){
      const bool mv=(t&26)!=26; const lptr p=L+L_MISC+(t<26?t:t-2)*2+hf*224; u32 e[16];
#pragma unroll
      for(int q=0;q<16;q++){ const int d=16*(q>>3)+8*((q>>2)&1)+(q&3); e[q]=LDS(u16,p+d*56); }
#pragma unroll
      for(int q=0;q<8;q++){ u32 v=e[2*q]|(e[2*q+1]<<16); v=(mv&&(q<6||hf==0))?v:0u; asm volatile("":"+v"(v)); wp[q>>2][q&3]=v; } }
  });
  dd(IC<6>{});
  LDS(float,L+L_KVX+w*256+lane*4)=ss; LDS(float,L+L_KVX+2048+ty*1024+l16+tt*4)=cz;
  SB(); mm(IC<6>{});
  BAR();
  { f32x4 z=LDS(f32x4,L+L_KVX+2048+ty*1024+l16); ss+=LDS(float,L+L_KVX+(w^4)*256+lane*4); cz=(z[0]+z[1])+(z[2]+z[3]); }
  STAMP(2);
  const float nmean=swp_hi(c1[12])*(-1.f/448);
  c1=__builtin_amdgcn_mfma_f32_32x32x2f32(cz,nmean,c1,0,0,0);
  const float rstd=__builtin_amdgcn_rsqf(__builtin_fmaxf(__builtin_fmaf(swp_add(ss),1.f/448,-nmean*nmean),0.f)+1e-5f);
  const int wro=(tt*4096+t*64),c0=((2*hf)^((t>>2)&3))<<4;
  const rsrc_t rhh=mk(ws+WS_HH+i*458752,act?458752:0);
  const u32 v8=(lane>>3), p8=((lane&7)^(lane>>2&4))<<4, vw2=v8*896+p8, sW2=w*224*896+cs*128;
#define W2D(l,n) do{ dma((l),vw2,r2,sW2+(n)*14336); dma((l)+1024,vw2,r2,sW2+(n)*14336+7168); }while(0)
  const int nq=(mu&4)|((mu&1)<<1)|((mu>>1)&1);
  const int ao=(8*hf+q4)*128+(q4>>1)*64+nq*8; const lptr a0=L+ao,a1=L+(ao^64);
  const u32x2 id4={(lane&3)==0?0x3F80u:(lane&3)==1?0x3F800000u:0u,(lane&3)==2?0x3F80u:(lane&3)==3?0x3F800000u:0u};
  const u32 vo=(lane&7)*896+v8*16, so_=((2*th+tt2)*32+kq*8)*896+cs*128, vf=v8*512+(lane&7)*16+kq*128;
  const u32 vw1=v8*3584+p8, sW1=(tt&1)*8*3584+j*256+(tt>>1)*128;
  u32x4 phq[4],w1f[28],w2f[14][2],xr; u32 b1v;
  auto misc=[&]{ const u32 sh=ty*229376+(8*j+2*tt)*1024;
    st16(PZ,rhh,l16,sh); st16<1024>(PZ,rhh,l16,sh); st16(PZ,rhh,l16,sh+114688); st16<1024>(PZ,rhh,l16,sh+114688);
    xr=ldp(vo,mk(x+(size_t)i*128*448,128*896),so_);
    const int tq=t>>2; asm volatile("buffer_load_ushort %0, %1, %2, %3 offen":"=v"(b1v):"v"((((tq&4)|((tq&1)<<1)|((tq>>1)&1))*4+(t&3))*2),"s"(mk(b1,act?3584:0)),"s"(j*256+tt*64):"memory"); };
  auto rdw1=[&]{ lwait(cnt+20,8); lptr q[4];
#pragma unroll
    for(int u=0;u<4;u++){ q[u]=L+65536+((tt&1)?ao^64:ao)+(tt>>1)*2048+((u+tt)&3)*4096; PINW(q[u]); }
#pragma unroll
    for(int n=12;n<20;n++) w1f[n]=tr2(q[n&3]+(w1l(n&~3)-65536),q[n&3]+(w1l(n&~3)-65536)+512);
#pragma unroll
    for(int n=0;n<8;n++) w1f[n]=tr2(q[n&3]+(w1l(n&~3)-65536),q[n&3]+(w1l(n&~3)-65536)+512);
#pragma unroll
    for(int u=0;u<4;u++){ q[u]+=65536; PINW(q[u]); }
#pragma unroll
    for(int n=8;n<12;n++) w1f[n]=tr2(q[n&3],q[n&3]+512);
#pragma unroll
    for(int u=0;u<4;u++){ q[u]-=98304; PINW(q[u]); }
#pragma unroll
    for(int n=20;n<28;n++) w1f[n]=tr2(q[n&3]+(w1l(n&~3)-32768),q[n&3]+(w1l(n&~3)-32768)+512); };
  if(ty==0){
    f32x16 p=mfma(wp[0],pk8(c1,0),f32x16{}); p=mfma(wp[1],pk8(c1,8),p);
    u32x4 ph[4]; dsoftmax(p,rstd*LOG2E,ph);
    LDS(u32x4,L+L_PHI+wro+c0)=ph[0]; LDS(u32x4,L+L_PHI+wro+(c0^16))=ph[1];
    LDS(u32x4,L+L_PHI+2048+wro+c0)=ph[2]; LDS(u32x4,L+L_PHI+2048+wro+(c0^16))=ph[3];
    waitv<afterz()>(); lsig(cnt);
    STAMP(3);
    lwait(cnt,8);
    if(tt<2){
      const int ko=(8*hf+q4)*64+(sg&1)*8; const lptr k0=L+ko+(((sg>>1)^(2*hf))<<4),k1=L+ko+256+(((sg>>1)^(2*hf+1))<<4);
      f32x16 c={};
#pragma unroll
      for(int u=0;u<8;u++){ const int o=(u>>1)*4096+(u&1)*1024; c=mfma(tr2(k0+L_PHI+tt*2048+o,k1+L_PHI+tt*2048+o),tr2(k0+L_VI+o,k1+L_VI+o),c); }
      st16(pk8(c,0),rkv,l16,i1*4096+tt*2048); st16<1024>(pk8(c,8),rkv,l16,i1*4096+tt*2048);
      lsig(cnt+8);
      STAMP(4);
    } else if(tid==128) __hip_atomic_store((u32*)(ws+WS_FLAG+(j1*16+i1)*128),tag,__ATOMIC_RELAXED,__HIP_MEMORY_SCOPE_AGENT);
    lwait(cnt+8,2);
    STAMP(5);
    sfor<20,28>([&](auto K){ dma(w1l(K.v)+tt*1024,vw1,r1,sW1+K.v*16*3584); });
    STAMP(6);
    { const rsrc_t rf=mk(ws,2u<<20); const u32 n=lane&15, vp=lane<16?WS_FLAG+(j1*16+n)*128:lane<32?WS_FLAG+(n*16+i)*128:lane<48?WS_KV+j1*65536+n*4096+tt*1024+1020:OOB;
      NOUNR for(int it=0;it<SPIN;it++){ u32 v; asm volatile("buffer_load_dword %0, %1, %2, 0 offen sc1\n\ts_waitcnt vmcnt(0)":"=v"(v):"v"(vp),"s"(rf):"memory");
        if(__builtin_amdgcn_ballot_w64(lane<32?v!=tag:v==~0u)==0) break; }
      lsig(cnt+20); }
    STAMP(8);
    u32x4 g[16];
    sfor<0,16>([&](auto N){ g[N.v]=ldc<0>(l16,rkv,((N.v+i1)&15)*4096+tt*1024); });
    misc();
    rdw1(); SB();
    STAMP(7);
    f32x4 s[4];
    NOUNR for(int it=0;it<SPIN;it++){ u32 m=0; s[0]=s[1]=s[2]=s[3]=f32x4{};
      sfor<0,8>([&](auto N){ constexpr int n=N.v; asm volatile("s_waitcnt vmcnt(%2)":"+v"(g[2*n]),"+v"(g[2*n+1]):"n"(20-2*n):"memory"); acc8(id4,g[2*n],s[0],s[1]); acc8(id4,g[2*n+1],s[2],s[3]); m=mx4(mx4(m,g[2*n]),g[2*n+1]); });
      if(!bad(m)) break;
      sfor<0,16>([&](auto N){ rld<0>(g[N.v],l16,rkv,((N.v+i1)&15)*4096+tt*1024); }); WAITV(0); }
    LDS(u32x4,L+L_KVX+tt*1024+l16)=pk44(s[0]+s[2],s[1]+s[3]);
    PRIO(0);
  } else {
#pragma unroll
    for(int r=0;r<16;r++) c1[r]*=rstd;
    u32x4 v0=pk8(c1,0),v1=pk8(c1,8); if(hf){ v1[2]=0x3F80u; v1[3]=0u; }
    LDS(u32x4,L+L_VI+wro+c0)=v0; LDS(u32x4,L+L_VI+wro+(c0^16))=v1;
    lsig(cnt);
    STAMP(3);
    sfor<0,20>([&](auto K){ dma(w1l(K.v)+tt*1024,vw1,r1,sW1+K.v*16*3584); });
    waitv<afterz()+20>();
    u32x4 fq[28]; float cq=0;
#pragma unroll
    for(int u=0;u<7;u++) fq[4*u]=tr2(aQ[0]+u*4096,aQ[0]+u*4096+256);
#pragma unroll
    for(int u=0;u<7;u++) cq=sum8(fq[4*u],cq);
    LDS(float,L+L_MISC+l16+tt*4)=cq;
    lsig(cnt+4); lwait(cnt+4,4);
    STAMP(4);
    auto ord=[](int s){ return s<7?4*s:((s-7)/3)*4+1+(s-7)%3; };
    f32x16 cQ={}; f32x4 z=LDS(f32x4,L+L_MISC+l16);
    sfor<0,28>([&](auto S){ constexpr int s=S.v;
      if constexpr(s<21){ constexpr int n=ord(s+7); fq[n]=tr2(aQ[n&3]+(n>>2)*4096,aQ[n&3]+(n>>2)*4096+256); }
      if constexpr(s==21) lsig(cnt+16);
      SB(); cQ=mfma(fq[ord(s)],xf[ord(s)],cQ); PINW(cQ); SB(); });
    WAITV(0); lsig(cnt+20);
    cQ=__builtin_amdgcn_mfma_f32_32x32x2f32((z[0]+z[1])+(z[2]+z[3]),nmean,cQ,0,0,0);
    f32x16 p=mfma(wp[0],pk8(cQ,0),f32x16{}); p=mfma(wp[1],pk8(cQ,8),p);
    dsoftmax(p,rstd*LOG2E,phq);
    PINW(phq[0]); PINW(phq[1]); PINW(phq[2]); PINW(phq[3]);
    STAMP(5);
    lwait(cnt+16,4);
    W2D(w*4096,0); W2D(w*4096+2048,1); W2D(tt*4096,-56); W2D(tt*4096+2048,-55);
    misc();
    STAMP(6);
    rdw1();
    STAMP(7);
  }
  BAR();
  STAMP(9);
  if(ty){ f32x16 c={};
#pragma unroll
    for(int n=0;n<4;n++) c=mfma(LDS(u32x4,L+L_KVX+n*1024+l16),phq[n],c);
    const float rd=__builtin_amdgcn_rcpf(swp_hi(c[12])+1e-6f);
#pragma unroll
    for(int r=0;r<16;r++) c[r]*=rd;
    ost(pk8(c,0),pk8(c,8));
  }
  STAMP(10);
  if(act){
  sfor<0,7>([&](auto U){ dma(L_WO+(w*7+U.v)*1024,vw2,r0,(w*7+U.v)*7168+cs*128); });
  W2D(L_R2+w*6144,2); W2D(L_R2+w*6144+2048,3); W2D(L_R2+w*6144+4096,4);
  float cb=0;
  if(ty){
#pragma unroll
    for(int n=14;n<28;n++) cb=sum8(w1f[n],cb);
  } else {
#pragma unroll
    for(int n=0;n<14;n++) cb=sum8(w1f[n],cb); }
  LDS(float,L+L_MISC+w*256+lane*4)=cb;
  STAMP(11);
  const rsrc_t rh=mk(ws+WS_H+i*114688,114688), rst=mk(ws+WS_ST+i*8192,8192);
  u32x4 hp;
  { const rsrc_t ro=mk(ws+WS_O+i*114688,114688); const u32 so=(2*th+tt2)*28672+kq*7168;
    asm volatile("s_waitcnt vmcnt(6)":"+v"(xr),"+v"(b1v)::"memory");
    BAR();
    lptr p0=a0+L_WO+kq*14336,p1=a1+L_WO+kq*14336; PINW(p0); PINW(p1);
    u32x4 fa[7][2]; f32x16 c[2];
#pragma unroll
    for(int n=0;n<7;n++){ fa[n][0]=tr2(p0+n*2048,p0+n*2048+512); fa[n][1]=tr2(p1+n*2048,p1+n*2048+512); }
    SB();
    await(lane<14?lane*512+496:OOB,ro,so);
    STAMP(12);
    u32x4 of[7];
    sfor<0,7>([&](auto N){ of[N.v]=ldc<(N.v&3)*1024>(l16,ro,so+(N.v>>2)*4096); });
    NOUNR for(int it=0;it<SPIN;it++){ u32 m=0; c[0]=f32x16{}; c[1]=f32x16{};
      sfor<0,7>([&](auto N){ constexpr int n=N.v; asm volatile("s_waitcnt vmcnt(%1)":"+v"(of[n]):"n"(6-n):"memory");
        c[0]=mfma(fa[n][0],of[n],c[0]); PINW(c[0]); c[1]=mfma(fa[n][1],of[n],c[1]); PINW(c[1]); m=mx4(m,of[n]); SB(); });
      if(!bad(m)) break;
      sfor<0,7>([&](auto N){ rld<(N.v&3)*1024>(of[N.v],l16,ro,so+(N.v>>2)*4096); }); WAITV(0); }
    SB();
    p0=a0+w*4096; p1=a1+w*4096;
#pragma unroll
    for(int n=0;n<2;n++){ w2f[n][0]=tr2(p0+n*2048,p0+n*2048+512); w2f[n][1]=tr2(p1+n*2048,p1+n*2048+512); }
    asm volatile("":::"memory");
    lptr e=L+w*4096+l16;
    const float cbo=LDS(float,L+L_MISC+(w^4)*256+lane*4);
    LDS(u32x4,e)=pk8(c[0],0); LDS(u32x4,e+1024)=pk8(c[0],8); LDS(u32x4,e+2048)=pk8(c[1],0); LDS(u32x4,e+3072)=pk8(c[1],8);
    cb=swp_add(cb+cbo); cb=hf?u2f(b1v<<16):cb; PINW(cb);
    STAMP(13);
    BAR();
    e=L+tt2*4096+vf;
    f32x4 s[4]={}; acc8(id4,xr,s[0],s[1]);
#pragma unroll
    for(int q=0;q<4;q++) acc8(id4,LDS(u32x4,e+q*8192),s[2*(q&1)],s[2*(q&1)+1]);
    s[0]+=s[2]; s[1]+=s[3];
    hp=pk44(s[0],s[1]);
    st16(hp,rh,vf,(2*th+tt2)*28672+cs*4096);
    float s1=((s[0][0]+s[0][1])+(s[0][2]+s[0][3]))+((s[1][0]+s[1][1])+(s[1][2]+s[1][3])), s2=s[0][0]*s[0][0], s3=s[1][0]*s[1][0];
#pragma unroll
    for(int q=1;q<4;q++){ s2=__builtin_fmaf(s[0][q],s[0][q],s2); s3=__builtin_fmaf(s[1][q],s[1][q],s3); }
    s1=red8(s1); s2=red8(s2+s3);
    asm volatile("buffer_store_dwordx2 %0, %1, %2, %3 offen sc1"::"v"(f32x2{s1,s2}),"v"(lane<8?lane*64:OOB),"s"(rst),"s"(((2*th+tt2)*32+kq*8)*64+cs*8):"memory");
    SB(); }
  STAMP(14);
  if(b==0&&tid==0) __hip_atomic_store((u32*)ws,tag,__ATOMIC_RELAXED,__HIP_MEMORY_SCOPE_AGENT);
  const u32 bX=ty?24576+tt*8192:118784+tt*10240, bY=ty?86016+tt*8192:bX+8192, bZ=ty?bY+2048:tt*6144;
  W2D(L_R3+w*2048,5);
  if(!ty){ W2D(bX,6); W2D(bX+2048,7); W2D(bX+4096,8); W2D(bX+6144,9); W2D(bX+8192,10); }
  { lptr p0=a0+L_R2+w*6144,p1=a1+L_R2+w*6144;
#pragma unroll
    for(int n=0;n<3;n++){ w2f[2+n][0]=tr2(p0+n*2048,p0+n*2048+512); w2f[2+n][1]=tr2(p1+n*2048,p1+n*2048+512); } }
  BAR();
  const u32 sH=ty*57344+tt*1024, lH0=ty*28672+tt*1024, lH1=(ty?57344:86016)+tt*1024;
  await(lane<14?lane*4096+1008:OOB,rh,sH);
  STAMP(15);
  { lptr p0=a0+L_R3+w*2048,p1=a1+L_R3+w*2048; PINW(p0); PINW(p1); w2f[5][0]=tr2(p0,p0+512); w2f[5][1]=tr2(p1,p1+512); }
  if(ty) __builtin_amdgcn_s_sleep(4);
  u32x4 hb[28],sa,sb; float sg_,K1,K2,Lg,bb; const u32 vst=t*64+hf*32;
  auto dmas=[&](u32 l,int k){ sfor<0,7>([&](auto U){ dmac(l+U.v*4096,l16,rh,sH+k*28672+U.v*4096); }); };
  dmas(lH0,0); sa=ldc<0>(vst,rst,ty*4096); sb=ldc<16>(vst,rst,ty*4096);
  auto valid=[&](u32 l,int k){ lptr p=L+l+l16; PINW(p);
    NOUNR for(int it=0;it<SPIN;it++){
#pragma unroll
      for(int n=0;n<7;n++) hb[4*n]=LDS(u32x4,p+n*4096);
      if(hf){ sb[2]=0; sb[3]=0; }
      u32 m=mx4(mx4(0,sa),sb);
#pragma unroll
      for(int n=0;n<7;n++) m=mx4(m,hb[4*n]);
      if(!bad(m)) break;
      dmas(l,k); rld<0>(sa,vst,rst,ty*4096+k*2048); rld<16>(sb,vst,rst,ty*4096+k*2048); asm volatile("s_waitcnt vmcnt(0)":"+v"(sa),"+v"(sb)::"memory"); }
    const float s1=swp_add((u2f(sa[0])+u2f(sa[2]))+(u2f(sb[0])+u2f(sb[2])))*(-1.f/448), s2=swp_add((u2f(sa[1])+u2f(sa[3]))+(u2f(sb[1])+u2f(sb[3])));
    sg_=__builtin_fmaf(__builtin_fmaxf(__builtin_fmaf(s2,1.f/448,-s1*s1),0.f),1.00001f,1e-10f);
    const float rs_=__builtin_amdgcn_rsqf(sg_); Lg=0.5f*__builtin_amdgcn_logf(sg_);
    sg_*=rs_; K1=-2.302208198f*rs_; K2=-0.1029432397f*rs_*rs_*rs_; bb=hf?sg_:s1; PINW(sg_); PINW(K1); PINW(K2); PINW(Lg); PINW(bb); };
  if(ty) BAR();
  asm volatile("s_waitcnt vmcnt(0)":"+v"(sa),"+v"(sb)::"memory"); valid(lH0,0);
  if(!ty) BAR();
  STAMP(16);
  dmas(lH1,1); sa=ldc<0>(vst,rst,ty*4096+2048); sb=ldc<16>(vst,rst,ty*4096+2048);
  if(ty) BAR();
  sfor<0,2>([&](auto KK){ constexpr int k=KK.v;
    lptr q[4];
#pragma unroll
    for(int u=1;u<4;u++){ q[u]=L+(k?lH1:lH0)+(((u+tt)&3)-tt)*1024+l16; PINW(q[u]); }
    f32x16 c={};
    auto ord=[](int s){ return s<7?4*s:((s-7)/3)*4+1+(s-7)%3; };
#define RDH(s) hb[ord(s)]=LDS(u32x4,q[ord(s)&3]+(ord(s)>>2)*4096)
    RDH(7); RDH(8); SB();
    sfor<0,28>([&](auto S){ constexpr int s=S.v;
      c=mfma(w1f[ord(s)],hb[ord(s)],c); PINW(c); SB();
      if constexpr(s+9<28) RDH(s+9);
      if constexpr(k==1&&s>=2&&s<10&&(s&1)==0){ if(ty) W2D(bY+(s-2)*1024,10+((s-2)>>1)); }
      SB(); });
    c=__builtin_amdgcn_mfma_f32_32x32x2f32(cb,bb,c,0,0,0); PINW(c);
    BAR();
    STAMP(17+2*k);
    u32 g[8];
#pragma unroll
    for(int p=0;p<8;p++){ float a=c[2*p],d=c[2*p+1];
      float u=__builtin_fmaf(a,__builtin_fmaf(a*a,K2,K1),Lg), v=__builtin_fmaf(d,__builtin_fmaf(d*d,K2,K1),Lg);
      u=__builtin_amdgcn_rcpf(__builtin_amdgcn_exp2f(u)+sg_); v=__builtin_amdgcn_rcpf(__builtin_amdgcn_exp2f(v)+sg_);
      g[p]=pk(a*u,d*v);
      if(p==3) st16(u32x4{g[0],g[1],g[2],g[3]},rhh,l16,(2*ty+k)*114688+(8*j+2*tt)*1024); }
    st16<1024>(u32x4{g[4],g[5],g[6],g[7]},rhh,l16,(2*ty+k)*114688+(8*j+2*tt)*1024);
    SB();
    if constexpr(k==0){ asm volatile("s_waitcnt vmcnt(2)":"+v"(sa),"+v"(sb)::"memory"); valid(lH1,1);
      if(ty){ W2D(bX,6); W2D(bX+2048,7); W2D(bX+4096,8); W2D(bX+6144,9); } else { W2D(bZ,11); W2D(bZ+2048,12); W2D(bZ+4096,13); }
      BAR(); STAMP(18); }
  });
  { const u32 s5=th*229376+w*14336, vs=lane<14?lane*1024+1008:OOB;
    u32x4 sn=ldc<0>(vs,rhh,s5);
    WAITV(3);
    { lptr p0=a0+bX,p1=a1+bX; PINW(p0); PINW(p1);
#pragma unroll
      for(int n=0;n<4;n++){ w2f[6+n][0]=tr2(p0+n*2048,p0+n*2048+512); w2f[6+n][1]=tr2(p1+n*2048,p1+n*2048+512); }
      p0=a0+bY; p1=a1+bY; PINW(p0); PINW(p1); w2f[10][0]=tr2(p0,p0+512); w2f[10][1]=tr2(p1,p1+512);
      p0=a0+bZ; p1=a1+bZ; PINW(p0); PINW(p1);
#pragma unroll
      for(int n=0;n<3;n++){ w2f[11+n][0]=tr2(p0+n*2048,p0+n*2048+512); w2f[11+n][1]=tr2(p1+n*2048,p1+n*2048+512); } }
    if(!ty) BAR();
    STAMP(20);
    u32x4 hh[14],q2; lptr e=L+bX+l16; PINW(e);
    sfor<0,2>([&](auto PP){ constexpr int p=PP.v; const u32 so=s5+p*114688;
      asm volatile("s_waitcnt vmcnt(0)":"+v"(sn)::"memory");
      if(bad(mx4(0,sn))) await(vs,rhh,so);
      STAMP(21+2*p);
      sfor<0,14>([&](auto N){ hh[N.v]=ldc<(N.v&3)*1024>(l16,rhh,so+(N.v>>2)*4096); });
      if constexpr(p==0) sn=ldc<0>(vs,rhh,so+114688); else q2=ldp(v8*16,mk(b2,896),cs*128);
      f32x16 c[2];
      NOUNR for(int it=0;it<SPIN;it++){ u32 m=0; c[0]=f32x16{}; c[1]=f32x16{};
        sfor<0,14>([&](auto N){ constexpr int n=N.v; asm volatile("s_waitcnt vmcnt(%1)":"+v"(hh[n]):"n"(14-n):"memory");
          c[0]=mfma(w2f[n][0],hh[n],c[0]); PINW(c[0]); c[1]=mfma(w2f[n][1],hh[n],c[1]); PINW(c[1]); m=mx4(m,hh[n]); SB(); });
        if(!bad(m)) break;
        sfor<0,14>([&](auto N){ rld<(N.v&3)*1024>(hh[N.v],l16,rhh,so+(N.v>>2)*4096); }); WAITV(0); }
      STAMP(22+2*p);
      LDS(u32x4,e+p*4096)=pk8(c[0],0); LDS(u32x4,e+p*4096+1024)=pk8(c[0],8); LDS(u32x4,e+p*4096+2048)=pk8(c[1],0); LDS(u32x4,e+p*4096+3072)=pk8(c[1],8); });
    asm volatile("s_waitcnt vmcnt(0)":"+v"(q2)::"memory");
    BAR();
    lptr e0=L+24576+tt2*4096+vf,e1=e0+(118784-24576); PINW(e1);
    f32x4 s[4]={}; acc8(id4,hp,s[0],s[1]); acc8(id4,q2,s[2],s[3]);
#pragma unroll
    for(int q=0;q<4;q++){ acc8(id4,LDS(u32x4,e0+q*8192),s[0],s[1]); acc8(id4,LDS(u32x4,e1+q*10240),s[2],s[3]); }
    __builtin_amdgcn_raw_buffer_store_b128(pk44(s[0]+s[2],s[1]+s[3]),mk(out+(size_t)i*128*448,128*896),vo,so_,0); }
  }
  STAMP(25);
  if(b==PBLK&&w==PW){ u32 d=(pb_-pa_)*PMUL; if(d>40000) d=40000; uint64_t t0_,t1_; asm volatile("s_memrealtime %0\n\ts_waitcnt lgkmcnt(0)":"=s"(t0_)::"memory");
    do{ asm volatile("s_memrealtime %0\n\ts_waitcnt lgkmcnt(0)":"=s"(t1_)::"memory"); }while((u32)t1_-(u32)t0_<d); }
}
}
#ifdef __HIP_DEVICE_COMPILE__
#pragma clang attribute pop
#endif
extern "C" void kernel_launch(void* const* d_in,const int* in_sizes,int n_in,void* d_out,int out_size,void* d_ws,size_t ws_size,hipStream_t stream){
  typedef const unsigned short* P;
  (void)hipFuncSetAttribute((const void*)fk::fused,hipFuncAttributeMaxDynamicSharedMemorySize,163840);
  fk::fused<<<256,512,163840,stream>>>((P)d_in[0],(P)d_in[1],(P)d_in[2],(P)d_in[3],(P)d_in[5],(P)d_in[6],(P)d_in[7],(P)d_in[8],(unsigned short*)d_out,(char*)d_ws);
}
```

```cpp
#include <hip/hip_runtime.h>
#include <stdint.h>
#ifdef __HIP_DEVICE_COMPILE__
#pragma clang attribute push(__attribute__((target("no-packed-fp32-ops"))),apply_to=function)
#endif
namespace fk {
typedef unsigned short u16; typedef unsigned u32;
typedef __attribute__((ext_vector_type(4))) u32 u32x4;
typedef __attribute__((ext_vector_type(2))) u32 u32x2;
typedef __attribute__((ext_vector_type(8))) __bf16 bf16x8;
typedef __attribute__((ext_vector_type(2))) __bf16 bf16x2;
typedef __attribute__((ext_vector_type(2))) float f32x2;
typedef __attribute__((ext_vector_type(4))) float f32x4;
typedef __attribute__((ext_vector_type(16))) float f32x16;
typedef short v4i16_t __attribute__((ext_vector_type(4)));
typedef __attribute__((address_space(3))) char* lptr;
typedef __amdgpu_buffer_rsrc_t rsrc_t;
#define DI __device__ __forceinline__
#define LDS(T,a) (*(__attribute__((address_space(3))) T*)(a))
#define STAMP(n)
#define CST(n)
constexpr u32 WS_FLAG=4096, WS_ST=512u<<10, WS_KV=1u<<20, WS_O=2u<<20, WS_H=4u<<20, WS_HH=8u<<20, OOB=0x80000000u;
constexpr int L_WK=98304, L_MISC=155648, L_CNT=L_MISC+2048, L_PF=L_CNT+64, L_KVX=159744, L_ST=159744;
constexpr int L_PHI=32768, L_VI=49152, L_WO=98304, L_R2=32768, L_R3=98304;
constexpr int w1l(int ks){ return ks<12?98304+ks*4096:ks<20?65536+(ks-12)*4096:32768+(ks-20)*4096; }
constexpr int xs(int c){ return (c%6==0?4:c%6==5?5:c%6-1)*16384; }
constexpr float LOG2E=1.4426950408889634f;
#define MAP 1
#define PF1C 9
#define PF2C 9
#define PZC 3
#define DEP 3
constexpr int xtra(int c){ return (c==PF1C?2:0)+(c==PZC?7:0)+(c==PF2C?3:0); }
constexpr int after(int c){ int n=c<DEP?(DEP-1-c)*3:xtra(c-DEP); for(int q=(c<DEP?0:c-DEP+1);q<c;q++) n+=(q+DEP<=6?3:0)+xtra(q); return n; }
constexpr int afterz(){ int n=(PF1C==PZC?2:0)+(PF2C==PZC?3:0); for(int q=PZC+1;q<7;q++) n+=(q+DEP<=6?3:0)+xtra(q); return n; }
#define NOUNR _Pragma("clang loop unroll(disable)")
DI float u2f(u32 v){ return __builtin_bit_cast(float,v); }
DI u32 f2u(float v){ return __builtin_bit_cast(u32,v); }
template<int V> struct IC{ static constexpr int v=V; };
template<int I,int N,class F> DI void sfor(F&& f){ if constexpr(I<N){ f(IC<I>{}); sfor<I+1,N>(f); } }
DI rsrc_t mk(const void* p,u32 bytes){ return __builtin_amdgcn_make_buffer_rsrc((void*)p,0,bytes,0x00020000); }
DI void dma(u32 l,u32 voff,rsrc_t r,u32 soff){ asm volatile("s_mov_b32 m0, %0\n\ts_nop 0\n\tbuffer_load_dwordx4 %1, %2, %3 offen lds"::"s"(l),"v"(voff),"s"(r),"s"(soff):"memory"); }
#define WAITV(n) asm volatile("s_waitcnt vmcnt(" #n ")":::"memory")
#define BAR() asm volatile("s_waitcnt lgkmcnt(0)\n\ts_barrier":::"memory")
#define PIN(x) asm volatile(""::"v"(x))
#define PINW(x) asm volatile("":"+v"(x))
#define SB() __builtin_amdgcn_sched_barrier(0)
DI f32x16 mfma(u32x4 a,u32x4 b,f32x16 c){ return __builtin_amdgcn_mfma_f32_32x32x16_bf16(__builtin_bit_cast(bf16x8,a),__builtin_bit_cast(bf16x8,b),c,0,0,0); }
DI f32x4 m44(u32x2 a,u32x2 b,f32x4 c){ return __builtin_amdgcn_mfma_f32_4x4x4bf16_1k(__builtin_bit_cast(v4i16_t,a),__builtin_bit_cast(v4i16_t,b),c,0,0,0); }
DI u32x2 tr(lptr p){ return __builtin_bit_cast(u32x2,__builtin_amdgcn_ds_read_tr16_b64_v4i16((__attribute__((address_space(3))) v4i16_t*)p)); }
DI u32x4 tr2(lptr p0,lptr p1){ u32x2 a=tr(p0),b=tr(p1); return u32x4{a[0],a[1],b[0],b[1]}; }
DI u32 pk(float lo,float hi){ return __builtin_bit_cast(u32,__builtin_convertvector(f32x2{lo,hi},bf16x2)); }
DI float dot2(u32 a,u32 b,float c){ return __builtin_amdgcn_fdot2_f32_bf16(__builtin_bit_cast(bf16x2,a),__builtin_bit_cast(bf16x2,b),c,false); }
DI float swp_lo(float v){ auto r=__builtin_amdgcn_permlane32_swap(f2u(v),f2u(v),false,false); return u2f(r[0]); }
DI float swp_hi(float v){ auto r=__builtin_amdgcn_permlane32_swap(f2u(v),f2u(v),false,false); return u2f(r[1]); }
DI float swp_add(float v){ auto r=__builtin_amdgcn_permlane32_swap(f2u(v),f2u(v),false,false); return u2f(r[0])+u2f(r[1]); }
DI float swp_max(float v){ auto r=__builtin_amdgcn_permlane32_swap(f2u(v),f2u(v),false,false); return __builtin_fmaxf(u2f(r[0]),u2f(r[1])); }
DI float swp_min(float v){ auto r=__builtin_amdgcn_permlane32_swap(f2u(v),f2u(v),false,false); return __builtin_fminf(u2f(r[0]),u2f(r[1])); }
DI float red8(float v){ v+=u2f(__builtin_amdgcn_update_dpp(0u,f2u(v),0x128,0xf,0xf,false)); auto r=__builtin_amdgcn_permlane16_swap(f2u(v),f2u(v),false,false); return swp_add(u2f(r[0])+u2f(r[1])); }
DI u32x4 pk8(const f32x16& c,int b){ return u32x4{pk(c[b],c[b+1]),pk(c[b+2],c[b+3]),pk(c[b+4],c[b+5]),pk(c[b+6],c[b+7])}; }
DI void dsoftmax(const f32x16& p,float c,u32x4* ph){
  float mx=p[0],mn=p[0];
#pragma unroll
  for(int r=1;r<14;r++){ mx=__builtin_fmaxf(mx,p[r]); mn=__builtin_fminf(mn,p[r]); }
  mx=swp_max(mx); mn=swp_min(mn);
  const float a=-mx*c,b=mn*c; f32x16 e,g; float s=0,u=0;
#pragma unroll
  for(int r=0;r<14;r++){ e[r]=__builtin_amdgcn_exp2f(__builtin_fmaf(p[r],c,a)); g[r]=__builtin_amdgcn_exp2f(__builtin_fmaf(p[r],-c,b)); s+=e[r]; u+=g[r]; }
  s=__builtin_amdgcn_rcpf(swp_add(s)); u=__builtin_amdgcn_rcpf(swp_add(u));
#pragma unroll
  for(int r=0;r<14;r++){ e[r]*=s; g[r]*=u; }
  ph[0]=pk8(e,0); ph[1]=u32x4{pk(e[8],e[9]),pk(e[10],e[11]),pk(e[12],e[13]),0u};
  ph[2]=pk8(g,0); ph[3]=u32x4{pk(g[8],g[9]),pk(g[10],g[11]),pk(g[12],g[13]),0u};
}
DI void dmac(u32 l,u32 voff,rsrc_t r,u32 soff){ asm volatile("s_mov_b32 m0, %0\n\ts_nop 0\n\tbuffer_load_dwordx4 %1, %2, %3 offen sc1 lds"::"s"(l),"v"(voff),"s"(r),"s"(soff):"memory"); }
DI void pfl(u32 voff,rsrc_t r,u32 soff){ asm volatile("s_mov_b32 m0, %0\n\ts_nop 0\n\tbuffer_load_dword %1, %2, %3 offen lds"::"s"(L_PF),"v"(voff),"s"(r),"s"(soff):"memory"); }
template<int O> DI void rld(u32x4& v,u32 voff,rsrc_t r,u32 soff){ asm volatile("buffer_load_dwordx4 %0, %1, %2, %3 offen offset:%4 sc1":"+v"(v):"v"(voff),"s"(r),"s"(soff),"n"(O):"memory"); }
template<int O> DI u32x4 ldc(u32 voff,rsrc_t r,u32 soff){ u32x4 v; asm volatile("buffer_load_dwordx4 %0, %1, %2, %3 offen offset:%4 sc1":"=v"(v):"v"(voff),"s"(r),"s"(soff),"n"(O):"memory"); return v; }
DI u32x4 ldp(u32 voff,rsrc_t r,u32 soff){ u32x4 v; asm volatile("buffer_load_dwordx4 %0, %1, %2, %3 offen":"=v"(v):"v"(voff),"s"(r),"s"(soff):"memory"); return v; }
template<int N> DI void waitv(){ asm volatile("s_waitcnt vmcnt(%0)"::"n"(N):"memory"); }
template<int O=0> DI void st16(u32x4 v,rsrc_t r,u32 voff,u32 soff){ asm volatile("buffer_store_dwordx4 %0, %1, %2, %3 offen offset:%4 sc1\n\ts_nop 1"::"v"(v),"v"(voff),"s"(r),"s"(soff),"n"(O):"memory"); }
DI u32 mx3(u32 a,u32 b,u32 c){ u32 r; asm("v_max3_u32 %0, %1, %2, %3":"=v"(r):"v"(a),"v"(b),"v"(c)); return r; }
DI u32 mx4(u32 m,u32x4 g){ return mx3(mx3(m,g[0],g[1]),g[2],g[3]); }
DI bool bad(u32 m){ return __builtin_amdgcn_ballot_w64(m==0xFFFFFFFFu)!=0; }
constexpr int SPIN=30000;
DI void await(u32 voff,rsrc_t r,u32 soff){ NOUNR for(int it=0;it<SPIN;it++){ u32x4 v=ldc<0>(voff,r,soff); asm volatile("s_waitcnt vmcnt(0)":"+v"(v)::"memory"); if(!bad(mx3(mx3(v[0],v[1],v[2]),v[3],v[3]))) break; } }
DI void lsig(lptr p){ asm volatile("s_mov_b64 exec, 1\n\tds_add_u32 %0, %1\n\ts_mov_b64 exec, -1"::"v"(p),"v"(1u):"memory"); }
DI void lwait(lptr p,u32 n){ NOUNR for(int it=0;it<SPIN*8;it++){ u32 v; asm volatile("ds_read_b32 %0, %1\n\ts_waitcnt lgkmcnt(0)":"=v"(v):"v"(p):"memory"); if(__builtin_amdgcn_readfirstlane(v)>=n) break; } }
DI float sum8(u32x4 f,float c){ c=dot2(f[0],0x3F803F80u,c); c=dot2(f[1],0x3F803F80u,c); c=dot2(f[2],0x3F803F80u,c); return dot2(f[3],0x3F803F80u,c); }
DI float sq8(u32x4 f,float c){ c=dot2(f[0],f[0],c); c=dot2(f[1],f[1],c); c=dot2(f[2],f[2],c); return dot2(f[3],f[3],c); }
DI void acc8(u32x2 id4,u32x4 g,f32x4& a,f32x4& b){ a=m44(id4,u32x2{g[0],g[1]},a); b=m44(id4,u32x2{g[2],g[3]},b); }
DI u32x4 pk44(f32x4 a,f32x4 b){ return u32x4{pk(a[0],a[1]),pk(a[2],a[3]),pk(b[0],b[1]),pk(b[2],b[3])}; }
#define PRIO(n) __builtin_amdgcn_s_setprio(n)

__global__ __launch_bounds__(512,2) void fused(const u16* __restrict__ x,const u16* __restrict__ wqkv,const u16* __restrict__ wphi,const u16* __restrict__ wo,
    const u16* __restrict__ w1,const u16* __restrict__ b1,const u16* __restrict__ w2,const u16* __restrict__ b2,u16* __restrict__ out,char* __restrict__ ws
    ){
  extern __shared__ __attribute__((aligned(16))) char smem[];
  const lptr L=(lptr)smem;
  const int tid=__builtin_amdgcn_workitem_id_x(),lane=tid&63,w=__builtin_amdgcn_readfirstlane(tid>>6),tt=w&3,ty=w>>2,t=lane&31,hf=lane>>5;
  const int b=__builtin_amdgcn_workgroup_id_x(),i=(b&7)*2+((b>>3)&1),j=b>>4;
  const int i1=MAP?j:i,j1=MAP?i:j,odd=j1&1;
  STAMP(0);
  asm volatile(""::"s"(x),"s"(wqkv),"s"(wphi),"s"(wo),"s"(w1),"s"(b1),"s"(w2),"s"(b2),"s"(out),"s"(ws));
  const u32 epoch=*(const __attribute__((address_space(4))) u32*)ws;
  const rsrc_t rw=mk(wqkv,448*2688), rx=mk(x+(size_t)i1*128*448,128*896);
  const u32 vw=(lane>>2)*2688+(lane&3)*16, hoff=56*j1-8*odd+tt*16*2688, sW=hoff+(ty+1)*896, lW=L_WK+ty*28672+tt*1024;
  const u32 vx0=(lane>>3)*896+(((lane&7)^(lane>>4))<<4), vx1=vx0^64, sX=(tt*32+ty*16)*896, lX=tt*4096+ty*2048;
#define CH(c) do{ dma(lW+(c)*4096,vw,rw,sW+(c)*64*2688); dma(xs(c)+lX,vx0,rx,sX+(c)*128); dma(xs(c)+lX+1024,vx1,rx,sX+7168+(c)*128); }while(0)
#define SBAR() asm volatile("s_barrier":::"memory")
  if(w<2) dma(L_MISC+w*1024,tid*16,mk(wphi+j1*784,1568),0);
  CH(0); sfor<1,DEP>([&](auto C){ SBAR(); CH(C.v); });
  if(tid<2) LDS(u32x4,L+L_CNT+tid*16)=u32x4{0,0,0,0};
  const bool act=j<14; const int th=j>=7, cs=j-7*th, tt2=w&1, kq=w>>1;
  const int q4=(lane&15)>>2,mu=((lane>>4)&1)*4+(lane&3),sg=4*(mu&1)+(mu>>1);
  const int rowo=(8*hf+q4)*64, kslot=(mu+odd)&7;
  lptr aW[4],aQ[4],xb[4];
  { const int sw=hf^((t>>1)&7), sl=ty?(odd?(sg==7?0:((sg+1)%7)+1):sg):kslot;
#pragma unroll
    for(int u=0;u<4;u++){ const int r=(u+tt)&3; aW[u]=L+L_WK+ty*28672+rowo+sl*8+r*1024; aQ[u]=L+rowo+kslot*8+r*1024; xb[u]=L+(tt*32+t)*128+((sw^(r*2))<<4); PINW(aW[u]); PINW(xb[u]); } }
  const lptr pp=L+lW+(lane&15)*64+(odd?0:56);
  u32 tag=epoch*0x9E3779B1u+0x7F4A7C15u; asm volatile("":"+s"(tag));
  const rsrc_t ro1=mk(ws+WS_O+i1*114688,114688), r1=mk(w1,act?448*3584:0), r2=mk(w2,act?1792*896:0), r0=mk(wo,act?448*896:0);
  const u32 l16=lane*16;
  const rsrc_t rkv=mk(ws+WS_KV+j1*65536,65536);
  const bool duty=((b>>3)&1)==(((b&7)-1u)<3u); const u32 dq=(b>>4)*8+w;
  auto ost=[&](u32x4 o0,u32x4 o1){ const u32 ub=t*16+((7*j1)>>1)*512, so=tt*28672;
    st16(o0,ro1,ub+(odd+2*hf)*512,so); st16(o1,ro1,hf?OOB:ub+(odd+1)*512,so);
    __builtin_amdgcn_raw_buffer_store_b64(u32x2{o1[0],o1[1]},ro1,hf?ub+(odd?8:1536):OOB,so,16); };
  u32x4 PZ={~0u,~0u,~0u,~0u}; PINW(PZ);
  const u32x2 pv={0x3F80u,0u};
  const lptr cnt=L+L_CNT;
  u32x4 xf[28],wp[2],fw[2][4]; f32x16 c1={}; float ss=0,cz=0;
  auto mm=[&](auto C){ constexpr int c=C.v;
#pragma unroll
    for(int u=0;u<4;u++){ c1=mfma(fw[c&1][u],xf[4*c+u],c1); PINW(c1); }
    SB(); };
  auto dd=[&](auto C){ constexpr int c=C.v; cz=sum8(fw[c&1][0],cz);
    if(ty){ ss=sq8(xf[4*c+2],ss); ss=sq8(xf[4*c+3],ss); } else { ss=sq8(xf[4*c],ss); ss=sq8(xf[4*c+1],ss); }
    PINW(ss); PINW(cz); SB(); };
  sfor<0,7>([&](auto C){ constexpr int c=C.v;
    waitv<after(c)>();
    asm volatile("s_mov_b64 exec, 0xffff\n\tds_write_b64 %0, %1 offset:%2\n\ts_mov_b64 exec, -1"::"v"(pp),"v"(pv),"i"(c*4096):"memory");
    BAR();
    if constexpr(c==0) STAMP(1); else STAMP(25+c);
    if constexpr(c==6){ if(!ty) PRIO(2); }
#pragma unroll
    for(int u=0;u<4;u++){ fw[c&1][u]=tr2(aW[u]+c*4096,aW[u]+c*4096+256); xf[4*c+u]=LDS(u32x4,xb[u]+xs(c)); }
    SB();
    if constexpr(c+DEP<=6) CH(c+DEP);
    if constexpr(c==PZC){
      if(ty){ sfor<0,7>([&](auto U){ dma(tt*1024+U.v*4096,vw,rw,hoff+U.v*64*2688); }); }
      else { st16(PZ,rkv,l16,i1*4096+tt*1024); ost(PZ,PZ); const int t1=j1>=7; const rsrc_t rp=mk(ws+WS_H+i1*114688,j1<14?114688:0); const u32 so=(2*t1+(tt&1))*28672+(4*(j1-7*t1)+(tt>>1))*1024; st16(PZ,rp,l16,so); st16<2048>(PZ,rp,l16,so);
        st16(PZ,mk(ws+WS_ST+i1*8192,8192),lane<8?l16:OOB,(j1*4+tt)*128); } }
    if constexpr(c==PF1C){ const rsrc_t r=mk(w1,duty?448*3584:0); pfl(lane*128,r,dq*12544); pfl(lane<34?lane*128+8192:OOB,r,dq*12544); }
    if constexpr(c==PF2C){ pfl(lane<25?lane*128:OOB,mk(wo,duty?448*896:0),dq*3136); const rsrc_t r=mk(w2,duty?1792*896:0); pfl(lane*128,r,dq*12544); pfl(lane<34?lane*128+8192:OOB,r,dq*12544); }
    SB();
    if constexpr(c>0){ mm(IC<c-1>{}); dd(IC<c-1>{}); }
    if constexpr(c==0){
      const bool mv=(t&26)!=26; const lptr p=L+L_MISC+(t<26?t:t-2)*2+hf*224; u32 e[16];
#pragma unroll
      for(int q=0;q<16;q++){ const int d=16*(q>>3)+8*((q>>2)&1)+(q&3); e[q]=LDS(u16,p+d*56); }
#pragma unroll
      for(int q=0;q<8;q++){ u32 v=e[2*q]|(e[2*q+1]<<16); v=(mv&&(q<6||hf==0))?v:0u; asm volatile("":"+v"(v)); wp[q>>2][q&3]=v; } }
  });
  dd(IC<6>{});
  LDS(float,L+L_KVX+w*256+lane*4)=ss; LDS(float,L+L_KVX+2048+ty*1024+l16+tt*4)=cz;
  SB(); mm(IC<6>{});
  BAR();
  { f32x4 z=LDS(f32x4,L+L_KVX+2048+ty*1024+l16); ss+=LDS(float,L+L_KVX+(w^4)*256+lane*4); cz=(z[0]+z[1])+(z[2]+z[3]); }
  STAMP(2);
  const float nmean=swp_hi(c1[12])*(-1.f/448);
  c1=__builtin_amdgcn_mfma_f32_32x32x2f32(cz,nmean,c1,0,0,0);
  const float rstd=__builtin_amdgcn_rsqf(__builtin_fmaxf(__builtin_fmaf(swp_add(ss),1.f/448,-nmean*nmean),0.f)+1e-5f);
  const int wro=(tt*4096+t*64),c0=((2*hf)^((t>>2)&3))<<4;
  const rsrc_t rhh=mk(ws+WS_HH+i*458752,act?458752:0);
  const u32 v8=(lane>>3), p8=((lane&7)^(lane>>2&4))<<4, vw2=v8*896+p8, sW2=w*224*896+cs*128;
#define W2D(l,n) do{ dma((l),vw2,r2,sW2+(n)*14336); dma((l)+1024,vw2,r2,sW2+(n)*14336+7168); }while(0)
  const int nq=(mu&4)|((mu&1)<<1)|((mu>>1)&1);
  const int ao=(8*hf+q4)*128+(q4>>1)*64+nq*8; const lptr a0=L+ao,a1=L+(ao^64);
  const u32x2 id4={(lane&3)==0?0x3F80u:(lane&3)==1?0x3F800000u:0u,(lane&3)==2?0x3F80u:(lane&3)==3?0x3F800000u:0u};
  const u32 vo=(lane&7)*896+v8*16, so_=((2*th+tt2)*32+kq*8)*896+cs*128, vf=v8*512+(lane&7)*16+kq*128;
  const u32 vw1=v8*3584+p8, sW1=(tt&1)*8*3584+j*256+(tt>>1)*128;
  u32x4 phq[4],w1f[28],w2f[14][2],xr; u32 b1v;
  auto misc=[&]{ const u32 sh=ty*229376+(8*j+2*tt)*1024;
    st16(PZ,rhh,l16,sh); st16<1024>(PZ,rhh,l16,sh); st16(PZ,rhh,l16,sh+114688); st16<1024>(PZ,rhh,l16,sh+114688);
    xr=ldp(vo,mk(x+(size_t)i*128*448,128*896),so_);
    const int tq=t>>2; asm volatile("buffer_load_ushort %0, %1, %2, %3 offen":"=v"(b1v):"v"((((tq&4)|((tq&1)<<1)|((tq>>1)&1))*4+(t&3))*2),"s"(mk(b1,act?3584:0)),"s"(j*256+tt*64):"memory"); };
  auto rdw1=[&]{ lwait(cnt+20,8); lptr q[4];
#pragma unroll
    for(int u=0;u<4;u++){ q[u]=L+65536+((tt&1)?ao^64:ao)+(tt>>1)*2048+((u+tt)&3)*4096; PINW(q[u]); }
#pragma unroll
    for(int n=12;n<20;n++) w1f[n]=tr2(q[n&3]+(w1l(n&~3)-65536),q[n&3]+(w1l(n&~3)-65536)+512);
#pragma unroll
    for(int n=0;n<8;n++) w1f[n]=tr2(q[n&3]+(w1l(n&~3)-65536),q[n&3]+(w1l(n&~3)-65536)+512);
#pragma unroll
    for(int u=0;u<4;u++){ q[u]+=65536; PINW(q[u]); }
#pragma unroll
    for(int n=8;n<12;n++) w1f[n]=tr2(q[n&3],q[n&3]+512);
#pragma unroll
    for(int u=0;u<4;u++){ q[u]-=98304; PINW(q[u]); }
#pragma unroll
    for(int n=20;n<28;n++) w1f[n]=tr2(q[n&3]+(w1l(n&~3)-32768),q[n&3]+(w1l(n&~3)-32768)+512); };
  if(ty==0){
    f32x16 p=mfma(wp[0],pk8(c1,0),f32x16{}); p=mfma(wp[1],pk8(c1,8),p);
    u32x4 ph[4]; dsoftmax(p,rstd*LOG2E,ph);
    LDS(u32x4,L+L_PHI+wro+c0)=ph[0]; LDS(u32x4,L+L_PHI+wro+(c0^16))=ph[1];
    LDS(u32x4,L+L_PHI+2048+wro+c0)=ph[2]; LDS(u32x4,L+L_PHI+2048+wro+(c0^16))=ph[3];
    waitv<afterz()>(); lsig(cnt);
    STAMP(3);
    lwait(cnt,8);
    if(tt<2){
      const int ko=(8*hf+q4)*64+(sg&1)*8; const lptr k0=L+ko+(((sg>>1)^(2*hf))<<4),k1=L+ko+256+(((sg>>1)^(2*hf+1))<<4);
      f32x16 c={};
#pragma unroll
      for(int u=0;u<8;u++){ const int o=(u>>1)*4096+(u&1)*1024; c=mfma(tr2(k0+L_PHI+tt*2048+o,k1+L_PHI+tt*2048+o),tr2(k0+L_VI+o,k1+L_VI+o),c); }
      st16(pk8(c,0),rkv,l16,i1*4096+tt*2048); st16<1024>(pk8(c,8),rkv,l16,i1*4096+tt*2048);
      lsig(cnt+8);
      STAMP(4);
    } else if(tid==128) __hip_atomic_store((u32*)(ws+WS_FLAG+(j1*16+i1)*128),tag,__ATOMIC_RELAXED,__HIP_MEMORY_SCOPE_AGENT);
    lwait(cnt+8,2);
    STAMP(5);
    sfor<20,28>([&](auto K){ dma(w1l(K.v)+tt*1024,vw1,r1,sW1+K.v*16*3584); });
    STAMP(6);
    { const rsrc_t rf=mk(ws,2u<<20); const u32 n=lane&15, vp=lane<16?WS_FLAG+(j1*16+n)*128:lane<32?WS_FLAG+(n*16+i)*128:lane<48?WS_KV+j1*65536+n*4096+tt*1024+1020:OOB;
      NOUNR for(int it=0;it<SPIN;it++){ u32 v; asm volatile("buffer_load_dword %0, %1, %2, 0 offen sc1\n\ts_waitcnt vmcnt(0)":"=v"(v):"v"(vp),"s"(rf):"memory");
        if(__builtin_amdgcn_ballot_w64(lane<32?v!=tag:v==~0u)==0) break; }
      lsig(cnt+20); }
    STAMP(8);
    u32x4 g[16];
    sfor<0,16>([&](auto N){ g[N.v]=ldc<0>(l16,rkv,((N.v+i1)&15)*4096+tt*1024); });
    misc();
    rdw1(); SB();
    STAMP(7);
    f32x4 s[4];
    NOUNR for(int it=0;it<SPIN;it++){ u32 m=0; s[0]=s[1]=s[2]=s[3]=f32x4{};
      sfor<0,8>([&](auto N){ constexpr int n=N.v; asm volatile("s_waitcnt vmcnt(%2)":"+v"(g[2*n]),"+v"(g[2*n+1]):"n"(20-2*n):"memory"); acc8(id4,g[2*n],s[0],s[1]); acc8(id4,g[2*n+1],s[2],s[3]); m=mx4(mx4(m,g[2*n]),g[2*n+1]); });
      if(!bad(m)) break;
      sfor<0,16>([&](auto N){ rld<0>(g[N.v],l16,rkv,((N.v+i1)&15)*4096+tt*1024); }); WAITV(0); }
    LDS(u32x4,L+L_KVX+tt*1024+l16)=pk44(s[0]+s[2],s[1]+s[3]);
    PRIO(0);
  } else {
#pragma unroll
    for(int r=0;r<16;r++) c1[r]*=rstd;
    u32x4 v0=pk8(c1,0),v1=pk8(c1,8); if(hf){ v1[2]=0x3F80u; v1[3]=0u; }
    LDS(u32x4,L+L_VI+wro+c0)=v0; LDS(u32x4,L+L_VI+wro+(c0^16))=v1;
    lsig(cnt);
    STAMP(3);
    sfor<0,20>([&](auto K){ dma(w1l(K.v)+tt*1024,vw1,r1,sW1+K.v*16*3584); });
    waitv<afterz()+20>();
    u32x4 fq[28]; float cq=0;
#pragma unroll
    for(int u=0;u<7;u++) fq[4*u]=tr2(aQ[0]+u*4096,aQ[0]+u*4096+256);
#pragma unroll
    for(int u=0;u<7;u++) cq=sum8(fq[4*u],cq);
    LDS(float,L+L_MISC+l16+tt*4)=cq;
    lsig(cnt+4); lwait(cnt+4,4);
    STAMP(4);
    auto ord=[](int s){ return s<7?4*s:((s-7)/3)*4+1+(s-7)%3; };
    f32x16 cQ={}; f32x4 z=LDS(f32x4,L+L_MISC+l16);
    sfor<0,28>([&](auto S){ constexpr int s=S.v;
      if constexpr(s<21){ constexpr int n=ord(s+7); fq[n]=tr2(aQ[n&3]+(n>>2)*4096,aQ[n&3]+(n>>2)*4096+256); }
      if constexpr(s==21) lsig(cnt+16);
      SB(); cQ=mfma(fq[ord(s)],xf[ord(s)],cQ); PINW(cQ); SB(); });
    WAITV(0); lsig(cnt+20);
    cQ=__builtin_amdgcn_mfma_f32_32x32x2f32((z[0]+z[1])+(z[2]+z[3]),nmean,cQ,0,0,0);
    f32x16 p=mfma(wp[0],pk8(cQ,0),f32x16{}); p=mfma(wp[1],pk8(cQ,8),p);
    dsoftmax(p,rstd*LOG2E,phq);
    PINW(phq[0]); PINW(phq[1]); PINW(phq[2]); PINW(phq[3]);
    STAMP(5);
    lwait(cnt+16,4);
    W2D(w*4096,0); W2D(w*4096+2048,1); W2D(tt*4096,-56); W2D(tt*4096+2048,-55);
    misc();
    STAMP(6);
    rdw1();
    STAMP(7);
  }
  BAR();
  STAMP(9);
  if(ty){ f32x16 c={};
#pragma unroll
    for(int n=0;n<4;n++) c=mfma(LDS(u32x4,L+L_KVX+n*1024+l16),phq[n],c);
    const float rd=__builtin_amdgcn_rcpf(swp_hi(c[12])+1e-6f);
#pragma unroll
    for(int r=0;r<16;r++) c[r]*=rd;
    ost(pk8(c,0),pk8(c,8));
  }
  STAMP(10);
  if(act){
  sfor<0,7>([&](auto U){ dma(L_WO+(w*7+U.v)*1024,vw2,r0,(w*7+U.v)*7168+cs*128); });
  W2D(L_R2+w*6144,2); W2D(L_R2+w*6144+2048,3); W2D(L_R2+w*6144+4096,4);
  float cb=0;
  if(ty){
#pragma unroll
    for(int n=14;n<28;n++) cb=sum8(w1f[n],cb);
  } else {
#pragma unroll
    for(int n=0;n<14;n++) cb=sum8(w1f[n],cb); }
  LDS(float,L+L_MISC+w*256+lane*4)=cb;
  STAMP(11);
  const rsrc_t rh=mk(ws+WS_H+i*114688,114688), rst=mk(ws+WS_ST+i*8192,8192);
  u32x4 hp;
  { const rsrc_t ro=mk(ws+WS_O+i*114688,114688); const u32 so=(2*th+tt2)*28672+kq*7168;
    asm volatile("s_waitcnt vmcnt(6)":"+v"(xr),"+v"(b1v)::"memory");
    BAR();
    lptr p0=a0+L_WO+kq*14336,p1=a1+L_WO+kq*14336; PINW(p0); PINW(p1);
    u32x4 fa[7][2]; f32x16 c[2];
#pragma unroll
    for(int n=0;n<7;n++){ fa[n][0]=tr2(p0+n*2048,p0+n*2048+512); fa[n][1]=tr2(p1+n*2048,p1+n*2048+512); }
    SB();
    await(lane<14?lane*512+496:OOB,ro,so);
    STAMP(12);
    u32x4 of[7];
    sfor<0,7>([&](auto N){ of[N.v]=ldc<(N.v&3)*1024>(l16,ro,so+(N.v>>2)*4096); });
    NOUNR for(int it=0;it<SPIN;it++){ u32 m=0; c[0]=f32x16{}; c[1]=f32x16{};
      sfor<0,7>([&](auto N){ constexpr int n=N.v; asm volatile("s_waitcnt vmcnt(%1)":"+v"(of[n]):"n"(6-n):"memory");
        c[0]=mfma(fa[n][0],of[n],c[0]); PINW(c[0]); c[1]=mfma(fa[n][1],of[n],c[1]); PINW(c[1]); m=mx4(m,of[n]); SB(); });
      if(!bad(m)) break;
      sfor<0,7>([&](auto N){ rld<(N.v&3)*1024>(of[N.v],l16,ro,so+(N.v>>2)*4096); }); WAITV(0); }
    SB();
    p0=a0+w*4096; p1=a1+w*4096;
#pragma unroll
    for(int n=0;n<2;n++){ w2f[n][0]=tr2(p0+n*2048,p0+n*2048+512); w2f[n][1]=tr2(p1+n*2048,p1+n*2048+512); }
    asm volatile("":::"memory");
    lptr e=L+w*4096+l16;
    const float cbo=LDS(float,L+L_MISC+(w^4)*256+lane*4);
    LDS(u32x4,e)=pk8(c[0],0); LDS(u32x4,e+1024)=pk8(c[0],8); LDS(u32x4,e+2048)=pk8(c[1],0); LDS(u32x4,e+3072)=pk8(c[1],8);
    cb=swp_add(cb+cbo); cb=hf?u2f(b1v<<16):cb; PINW(cb);
    STAMP(13);
    BAR();
    e=L+tt2*4096+vf;
    f32x4 s[4]={}; acc8(id4,xr,s[0],s[1]);
#pragma unroll
    for(int q=0;q<4;q++) acc8(id4,LDS(u32x4,e+q*8192),s[2*(q&1)],s[2*(q&1)+1]);
    s[0]+=s[2]; s[1]+=s[3];
    hp=pk44(s[0],s[1]);
    st16(hp,rh,vf,(2*th+tt2)*28672+cs*4096);
    float s1=((s[0][0]+s[0][1])+(s[0][2]+s[0][3]))+((s[1][0]+s[1][1])+(s[1][2]+s[1][3])), s2=s[0][0]*s[0][0], s3=s[1][0]*s[1][0];
#pragma unroll
    for(int q=1;q<4;q++){ s2=__builtin_fmaf(s[0][q],s[0][q],s2); s3=__builtin_fmaf(s[1][q],s[1][q],s3); }
    s1=red8(s1); s2=red8(s2+s3);
    asm volatile("buffer_store_dwordx2 %0, %1, %2, %3 offen sc1"::"v"(f32x2{s1,s2}),"v"(lane<8?lane*64:OOB),"s"(rst),"s"(((2*th+tt2)*32+kq*8)*64+cs*8):"memory");
    SB(); }
  STAMP(14);
  if(b==0&&tid==0) __hip_atomic_store((u32*)ws,tag,__ATOMIC_RELAXED,__HIP_MEMORY_SCOPE_AGENT);
  const u32 bX=ty?24576+tt*8192:118784+tt*10240, bY=ty?86016+tt*8192:bX+8192, bZ=ty?bY+2048:tt*6144;
  W2D(L_R3+w*2048,5);
  if(!ty){ W2D(bX,6); W2D(bX+2048,7); W2D(bX+4096,8); W2D(bX+6144,9); W2D(bX+8192,10); }
  { lptr p0=a0+L_R2+w*6144,p1=a1+L_R2+w*6144;
#pragma unroll
    for(int n=0;n<3;n++){ w2f[2+n][0]=tr2(p0+n*2048,p0+n*2048+512); w2f[2+n][1]=tr2(p1+n*2048,p1+n*2048+512); } }
  BAR();
  const u32 sH=ty*57344+tt*1024, lH0=ty*28672+tt*1024, lH1=(ty?57344:86016)+tt*1024;
  await(lane<14?lane*4096+1008:OOB,rh,sH);
  STAMP(15);
  { lptr p0=a0+L_R3+w*2048,p1=a1+L_R3+w*2048; PINW(p0); PINW(p1); w2f[5][0]=tr2(p0,p0+512); w2f[5][1]=tr2(p1,p1+512); }
  if(ty) __builtin_amdgcn_s_sleep(4);
  u32x4 hb[28],sa,sb; float sg_,K1,K2,Lg,bb; const u32 vst=t*64+hf*32;
  auto dmas=[&](u32 l,int k){ sfor<0,7>([&](auto U){ dmac(l+U.v*4096,l16,rh,sH+k*28672+U.v*4096); }); };
  dmas(lH0,0); sa=ldc<0>(vst,rst,ty*4096); sb=ldc<16>(vst,rst,ty*4096);
  auto valid=[&](u32 l,int k){ lptr p=L+l+l16; PINW(p);
    NOUNR for(int it=0;it<SPIN;it++){
#pragma unroll
      for(int n=0;n<7;n++) hb[4*n]=LDS(u32x4,p+n*4096);
      if(hf){ sb[2]=0; sb[3]=0; }
      u32 m=mx4(mx4(0,sa),sb);
#pragma unroll
      for(int n=0;n<7;n++) m=mx4(m,hb[4*n]);
      if(!bad(m)) break;
      dmas(l,k); rld<0>(sa,vst,rst,ty*4096+k*2048); rld<16>(sb,vst,rst,ty*4096+k*2048); asm volatile("s_waitcnt vmcnt(0)":"+v"(sa),"+v"(sb)::"memory"); }
    const float s1=swp_add((u2f(sa[0])+u2f(sa[2]))+(u2f(sb[0])+u2f(sb[2])))*(-1.f/448), s2=swp_add((u2f(sa[1])+u2f(sa[3]))+(u2f(sb[1])+u2f(sb[3])));
    sg_=__builtin_fmaf(__builtin_fmaxf(__builtin_fmaf(s2,1.f/448,-s1*s1),0.f),1.00001f,1e-10f);
    const float rs_=__builtin_amdgcn_rsqf(sg_); Lg=0.5f*__builtin_amdgcn_logf(sg_);
    sg_*=rs_; K1=-2.302208198f*rs_; K2=-0.1029432397f*rs_*rs_*rs_; bb=hf?sg_:s1; PINW(sg_); PINW(K1); PINW(K2); PINW(Lg); PINW(bb); };
  if(ty) BAR();
  asm volatile("s_waitcnt vmcnt(0)":"+v"(sa),"+v"(sb)::"memory"); valid(lH0,0);
  if(!ty) BAR();
  STAMP(16);
  dmas(lH1,1); sa=ldc<0>(vst,rst,ty*4096+2048); sb=ldc<16>(vst,rst,ty*4096+2048);
  if(ty) BAR();
  sfor<0,2>([&](auto KK){ constexpr int k=KK.v;
    lptr q[4];
#pragma unroll
    for(int u=1;u<4;u++){ q[u]=L+(k?lH1:lH0)+(((u+tt)&3)-tt)*1024+l16; PINW(q[u]); }
    f32x16 c={};
    auto ord=[](int s){ return s<7?4*s:((s-7)/3)*4+1+(s-7)%3; };
#define RDH(s) hb[ord(s)]=LDS(u32x4,q[ord(s)&3]+(ord(s)>>2)*4096)
    RDH(7); RDH(8); SB();
    sfor<0,28>([&](auto S){ constexpr int s=S.v;
      c=mfma(w1f[ord(s)],hb[ord(s)],c); PINW(c); SB();
      if constexpr(s+9<28) RDH(s+9);
      if constexpr(k==1&&s>=2&&s<10&&(s&1)==0){ if(ty) W2D(bY+(s-2)*1024,10+((s-2)>>1)); }
      SB(); });
    c=__builtin_amdgcn_mfma_f32_32x32x2f32(cb,bb,c,0,0,0); PINW(c);
    BAR();
    STAMP(17+2*k);
    u32 g[8];
#pragma unroll
    for(int p=0;p<8;p++){ float a=c[2*p],d=c[2*p+1];
      float u=__builtin_fmaf(a,__builtin_fmaf(a*a,K2,K1),Lg), v=__builtin_fmaf(d,__builtin_fmaf(d*d,K2,K1),Lg);
      u=__builtin_amdgcn_rcpf(__builtin_amdgcn_exp2f(u)+sg_); v=__builtin_amdgcn_rcpf(__builtin_amdgcn_exp2f(v)+sg_);
      g[p]=pk(a*u,d*v);
      if(p==3) st16(u32x4{g[0],g[1],g[2],g[3]},rhh,l16,(2*ty+k)*114688+(8*j+2*tt)*1024); }
    st16<1024>(u32x4{g[4],g[5],g[6],g[7]},rhh,l16,(2*ty+k)*114688+(8*j+2*tt)*1024);
    SB();
    if constexpr(k==0){ asm volatile("s_waitcnt vmcnt(2)":"+v"(sa),"+v"(sb)::"memory"); valid(lH1,1);
      if(ty){ W2D(bX,6); W2D(bX+2048,7); W2D(bX+4096,8); W2D(bX+6144,9); } else { W2D(bZ,11); W2D(bZ+2048,12); W2D(bZ+4096,13); }
      BAR(); STAMP(18); }
  });
  { const u32 s5=th*229376+w*14336, vs=lane<14?lane*1024+1008:OOB;
    u32x4 sn=ldc<0>(vs,rhh,s5);
    WAITV(3);
    { lptr p0=a0+bX,p1=a1+bX; PINW(p0); PINW(p1);
#pragma unroll
      for(int n=0;n<4;n++){ w2f[6+n][0]=tr2(p0+n*2048,p0+n*2048+512); w2f[6+n][1]=tr2(p1+n*2048,p1+n*2048+512); }
      p0=a0+bY; p1=a1+bY; PINW(p0); PINW(p1); w2f[10][0]=tr2(p0,p0+512); w2f[10][1]=tr2(p1,p1+512);
      p0=a0+bZ; p1=a1+bZ; PINW(p0); PINW(p1);
#pragma unroll
      for(int n=0;n<3;n++){ w2f[11+n][0]=tr2(p0+n*2048,p0+n*2048+512); w2f[11+n][1]=tr2(p1+n*2048,p1+n*2048+512); } }
    if(!ty) BAR();
    STAMP(20);
    u32x4 hh[14],q2; lptr e=L+bX+l16; PINW(e);
    sfor<0,2>([&](auto PP){ constexpr int p=PP.v; const u32 so=s5+p*114688;
      asm volatile("s_waitcnt vmcnt(0)":"+v"(sn)::"memory");
      if(bad(mx4(0,sn))) await(vs,rhh,so);
      STAMP(21+2*p);
      sfor<0,14>([&](auto N){ hh[N.v]=ldc<(N.v&3)*1024>(l16,rhh,so+(N.v>>2)*4096); });
      if constexpr(p==0) sn=ldc<0>(vs,rhh,so+114688); else q2=ldp(v8*16,mk(b2,896),cs*128);
      f32x16 c[2];
      NOUNR for(int it=0;it<SPIN;it++){ u32 m=0; c[0]=f32x16{}; c[1]=f32x16{};
        sfor<0,14>([&](auto N){ constexpr int n=N.v; asm volatile("s_waitcnt vmcnt(%1)":"+v"(hh[n]):"n"(14-n):"memory");
          c[0]=mfma(w2f[n][0],hh[n],c[0]); PINW(c[0]); c[1]=mfma(w2f[n][1],hh[n],c[1]); PINW(c[1]); m=mx4(m,hh[n]); SB(); });
        if(!bad(m)) break;
        sfor<0,14>([&](auto N){ rld<(N.v&3)*1024>(hh[N.v],l16,rhh,so+(N.v>>2)*4096); }); WAITV(0); }
      STAMP(22+2*p);
      LDS(u32x4,e+p*4096)=pk8(c[0],0); LDS(u32x4,e+p*4096+1024)=pk8(c[0],8); LDS(u32x4,e+p*4096+2048)=pk8(c[1],0); LDS(u32x4,e+p*4096+3072)=pk8(c[1],8); });
    asm volatile("s_waitcnt vmcnt(0)":"+v"(q2)::"memory");
    BAR();
    lptr e0=L+24576+tt2*4096+vf,e1=e0+(118784-24576); PINW(e1);
    f32x4 s[4]={}; acc8(id4,hp,s[0],s[1]); acc8(id4,q2,s[2],s[3]);
#pragma unroll
    for(int q=0;q<4;q++){ acc8(id4,LDS(u32x4,e0+q*8192),s[0],s[1]); acc8(id4,LDS(u32x4,e1+q*10240),s[2],s[3]); }
    __builtin_amdgcn_raw_buffer_store_b128(pk44(s[0]+s[2],s[1]+s[3]),mk(out+(size_t)i*128*448,128*896),vo,so_,17); }
  }
  STAMP(25);
}
}
#ifdef __HIP_DEVICE_COMPILE__
#pragma clang attribute pop
#endif
extern "C" void kernel_launch(void* const* d_in,const int* in_sizes,int n_in,void* d_out,int out_size,void* d_ws,size_t ws_size,hipStream_t stream){
  typedef const unsigned short* P;
  (void)hipFuncSetAttribute((const void*)fk::fused,hipFuncAttributeMaxDynamicSharedMemorySize,163840);
  fk::fused<<<256,512,163840,stream>>>((P)d_in[0],(P)d_in[1],(P)d_in[2],(P)d_in[3],(P)d_in[5],(P)d_in[6],(P)d_in[7],(P)d_in[8],(unsigned short*)d_out,(char*)d_ws);
}
```

```cpp
#define PROBE 1
#define PA 0
#define PB 1
#define PMUL 4
#define PBLK 223
#define PW 7
#include <hip/hip_runtime.h>
#include <stdint.h>
#ifdef __HIP_DEVICE_COMPILE__
#pragma clang attribute push(__attribute__((target("no-packed-fp32-ops"))),apply_to=function)
#endif
namespace fk {
typedef unsigned short u16; typedef unsigned u32;
typedef __attribute__((ext_vector_type(4))) u32 u32x4;
typedef __attribute__((ext_vector_type(2))) u32 u32x2;
typedef __attribute__((ext_vector_type(8))) __bf16 bf16x8;
typedef __attribute__((ext_vector_type(2))) __bf16 bf16x2;
typedef __attribute__((ext_vector_type(2))) float f32x2;
typedef __attribute__((ext_vector_type(4))) float f32x4;
typedef __attribute__((ext_vector_type(16))) float f32x16;
typedef short v4i16_t __attribute__((ext_vector_type(4)));
typedef __attribute__((address_space(3))) char* lptr;
typedef __amdgpu_buffer_rsrc_t rsrc_t;
#define DI __device__ __forceinline__
#define LDS(T,a) (*(__attribute__((address_space(3))) T*)(a))
#define STAMP(n) do{ if((n)==PA||(n)==PB){ uint64_t t_; asm volatile("s_memrealtime %0\n\ts_waitcnt lgkmcnt(0)":"=s"(t_)::"memory"); if((n)==PA) pa_=(u32)t_; if((n)==PB) pb_=(u32)t_; } }while(0)
#define CST(n) do{ if((n)==46||(n)==47){ uint64_t t_; asm volatile("s_memtime %0\n\ts_waitcnt lgkmcnt(0)":"=s"(t_)::"memory"); if((n)==46) ca_=(u32)t_; else cb_=(u32)t_; } }while(0)
constexpr u32 WS_FLAG=4096, WS_ST=512u<<10, WS_KV=1u<<20, WS_O=2u<<20, WS_H=4u<<20, WS_HH=8u<<20, OOB=0x80000000u;
constexpr int L_WK=98304, L_MISC=155648, L_CNT=L_MISC+2048, L_PF=L_CNT+64, L_KVX=159744, L_ST=159744;
constexpr int L_PHI=32768, L_VI=49152, L_WO=98304, L_R2=32768, L_R3=98304;
constexpr int w1l(int ks){ return ks<12?98304+ks*4096:ks<20?65536+(ks-12)*4096:32768+(ks-20)*4096; }
constexpr int xs(int c){ return (c%6==0?4:c%6==5?5:c%6-1)*16384; }
constexpr float LOG2E=1.4426950408889634f;
#define MAP 1
#define PF1C 9
#define PF2C 9
#define PZC 3
#define DEP 3
constexpr int xtra(int c){ return (c==PF1C?2:0)+(c==PZC?7:0)+(c==PF2C?3:0); }
constexpr int after(int c){ int n=c<DEP?(DEP-1-c)*3:xtra(c-DEP); for(int q=(c<DEP?0:c-DEP+1);q<c;q++) n+=(q+DEP<=6?3:0)+xtra(q); return n; }
constexpr int afterz(){ int n=(PF1C==PZC?2:0)+(PF2C==PZC?3:0); for(int q=PZC+1;q<7;q++) n+=(q+DEP<=6?3:0)+xtra(q); return n; }
#define NOUNR _Pragma("clang loop unroll(disable)")
DI float u2f(u32 v){ return __builtin_bit_cast(float,v); }
DI u32 f2u(float v){ return __builtin_bit_cast(u32,v); }
template<int V> struct IC{ static constexpr int v=V; };
template<int I,int N,class F> DI void sfor(F&& f){ if constexpr(I<N){ f(IC<I>{}); sfor<I+1,N>(f); } }
DI rsrc_t mk(const void* p,u32 bytes){ return __builtin_amdgcn_make_buffer_rsrc((void*)p,0,bytes,0x00020000); }
DI void dma(u32 l,u32 voff,rsrc_t r,u32 soff){ asm volatile("s_mov_b32 m0, %0\n\ts_nop 0\n\tbuffer_load_dwordx4 %1, %2, %3 offen lds"::"s"(l),"v"(voff),"s"(r),"s"(soff):"memory"); }
#define WAITV(n) asm volatile("s_waitcnt vmcnt(" #n ")":::"memory")
#define BAR() asm volatile("s_waitcnt lgkmcnt(0)\n\ts_barrier":::"memory")
#define PIN(x) asm volatile(""::"v"(x))
#define PINW(x) asm volatile("":"+v"(x))
#define SB() __builtin_amdgcn_sched_barrier(0)
DI f32x16 mfma(u32x4 a,u32x4 b,f32x16 c){ return __builtin_amdgcn_mfma_f32_32x32x16_bf16(__builtin_bit_cast(bf16x8,a),__builtin_bit_cast(bf16x8,b),c,0,0,0); }
DI f32x4 m44(u32x2 a,u32x2 b,f32x4 c){ return __builtin_amdgcn_mfma_f32_4x4x4bf16_1k(__builtin_bit_cast(v4i16_t,a),__builtin_bit_cast(v4i16_t,b),c,0,0,0); }
DI u32x2 tr(lptr p){ return __builtin_bit_cast(u32x2,__builtin_amdgcn_ds_read_tr16_b64_v4i16((__attribute__((address_space(3))) v4i16_t*)p)); }
DI u32x4 tr2(lptr p0,lptr p1){ u32x2 a=tr(p0),b=tr(p1); return u32x4{a[0],a[1],b[0],b[1]}; }
DI u32 pk(float lo,float hi){ return __builtin_bit_cast(u32,__builtin_convertvector(f32x2{lo,hi},bf16x2)); }
DI float dot2(u32 a,u32 b,float c){ return __builtin_amdgcn_fdot2_f32_bf16(__builtin_bit_cast(bf16x2,a),__builtin_bit_cast(bf16x2,b),c,false); }
DI float swp_lo(float v){ auto r=__builtin_amdgcn_permlane32_swap(f2u(v),f2u(v),false,false); return u2f(r[0]); }
DI float swp_hi(float v){ auto r=__builtin_amdgcn_permlane32_swap(f2u(v),f2u(v),false,false); return u2f(r[1]); }
DI float swp_add(float v){ auto r=__builtin_amdgcn_permlane32_swap(f2u(v),f2u(v),false,false); return u2f(r[0])+u2f(r[1]); }
DI float swp_max(float v){ auto r=__builtin_amdgcn_permlane32_swap(f2u(v),f2u(v),false,false); return __builtin_fmaxf(u2f(r[0]),u2f(r[1])); }
DI float swp_min(float v){ auto r=__builtin_amdgcn_permlane32_swap(f2u(v),f2u(v),false,false); return __builtin_fminf(u2f(r[0]),u2f(r[1])); }
DI float red8(float v){ v+=u2f(__builtin_amdgcn_update_dpp(0u,f2u(v),0x128,0xf,0xf,false)); auto r=__builtin_amdgcn_permlane16_swap(f2u(v),f2u(v),false,false); return swp_add(u2f(r[0])+u2f(r[1])); }
DI u32x4 pk8(const f32x16& c,int b){ return u32x4{pk(c[b],c[b+1]),pk(c[b+2],c[b+3]),pk(c[b+4],c[b+5]),pk(c[b+6],c[b+7])}; }
DI void dsoftmax(const f32x16& p,float c,u32x4* ph){
  float mx=p[0],mn=p[0];
#pragma unroll
  for(int r=1;r<14;r++){ mx=__builtin_fmaxf(mx,p[r]); mn=__builtin_fminf(mn,p[r]); }
  mx=swp_max(mx); mn=swp_min(mn);
  const float a=-mx*c,b=mn*c; f32x16 e,g; float s=0,u=0;
#pragma unroll
  for(int r=0;r<14;r++){ e[r]=__builtin_amdgcn_exp2f(__builtin_fmaf(p[r],c,a)); g[r]=__builtin_amdgcn_exp2f(__builtin_fmaf(p[r],-c,b)); s+=e[r]; u+=g[r]; }
  s=__builtin_amdgcn_rcpf(swp_add(s)); u=__builtin_amdgcn_rcpf(swp_add(u));
#pragma unroll
  for(int r=0;r<14;r++){ e[r]*=s; g[r]*=u; }
  ph[0]=pk8(e,0); ph[1]=u32x4{pk(e[8],e[9]),pk(e[10],e[11]),pk(e[12],e[13]),0u};
  ph[2]=pk8(g,0); ph[3]=u32x4{pk(g[8],g[9]),pk(g[10],g[11]),pk(g[12],g[13]),0u};
}
DI void dmac(u32 l,u32 voff,rsrc_t r,u32 soff){ asm volatile("s_mov_b32 m0, %0\n\ts_nop 0\n\tbuffer_load_dwordx4 %1, %2, %3 offen sc1 lds"::"s"(l),"v"(voff),"s"(r),"s"(soff):"memory"); }
DI void pfl(u32 voff,rsrc_t r,u32 soff){ asm volatile("s_mov_b32 m0, %0\n\ts_nop 0\n\tbuffer_load_dword %1, %2, %3 offen lds"::"s"(L_PF),"v"(voff),"s"(r),"s"(soff):"memory"); }
template<int O> DI void rld(u32x4& v,u32 voff,rsrc_t r,u32 soff){ asm volatile("buffer_load_dwordx4 %0, %1, %2, %3 offen offset:%4 sc1":"+v"(v):"v"(voff),"s"(r),"s"(soff),"n"(O):"memory"); }
template<int O> DI u32x4 ldc(u32 voff,rsrc_t r,u32 soff){ u32x4 v; asm volatile("buffer_load_dwordx4 %0, %1, %2, %3 offen offset:%4 sc1":"=v"(v):"v"(voff),"s"(r),"s"(soff),"n"(O):"memory"); return v; }
DI u32x4 ldp(u32 voff,rsrc_t r,u32 soff){ u32x4 v; asm volatile("buffer_load_dwordx4 %0, %1, %2, %3 offen":"=v"(v):"v"(voff),"s"(r),"s"(soff):"memory"); return v; }
template<int N> DI void waitv(){ asm volatile("s_waitcnt vmcnt(%0)"::"n"(N):"memory"); }
template<int O=0> DI void st16(u32x4 v,rsrc_t r,u32 voff,u32 soff){ asm volatile("buffer_store_dwordx4 %0, %1, %2, %3 offen offset:%4 sc1\n\ts_nop 1"::"v"(v),"v"(voff),"s"(r),"s"(soff),"n"(O):"memory"); }
DI u32 mx3(u32 a,u32 b,u32 c){ u32 r; asm("v_max3_u32 %0, %1, %2, %3":"=v"(r):"v"(a),"v"(b),"v"(c)); return r; }
DI u32 mx4(u32 m,u32x4 g){ return mx3(mx3(m,g[0],g[1]),g[2],g[3]); }
DI bool bad(u32 m){ return __builtin_amdgcn_ballot_w64(m==0xFFFFFFFFu)!=0; }
constexpr int SPIN=30000;
DI void await(u32 voff,rsrc_t r,u32 soff){ NOUNR for(int it=0;it<SPIN;it++){ u32x4 v=ldc<0>(voff,r,soff); asm volatile("s_waitcnt vmcnt(0)":"+v"(v)::"memory"); if(!bad(mx3(mx3(v[0],v[1],v[2]),v[3],v[3]))) break; } }
DI void lsig(lptr p){ asm volatile("s_mov_b64 exec, 1\n\tds_add_u32 %0, %1\n\ts_mov_b64 exec, -1"::"v"(p),"v"(1u):"memory"); }
DI void lwait(lptr p,u32 n){ NOUNR for(int it=0;it<SPIN*8;it++){ u32 v; asm volatile("ds_read_b32 %0, %1\n\ts_waitcnt lgkmcnt(0)":"=v"(v):"v"(p):"memory"); if(__builtin_amdgcn_readfirstlane(v)>=n) break; } }
DI float sum8(u32x4 f,float c){ c=dot2(f[0],0x3F803F80u,c); c=dot2(f[1],0x3F803F80u,c); c=dot2(f[2],0x3F803F80u,c); return dot2(f[3],0x3F803F80u,c); }
DI float sq8(u32x4 f,float c){ c=dot2(f[0],f[0],c); c=dot2(f[1],f[1],c); c=dot2(f[2],f[2],c); return dot2(f[3],f[3],c); }
DI void acc8(u32x2 id4,u32x4 g,f32x4& a,f32x4& b){ a=m44(id4,u32x2{g[0],g[1]},a); b=m44(id4,u32x2{g[2],g[3]},b); }
DI u32x4 pk44(f32x4 a,f32x4 b){ return u32x4{pk(a[0],a[1]),pk(a[2],a[3]),pk(b[0],b[1]),pk(b[2],b[3])}; }
#define PRIO(n) __builtin_amdgcn_s_setprio(n)

__global__ __launch_bounds__(512,2) void fused(const u16* __restrict__ x,const u16* __restrict__ wqkv,const u16* __restrict__ wphi,const u16* __restrict__ wo,
    const u16* __restrict__ w1,const u16* __restrict__ b1,const u16* __restrict__ w2,const u16* __restrict__ b2,u16* __restrict__ out,char* __restrict__ ws
    ){
  extern __shared__ __attribute__((aligned(16))) char smem[];
  const lptr L=(lptr)smem;
  const int tid=__builtin_amdgcn_workitem_id_x(),lane=tid&63,w=__builtin_amdgcn_readfirstlane(tid>>6),tt=w&3,ty=w>>2,t=lane&31,hf=lane>>5;
  const int b=__builtin_amdgcn_workgroup_id_x(),i=(b&7)*2+((b>>3)&1),j=b>>4;
  const int i1=MAP?j:i,j1=MAP?i:j,odd=j1&1;
  u32 pa_=0,pb_=0;
  STAMP(0);
  asm volatile(""::"s"(x),"s"(wqkv),"s"(wphi),"s"(wo),"s"(w1),"s"(b1),"s"(w2),"s"(b2),"s"(out),"s"(ws));
  const u32 epoch=*(const __attribute__((address_space(4))) u32*)ws;
  const rsrc_t rw=mk(wqkv,448*2688), rx=mk(x+(size_t)i1*128*448,128*896);
  const u32 vw=(lane>>2)*2688+(lane&3)*16, hoff=56*j1-8*odd+tt*16*2688, sW=hoff+(ty+1)*896, lW=L_WK+ty*28672+tt*1024;
  const u32 vx0=(lane>>3)*896+(((lane&7)^(lane>>4))<<4), vx1=vx0^64, sX=(tt*32+ty*16)*896, lX=tt*4096+ty*2048;
#define CH(c) do{ dma(lW+(c)*4096,vw,rw,sW+(c)*64*2688); dma(xs(c)+lX,vx0,rx,sX+(c)*128); dma(xs(c)+lX+1024,vx1,rx,sX+7168+(c)*128); }while(0)
#define SBAR() asm volatile("s_barrier":::"memory")
  if(w<2) dma(L_MISC+w*1024,tid*16,mk(wphi+j1*784,1568),0);
  CH(0); sfor<1,DEP>([&](auto C){ SBAR(); CH(C.v); });
  if(tid<2) LDS(u32x4,L+L_CNT+tid*16)=u32x4{0,0,0,0};
  const bool act=j<14; const int th=j>=7, cs=j-7*th, tt2=w&1, kq=w>>1;
  const int q4=(lane&15)>>2,mu=((lane>>4)&1)*4+(lane&3),sg=4*(mu&1)+(mu>>1);
  const int rowo=(8*hf+q4)*64, kslot=(mu+odd)&7;
  lptr aW[4],aQ[4],xb[4];
  { const int sw=hf^((t>>1)&7), sl=ty?(odd?(sg==7?0:((sg+1)%7)+1):sg):kslot;
#pragma unroll
    for(int u=0;u<4;u++){ const int r=(u+tt)&3; aW[u]=L+L_WK+ty*28672+rowo+sl*8+r*1024; aQ[u]=L+rowo+kslot*8+r*1024; xb[u]=L+(tt*32+t)*128+((sw^(r*2))<<4); PINW(aW[u]); PINW(xb[u]); } }
  const lptr pp=L+lW+(lane&15)*64+(odd?0:56);
  u32 tag=epoch*0x9E3779B1u+0x7F4A7C15u; asm volatile("":"+s"(tag));
  const rsrc_t ro1=mk(ws+WS_O+i1*114688,114688), r1=mk(w1,act?448*3584:0), r2=mk(w2,act?1792*896:0), r0=mk(wo,act?448*896:0);
  const u32 l16=lane*16;
  const rsrc_t rkv=mk(ws+WS_KV+j1*65536,65536);
  const bool duty=((b>>3)&1)==(((b&7)-1u)<3u); const u32 dq=(b>>4)*8+w;
  auto ost=[&](u32x4 o0,u32x4 o1){ const u32 ub=t*16+((7*j1)>>1)*512, so=tt*28672;
    st16(o0,ro1,ub+(odd+2*hf)*512,so); st16(o1,ro1,hf?OOB:ub+(odd+1)*512,so);
    __builtin_amdgcn_raw_buffer_store_b64(u32x2{o1[0],o1[1]},ro1,hf?ub+(odd?8:1536):OOB,so,16); };
  u32x4 PZ={~0u,~0u,~0u,~0u}; PINW(PZ);
  const u32x2 pv={0x3F80u,0u};
  const lptr cnt=L+L_CNT;
  u32x4 xf[28],wp[2],fw[2][4]; f32x16 c1={}; float ss=0,cz=0;
  auto mm=[&](auto C){ constexpr int c=C.v;
#pragma unroll
    for(int u=0;u<4;u++){ c1=mfma(fw[c&1][u],xf[4*c+u],c1); PINW(c1); }
    SB(); };
  auto dd=[&](auto C){ constexpr int c=C.v; cz=sum8(fw[c&1][0],cz);
    if(ty){ ss=sq8(xf[4*c+2],ss); ss=sq8(xf[4*c+3],ss); } else { ss=sq8(xf[4*c],ss); ss=sq8(xf[4*c+1],ss); }
    PINW(ss); PINW(cz); SB(); };
  sfor<0,7>([&](auto C){ constexpr int c=C.v;
    waitv<after(c)>();
    asm volatile("s_mov_b64 exec, 0xffff\n\tds_write_b64 %0, %1 offset:%2\n\ts_mov_b64 exec, -1"::"v"(pp),"v"(pv),"i"(c*4096):"memory");
    BAR();
    if constexpr(c==0) STAMP(1); else STAMP(25+c);
    if constexpr(c==6){ if(!ty) PRIO(2); }
#pragma unroll
    for(int u=0;u<4;u++){ fw[c&1][u]=tr2(aW[u]+c*4096,aW[u]+c*4096+256); xf[4*c+u]=LDS(u32x4,xb[u]+xs(c)); }
    SB();
    if constexpr(c+DEP<=6) CH(c+DEP);
    if constexpr(c==PZC){
      if(ty){ sfor<0,7>([&](auto U){ dma(tt*1024+U.v*4096,vw,rw,hoff+U.v*64*2688); }); }
      else { st16(PZ,rkv,l16,i1*4096+tt*1024); ost(PZ,PZ); const int t1=j1>=7; const rsrc_t rp=mk(ws+WS_H+i1*114688,j1<14?114688:0); const u32 so=(2*t1+(tt&1))*28672+(4*(j1-7*t1)+(tt>>1))*1024; st16(PZ,rp,l16,so); st16<2048>(PZ,rp,l16,so);
        st16(PZ,mk(ws+WS_ST+i1*8192,8192),lane<8?l16:OOB,(j1*4+tt)*128); } }
    if constexpr(c==PF1C){ const rsrc_t r=mk(w1,duty?448*3584:0); pfl(lane*128,r,dq*12544); pfl(lane<34?lane*128+8192:OOB,r,dq*12544); }
    if constexpr(c==PF2C){ pfl(lane<25?lane*128:OOB,mk(wo,duty?448*896:0),dq*3136); const rsrc_t r=mk(w2,duty?1792*896:0); pfl(lane*128,r,dq*12544); pfl(lane<34?lane*128+8192:OOB,r,dq*12544); }
    SB();
    if constexpr(c>0){ mm(IC<c-1>{}); dd(IC<c-1>{}); }
    if constexpr(c==0){
      const bool mv=(t&26)!=26; const lptr p=L+L_MISC+(t<26?t:t-2)*2+hf*224; u32 e[16];
#pragma unroll
      for(int q=0;q<16;q++){ const int d=16*(q>>3)+8*((q>>2)&1)+(q&3); e[q]=LDS(u16,p+d*56); }
#pragma unroll
      for(int q=0;q<8;q++){ u32 v=e[2*q]|(e[2*q+1]<<16); v=(mv&&(q<6||hf==0))?v:0u; asm volatile("":"+v"(v)); wp[q>>2][q&3]=v; } }
  });
  dd(IC<6>{});
  LDS(float,L+L_KVX+w*256+lane*4)=ss; LDS(float,L+L_KVX+2048+ty*1024+l16+tt*4)=cz;
  SB(); mm(IC<6>{});
  BAR();
  { f32x4 z=LDS(f32x4,L+L_KVX+2048+ty*1024+l16); ss+=LDS(float,L+L_KVX+(w^4)*256+lane*4); cz=(z[0]+z[1])+(z[2]+z[3]); }
  STAMP(2);
  const float nmean=swp_hi(c1[12])*(-1.f/448);
  c1=__builtin_amdgcn_mfma_f32_32x32x2f32(cz,nmean,c1,0,0,0);
  const float rstd=__builtin_amdgcn_rsqf(__builtin_fmaxf(__builtin_fmaf(swp_add(ss),1.f/448,-nmean*nmean),0.f)+1e-5f);
  const int wro=(tt*4096+t*64),c0=((2*hf)^((t>>2)&3))<<4;
  const rsrc_t rhh=mk(ws+WS_HH+i*458752,act?458752:0);
  const u32 v8=(lane>>3), p8=((lane&7)^(lane>>2&4))<<4, vw2=v8*896+p8, sW2=w*224*896+cs*128;
#define W2D(l,n) do{ dma((l),vw2,r2,sW2+(n)*14336); dma((l)+1024,vw2,r2,sW2+(n)*14336+7168); }while(0)
  const int nq=(mu&4)|((mu&1)<<1)|((mu>>1)&1);
  const int ao=(8*hf+q4)*128+(q4>>1)*64+nq*8; const lptr a0=L+ao,a1=L+(ao^64);
  const u32x2 id4={(lane&3)==0?0x3F80u:(lane&3)==1?0x3F800000u:0u,(lane&3)==2?0x3F80u:(lane&3)==3?0x3F800000u:0u};
  const u32 vo=(lane&7)*896+v8*16, so_=((2*th+tt2)*32+kq*8)*896+cs*128, vf=v8*512+(lane&7)*16+kq*128;
  const u32 vw1=v8*3584+p8, sW1=(tt&1)*8*3584+j*256+(tt>>1)*128;
  u32x4 phq[4],w1f[28],w2f[14][2],xr; u32 b1v;
  auto misc=[&]{ const u32 sh=ty*229376+(8*j+2*tt)*1024;
    st16(PZ,rhh,l16,sh); st16<1024>(PZ,rhh,l16,sh); st16(PZ,rhh,l16,sh+114688); st16<1024>(PZ,rhh,l16,sh+114688);
    xr=ldp(vo,mk(x+(size_t)i*128*448,128*896),so_);
    const int tq=t>>2; asm volatile("buffer_load_ushort %0, %1, %2, %3 offen":"=v"(b1v):"v"((((tq&4)|((tq&1)<<1)|((tq>>1)&1))*4+(t&3))*2),"s"(mk(b1,act?3584:0)),"s"(j*256+tt*64):"memory"); };
  auto rdw1=[&]{ lwait(cnt+20,8); lptr q[4];
#pragma unroll
    for(int u=0;u<4;u++){ q[u]=L+65536+((tt&1)?ao^64:ao)+(tt>>1)*2048+((u+tt)&3)*4096; PINW(q[u]); }
#pragma unroll
    for(int n=12;n<20;n++) w1f[n]=tr2(q[n&3]+(w1l(n&~3)-65536),q[n&3]+(w1l(n&~3)-65536)+512);
#pragma unroll
    for(int n=0;n<8;n++) w1f[n]=tr2(q[n&3]+(w1l(n&~3)-65536),q[n&3]+(w1l(n&~3)-65536)+512);
#pragma unroll
    for(int u=0;u<4;u++){ q[u]+=65536; PINW(q[u]); }
#pragma unroll
    for(int n=8;n<12;n++) w1f[n]=tr2(q[n&3],q[n&3]+512);
#pragma unroll
    for(int u=0;u<4;u++){ q[u]-=98304; PINW(q[u]); }
#pragma unroll
    for(int n=20;n<28;n++) w1f[n]=tr2(q[n&3]+(w1l(n&~3)-32768),q[n&3]+(w1l(n&~3)-32768)+512); };
  if(ty==0){
    f32x16 p=mfma(wp[0],pk8(c1,0),f32x16{}); p=mfma(wp[1],pk8(c1,8),p);
    u32x4 ph[4]; dsoftmax(p,rstd*LOG2E,ph);
    LDS(u32x4,L+L_PHI+wro+c0)=ph[0]; LDS(u32x4,L+L_PHI+wro+(c0^16))=ph[1];
    LDS(u32x4,L+L_PHI+2048+wro+c0)=ph[2]; LDS(u32x4,L+L_PHI+2048+wro+(c0^16))=ph[3];
    waitv<afterz()>(); lsig(cnt);
    STAMP(3);
    lwait(cnt,8);
    if(tt<2){
      const int ko=(8*hf+q4)*64+(sg&1)*8; const lptr k0=L+ko+(((sg>>1)^(2*hf))<<4),k1=L+ko+256+(((sg>>1)^(2*hf+1))<<4);
      f32x16 c={};
#pragma unroll
      for(int u=0;u<8;u++){ const int o=(u>>1)*4096+(u&1)*1024; c=mfma(tr2(k0+L_PHI+tt*2048+o,k1+L_PHI+tt*2048+o),tr2(k0+L_VI+o,k1+L_VI+o),c); }
      st16(pk8(c,0),rkv,l16,i1*4096+tt*2048); st16<1024>(pk8(c,8),rkv,l16,i1*4096+tt*2048);
      lsig(cnt+8);
      STAMP(4);
    } else if(tid==128) __hip_atomic_store((u32*)(ws+WS_FLAG+(j1*16+i1)*128),tag,__ATOMIC_RELAXED,__HIP_MEMORY_SCOPE_AGENT);
    lwait(cnt+8,2);
    STAMP(5);
    sfor<20,28>([&](auto K){ dma(w1l(K.v)+tt*1024,vw1,r1,sW1+K.v*16*3584); });
    STAMP(6);
    { const rsrc_t rf=mk(ws,2u<<20); const u32 n=lane&15, vp=lane<16?WS_FLAG+(j1*16+n)*128:lane<32?WS_FLAG+(n*16+i)*128:lane<48?WS_KV+j1*65536+n*4096+tt*1024+1020:OOB;
      NOUNR for(int it=0;it<SPIN;it++){ u32 v; asm volatile("buffer_load_dword %0, %1, %2, 0 offen sc1\n\ts_waitcnt vmcnt(0)":"=v"(v):"v"(vp),"s"(rf):"memory");
        if(__builtin_amdgcn_ballot_w64(lane<32?v!=tag:v==~0u)==0) break; }
      lsig(cnt+20); }
    STAMP(8);
    u32x4 g[16];
    sfor<0,16>([&](auto N){ g[N.v]=ldc<0>(l16,rkv,((N.v+i1)&15)*4096+tt*1024); });
    misc();
    rdw1(); SB();
    STAMP(7);
    f32x4 s[4];
    NOUNR for(int it=0;it<SPIN;it++){ u32 m=0; s[0]=s[1]=s[2]=s[3]=f32x4{};
      sfor<0,8>([&](auto N){ constexpr int n=N.v; asm volatile("s_waitcnt vmcnt(%2)":"+v"(g[2*n]),"+v"(g[2*n+1]):"n"(20-2*n):"memory"); acc8(id4,g[2*n],s[0],s[1]); acc8(id4,g[2*n+1],s[2],s[3]); m=mx4(mx4(m,g[2*n]),g[2*n+1]); });
      if(!bad(m)) break;
      sfor<0,16>([&](auto N){ rld<0>(g[N.v],l16,rkv,((N.v+i1)&15)*4096+tt*1024); }); WAITV(0); }
    LDS(u32x4,L+L_KVX+tt*1024+l16)=pk44(s[0]+s[2],s[1]+s[3]);
    PRIO(0);
  } else {
#pragma unroll
    for(int r=0;r<16;r++) c1[r]*=rstd;
    u32x4 v0=pk8(c1,0),v1=pk8(c1,8); if(hf){ v1[2]=0x3F80u; v1[3]=0u; }
    LDS(u32x4,L+L_VI+wro+c0)=v0; LDS(u32x4,L+L_VI+wro+(c0^16))=v1;
    lsig(cnt);
    STAMP(3);
    sfor<0,20>([&](auto K){ dma(w1l(K.v)+tt*1024,vw1,r1,sW1+K.v*16*3584); });
    waitv<afterz()+20>();
    u32x4 fq[28]; float cq=0;
#pragma unroll
    for(int u=0;u<7;u++) fq[4*u]=tr2(aQ[0]+u*4096,aQ[0]+u*4096+256);
#pragma unroll
    for(int u=0;u<7;u++) cq=sum8(fq[4*u],cq);
    LDS(float,L+L_MISC+l16+tt*4)=cq;
    lsig(cnt+4); lwait(cnt+4,4);
    STAMP(4);
    auto ord=[](int s){ return s<7?4*s:((s-7)/3)*4+1+(s-7)%3; };
    f32x16 cQ={}; f32x4 z=LDS(f32x4,L+L_MISC+l16);
    sfor<0,28>([&](auto S){ constexpr int s=S.v;
      if constexpr(s<21){ constexpr int n=ord(s+7); fq[n]=tr2(aQ[n&3]+(n>>2)*4096,aQ[n&3]+(n>>2)*4096+256); }
      if constexpr(s==21) lsig(cnt+16);
      SB(); cQ=mfma(fq[ord(s)],xf[ord(s)],cQ); PINW(cQ); SB(); });
    WAITV(0); lsig(cnt+20);
    cQ=__builtin_amdgcn_mfma_f32_32x32x2f32((z[0]+z[1])+(z[2]+z[3]),nmean,cQ,0,0,0);
    f32x16 p=mfma(wp[0],pk8(cQ,0),f32x16{}); p=mfma(wp[1],pk8(cQ,8),p);
    dsoftmax(p,rstd*LOG2E,phq);
    PINW(phq[0]); PINW(phq[1]); PINW(phq[2]); PINW(phq[3]);
    STAMP(5);
    lwait(cnt+16,4);
    W2D(w*4096,0); W2D(w*4096+2048,1); W2D(tt*4096,-56); W2D(tt*4096+2048,-55);
    misc();
    STAMP(6);
    rdw1();
    STAMP(7);
  }
  BAR();
  STAMP(9);
  if(ty){ f32x16 c={};
#pragma unroll
    for(int n=0;n<4;n++) c=mfma(LDS(u32x4,L+L_KVX+n*1024+l16),phq[n],c);
    const float rd=__builtin_amdgcn_rcpf(swp_hi(c[12])+1e-6f);
#pragma unroll
    for(int r=0;r<16;r++) c[r]*=rd;
    ost(pk8(c,0),pk8(c,8));
  }
  STAMP(10);
  if(act){
  sfor<0,7>([&](auto U){ dma(L_WO+(w*7+U.v)*1024,vw2,r0,(w*7+U.v)*7168+cs*128); });
  W2D(L_R2+w*6144,2); W2D(L_R2+w*6144+2048,3); W2D(L_R2+w*6144+4096,4);
  float cb=0;
  if(ty){
#pragma unroll
    for(int n=14;n<28;n++) cb=sum8(w1f[n],cb);
  } else {
#pragma unroll
    for(int n=0;n<14;n++) cb=sum8(w1f[n],cb); }
  LDS(float,L+L_MISC+w*256+lane*4)=cb;
  STAMP(11);
  const rsrc_t rh=mk(ws+WS_H+i*114688,114688), rst=mk(ws+WS_ST+i*8192,8192);
  u32x4 hp;
  { const rsrc_t ro=mk(ws+WS_O+i*114688,114688); const u32 so=(2*th+tt2)*28672+kq*7168;
    asm volatile("s_waitcnt vmcnt(6)":"+v"(xr),"+v"(b1v)::"memory");
    BAR();
    lptr p0=a0+L_WO+kq*14336,p1=a1+L_WO+kq*14336; PINW(p0); PINW(p1);
    u32x4 fa[7][2]; f32x16 c[2];
#pragma unroll
    for(int n=0;n<7;n++){ fa[n][0]=tr2(p0+n*2048,p0+n*2048+512); fa[n][1]=tr2(p1+n*2048,p1+n*2048+512); }
    SB();
    await(lane<14?lane*512+496:OOB,ro,so);
    STAMP(12);
    u32x4 of[7];
    sfor<0,7>([&](auto N){ of[N.v]=ldc<(N.v&3)*1024>(l16,ro,so+(N.v>>2)*4096); });
    NOUNR for(int it=0;it<SPIN;it++){ u32 m=0; c[0]=f32x16{}; c[1]=f32x16{};
      sfor<0,7>([&](auto N){ constexpr int n=N.v; asm volatile("s_waitcnt vmcnt(%1)":"+v"(of[n]):"n"(6-n):"memory");
        c[0]=mfma(fa[n][0],of[n],c[0]); PINW(c[0]); c[1]=mfma(fa[n][1],of[n],c[1]); PINW(c[1]); m=mx4(m,of[n]); SB(); });
      if(!bad(m)) break;
      sfor<0,7>([&](auto N){ rld<(N.v&3)*1024>(of[N.v],l16,ro,so+(N.v>>2)*4096); }); WAITV(0); }
    SB();
    p0=a0+w*4096; p1=a1+w*4096;
#pragma unroll
    for(int n=0;n<2;n++){ w2f[n][0]=tr2(p0+n*2048,p0+n*2048+512); w2f[n][1]=tr2(p1+n*2048,p1+n*2048+512); }
    asm volatile("":::"memory");
    lptr e=L+w*4096+l16;
    const float cbo=LDS(float,L+L_MISC+(w^4)*256+lane*4);
    LDS(u32x4,e)=pk8(c[0],0); LDS(u32x4,e+1024)=pk8(c[0],8); LDS(u32x4,e+2048)=pk8(c[1],0); LDS(u32x4,e+3072)=pk8(c[1],8);
    cb=swp_add(cb+cbo); cb=hf?u2f(b1v<<16):cb; PINW(cb);
    STAMP(13);
    BAR();
    e=L+tt2*4096+vf;
    f32x4 s[4]={}; acc8(id4,xr,s[0],s[1]);
#pragma unroll
    for(int q=0;q<4;q++) acc8(id4,LDS(u32x4,e+q*8192),s[2*(q&1)],s[2*(q&1)+1]);
    s[0]+=s[2]; s[1]+=s[3];
    hp=pk44(s[0],s[1]);
    st16(hp,rh,vf,(2*th+tt2)*28672+cs*4096);
    float s1=((s[0][0]+s[0][1])+(s[0][2]+s[0][3]))+((s[1][0]+s[1][1])+(s[1][2]+s[1][3])), s2=s[0][0]*s[0][0], s3=s[1][0]*s[1][0];
#pragma unroll
    for(int q=1;q<4;q++){ s2=__builtin_fmaf(s[0][q],s[0][q],s2); s3=__builtin_fmaf(s[1][q],s[1][q],s3); }
    s1=red8(s1); s2=red8(s2+s3);
    asm volatile("buffer_store_dwordx2 %0, %1, %2, %3 offen sc1"::"v"(f32x2{s1,s2}),"v"(lane<8?lane*64:OOB),"s"(rst),"s"(((2*th+tt2)*32+kq*8)*64+cs*8):"memory");
    SB(); }
  STAMP(14);
  if(b==0&&tid==0) __hip_atomic_store((u32*)ws,tag,__ATOMIC_RELAXED,__HIP_MEMORY_SCOPE_AGENT);
  const u32 bX=ty?24576+tt*8192:118784+tt*10240, bY=ty?86016+tt*8192:bX+8192, bZ=ty?bY+2048:tt*6144;
  W2D(L_R3+w*2048,5);
  if(!ty){ W2D(bX,6); W2D(bX+2048,7); W2D(bX+4096,8); W2D(bX+6144,9); W2D(bX+8192,10); }
  { lptr p0=a0+L_R2+w*6144,p1=a1+L_R2+w*6144;
#pragma unroll
    for(int n=0;n<3;n++){ w2f[2+n][0]=tr2(p0+n*2048,p0+n*2048+512); w2f[2+n][1]=tr2(p1+n*2048,p1+n*2048+512); } }
  BAR();
  const u32 sH=ty*57344+tt*1024, lH0=ty*28672+tt*1024, lH1=(ty?57344:86016)+tt*1024;
  await(lane<14?lane*4096+1008:OOB,rh,sH);
  STAMP(15);
  { lptr p0=a0+L_R3+w*2048,p1=a1+L_R3+w*2048; PINW(p0); PINW(p1); w2f[5][0]=tr2(p0,p0+512); w2f[5][1]=tr2(p1,p1+512); }
  if(ty) __builtin_amdgcn_s_sleep(4);
  u32x4 hb[28],sa,sb; float sg_,K1,K2,Lg,bb; const u32 vst=t*64+hf*32;
  auto dmas=[&](u32 l,int k){ sfor<0,7>([&](auto U){ dmac(l+U.v*4096,l16,rh,sH+k*28672+U.v*4096); }); };
  dmas(lH0,0); sa=ldc<0>(vst,rst,ty*4096); sb=ldc<16>(vst,rst,ty*4096);
  auto valid=[&](u32 l,int k){ lptr p=L+l+l16; PINW(p);
    NOUNR for(int it=0;it<SPIN;it++){
#pragma unroll
      for(int n=0;n<7;n++) hb[4*n]=LDS(u32x4,p+n*4096);
      if(hf){ sb[2]=0; sb[3]=0; }
      u32 m=mx4(mx4(0,sa),sb);
#pragma unroll
      for(int n=0;n<7;n++) m=mx4(m,hb[4*n]);
      if(!bad(m)) break;
      dmas(l,k); rld<0>(sa,vst,rst,ty*4096+k*2048); rld<16>(sb,vst,rst,ty*4096+k*2048); asm volatile("s_waitcnt vmcnt(0)":"+v"(sa),"+v"(sb)::"memory"); }
    const float s1=swp_add((u2f(sa[0])+u2f(sa[2]))+(u2f(sb[0])+u2f(sb[2])))*(-1.f/448), s2=swp_add((u2f(sa[1])+u2f(sa[3]))+(u2f(sb[1])+u2f(sb[3])));
    sg_=__builtin_fmaf(__builtin_fmaxf(__builtin_fmaf(s2,1.f/448,-s1*s1),0.f),1.00001f,1e-10f);
    const float rs_=__builtin_amdgcn_rsqf(sg_); Lg=0.5f*__builtin_amdgcn_logf(sg_);
    sg_*=rs_; K1=-2.302208198f*rs_; K2=-0.1029432397f*rs_*rs_*rs_; bb=hf?sg_:s1; PINW(sg_); PINW(K1); PINW(K2); PINW(Lg); PINW(bb); };
  if(ty) BAR();
  asm volatile("s_waitcnt vmcnt(0)":"+v"(sa),"+v"(sb)::"memory"); valid(lH0,0);
  if(!ty) BAR();
  STAMP(16);
  dmas(lH1,1); sa=ldc<0>(vst,rst,ty*4096+2048); sb=ldc<16>(vst,rst,ty*4096+2048);
  if(ty) BAR();
  sfor<0,2>([&](auto KK){ constexpr int k=KK.v;
    lptr q[4];
#pragma unroll
    for(int u=1;u<4;u++){ q[u]=L+(k?lH1:lH0)+(((u+tt)&3)-tt)*1024+l16; PINW(q[u]); }
    f32x16 c={};
    auto ord=[](int s){ return s<7?4*s:((s-7)/3)*4+1+(s-7)%3; };
#define RDH(s) hb[ord(s)]=LDS(u32x4,q[ord(s)&3]+(ord(s)>>2)*4096)
    RDH(7); RDH(8); SB();
    sfor<0,28>([&](auto S){ constexpr int s=S.v;
      c=mfma(w1f[ord(s)],hb[ord(s)],c); PINW(c); SB();
      if constexpr(s+9<28) RDH(s+9);
      if constexpr(k==1&&s>=2&&s<10&&(s&1)==0){ if(ty) W2D(bY+(s-2)*1024,10+((s-2)>>1)); }
      SB(); });
    c=__builtin_amdgcn_mfma_f32_32x32x2f32(cb,bb,c,0,0,0); PINW(c);
    BAR();
    STAMP(17+2*k);
    u32 g[8];
#pragma unroll
    for(int p=0;p<8;p++){ float a=c[2*p],d=c[2*p+1];
      float u=__builtin_fmaf(a,__builtin_fmaf(a*a,K2,K1),Lg), v=__builtin_fmaf(d,__builtin_fmaf(d*d,K2,K1),Lg);
      u=__builtin_amdgcn_rcpf(__builtin_amdgcn_exp2f(u)+sg_); v=__builtin_amdgcn_rcpf(__builtin_amdgcn_exp2f(v)+sg_);
      g[p]=pk(a*u,d*v);
      if(p==3) st16(u32x4{g[0],g[1],g[2],g[3]},rhh,l16,(2*ty+k)*114688+(8*j+2*tt)*1024); }
    st16<1024>(u32x4{g[4],g[5],g[6],g[7]},rhh,l16,(2*ty+k)*114688+(8*j+2*tt)*1024);
    SB();
    if constexpr(k==0){ asm volatile("s_waitcnt vmcnt(2)":"+v"(sa),"+v"(sb)::"memory"); valid(lH1,1);
      if(ty){ W2D(bX,6); W2D(bX+2048,7); W2D(bX+4096,8); W2D(bX+6144,9); } else { W2D(bZ,11); W2D(bZ+2048,12); W2D(bZ+4096,13); }
      BAR(); STAMP(18); }
  });
  { const u32 s5=th*229376+w*14336, vs=lane<14?lane*1024+1008:OOB;
    u32x4 sn=ldc<0>(vs,rhh,s5);
    WAITV(3);
    { lptr p0=a0+bX,p1=a1+bX; PINW(p0); PINW(p1);
#pragma unroll
      for(int n=0;n<4;n++){ w2f[6+n][0]=tr2(p0+n*2048,p0+n*2048+512); w2f[6+n][1]=tr2(p1+n*2048,p1+n*2048+512); }
      p0=a0+bY; p1=a1+bY; PINW(p0); PINW(p1); w2f[10][0]=tr2(p0,p0+512); w2f[10][1]=tr2(p1,p1+512);
      p0=a0+bZ; p1=a1+bZ; PINW(p0); PINW(p1);
#pragma unroll
      for(int n=0;n<3;n++){ w2f[11+n][0]=tr2(p0+n*2048,p0+n*2048+512); w2f[11+n][1]=tr2(p1+n*2048,p1+n*2048+512); } }
    if(!ty) BAR();
    STAMP(20);
    u32x4 hh[14],q2; lptr e=L+bX+l16; PINW(e);
    sfor<0,2>([&](auto PP){ constexpr int p=PP.v; const u32 so=s5+p*114688;
      asm volatile("s_waitcnt vmcnt(0)":"+v"(sn)::"memory");
      if(bad(mx4(0,sn))) await(vs,rhh,so);
      STAMP(21+2*p);
      sfor<0,14>([&](auto N){ hh[N.v]=ldc<(N.v&3)*1024>(l16,rhh,so+(N.v>>2)*4096); });
      if constexpr(p==0) sn=ldc<0>(vs,rhh,so+114688); else q2=ldp(v8*16,mk(b2,896),cs*128);
      f32x16 c[2];
      NOUNR for(int it=0;it<SPIN;it++){ u32 m=0; c[0]=f32x16{}; c[1]=f32x16{};
        sfor<0,14>([&](auto N){ constexpr int n=N.v; asm volatile("s_waitcnt vmcnt(%1)":"+v"(hh[n]):"n"(14-n):"memory");
          c[0]=mfma(w2f[n][0],hh[n],c[0]); PINW(c[0]); c[1]=mfma(w2f[n][1],hh[n],c[1]); PINW(c[1]); m=mx4(m,hh[n]); SB(); });
        if(!bad(m)) break;
        sfor<0,14>([&](auto N){ rld<(N.v&3)*1024>(hh[N.v],l16,rhh,so+(N.v>>2)*4096); }); WAITV(0); }
      STAMP(22+2*p);
      LDS(u32x4,e+p*4096)=pk8(c[0],0); LDS(u32x4,e+p*4096+1024)=pk8(c[0],8); LDS(u32x4,e+p*4096+2048)=pk8(c[1],0); LDS(u32x4,e+p*4096+3072)=pk8(c[1],8); });
    asm volatile("s_waitcnt vmcnt(0)":"+v"(q2)::"memory");
    BAR();
    lptr e0=L+24576+tt2*4096+vf,e1=e0+(118784-24576); PINW(e1);
    f32x4 s[4]={}; acc8(id4,hp,s[0],s[1]); acc8(id4,q2,s[2],s[3]);
#pragma unroll
    for(int q=0;q<4;q++){ acc8(id4,LDS(u32x4,e0+q*8192),s[0],s[1]); acc8(id4,LDS(u32x4,e1+q*10240),s[2],s[3]); }
    __builtin_amdgcn_raw_buffer_store_b128(pk44(s[0]+s[2],s[1]+s[3]),mk(out+(size_t)i*128*448,128*896),vo,so_,0); }
  }
  STAMP(25);
  if(b==PBLK&&w==PW){ u32 d=(pb_-pa_)*PMUL; if(d>40000) d=40000; uint64_t t0_,t1_; asm volatile("s_memrealtime %0\n\ts_waitcnt lgkmcnt(0)":"=s"(t0_)::"memory");
    do{ asm volatile("s_memrealtime %0\n\ts_waitcnt lgkmcnt(0)":"=s"(t1_)::"memory"); }while((u32)t1_-(u32)t0_<d); }
}
}
#ifdef __HIP_DEVICE_COMPILE__
#pragma clang attribute pop
#endif
extern "C" void kernel_launch(void* const* d_in,const int* in_sizes,int n_in,void* d_out,int out_size,void* d_ws,size_t ws_size,hipStream_t stream){
  typedef const unsigned short* P;
  (void)hipFuncSetAttribute((const void*)fk::fused,hipFuncAttributeMaxDynamicSharedMemorySize,163840);
  fk::fused<<<256,512,163840,stream>>>((P)d_in[0],(P)d_in[1],(P)d_in[2],(P)d_in[3],(P)d_in[5],(P)d_in[6],(P)d_in[7],(P)d_in[8],(unsigned short*)d_out,(char*)d_ws);
}
```

```cpp
#include <hip/hip_runtime.h>
#include <stdint.h>
#ifdef __HIP_DEVICE_COMPILE__
#pragma clang attribute push(__attribute__((target("no-packed-fp32-ops"))),apply_to=function)
#endif
namespace fk {
typedef unsigned short u16; typedef unsigned u32;
typedef __attribute__((ext_vector_type(4))) u32 u32x4;
typedef __attribute__((ext_vector_type(2))) u32 u32x2;
typedef __attribute__((ext_vector_type(8))) __bf16 bf16x8;
typedef __attribute__((ext_vector_type(2))) __bf16 bf16x2;
typedef __attribute__((ext_vector_type(2))) float f32x2;
typedef __attribute__((ext_vector_type(4))) float f32x4;
typedef __attribute__((ext_vector_type(16))) float f32x16;
typedef short v4i16_t __attribute__((ext_vector_type(4)));
typedef __attribute__((address_space(3))) char* lptr;
typedef __amdgpu_buffer_rsrc_t rsrc_t;
#define DI __device__ __forceinline__
#define LDS(T,a) (*(__attribute__((address_space(3))) T*)(a))
#define STAMP(n)
#define CNT(n,v)
#define CST(n)
constexpr u32 WS_FLAG=4096, WS_ST=512u<<10, WS_KV=1u<<20, WS_O=2u<<20, WS_H=4u<<20, WS_HH=8u<<20, OOB=0x80000000u;
constexpr int L_WK=98304, L_MISC=155648, L_CNT=L_MISC+2048, L_ONE=L_CNT+64, L_KVX=159744;
constexpr int L_PHI=32768, L_VI=49152, L_WO=98304, L_R2=32768, L_R3=98304, L_R4=65536, L_S=114688, L_SIG=53248, L_PX=131072;
constexpr int w1l(int ks){ return ks<12?98304+ks*4096:ks<20?65536+(ks-12)*4096:32768+(ks-20)*4096; }
constexpr int xs(int c){ return (c%6==0?4:c%6==5?5:c%6-1)*16384; }
constexpr float LOG2E=1.4426950408889634f;
#define DEP 3
#define PZS 3
#define NQE 4
#define W1E 12
#define LA 6
#define R14J 2
#define XR 0
#define ROT 1
constexpr int yng(int c){ return ((c-1+DEP<6?c-1+DEP:6)-c)*3; }
constexpr int nq(int s){ return s+DEP<=6||s<4?0:s==4?(NQE<4?NQE:4):s==5?(NQE>4?NQE-4:0):0; }
constexpr int nqc(int s){ return s<4?0:nqc(s-1)+nq(s); }
#define NOUNR _Pragma("clang loop unroll(disable)")
DI float u2f(u32 v){ return __builtin_bit_cast(float,v); }
DI u32 f2u(float v){ return __builtin_bit_cast(u32,v); }
template<int V> struct IC{ static constexpr int v=V; };
template<int I,int N,class F> DI void sfor(F&& f){ if constexpr(I<N){ f(IC<I>{}); sfor<I+1,N>(f); } }
DI rsrc_t mk(const void* p,u32 bytes){ return __builtin_amdgcn_make_buffer_rsrc((void*)p,0,bytes,0x00020000); }
DI void dma(u32 l,u32 voff,rsrc_t r,u32 soff){ asm volatile("s_mov_b32 m0, %0\n\ts_nop 0\n\tbuffer_load_dwordx4 %1, %2, %3 offen lds"::"s"(l),"v"(voff),"s"(r),"s"(soff):"memory"); }
#define WAITV(n) asm volatile("s_waitcnt vmcnt(" #n ")":::"memory")
#define BAR() asm volatile("s_waitcnt lgkmcnt(0)\n\ts_barrier":::"memory")
#define PIN(x) asm volatile(""::"v"(x))
#define PINW(x) asm volatile("":"+v"(x))
#define SB() __builtin_amdgcn_sched_barrier(0)
DI f32x16 mfma(u32x4 a,u32x4 b,f32x16 c){ return __builtin_amdgcn_mfma_f32_32x32x16_bf16(__builtin_bit_cast(bf16x8,a),__builtin_bit_cast(bf16x8,b),c,0,0,0); }
DI f32x4 m44(u32x2 a,u32x2 b,f32x4 c){ return __builtin_amdgcn_mfma_f32_4x4x4bf16_1k(__builtin_bit_cast(v4i16_t,a),__builtin_bit_cast(v4i16_t,b),c,0,0,0); }
DI u32x2 tr(lptr p){ return __builtin_bit_cast(u32x2,__builtin_amdgcn_ds_read_tr16_b64_v4i16((__attribute__((address_space(3))) v4i16_t*)p)); }
DI u32x4 tr2(lptr p0,lptr p1){ u32x2 a=tr(p0),b=tr(p1); return u32x4{a[0],a[1],b[0],b[1]}; }
DI u32 pk(float lo,float hi){ return __builtin_bit_cast(u32,__builtin_convertvector(f32x2{lo,hi},bf16x2)); }
DI float dot2(u32 a,u32 b,float c){ return __builtin_amdgcn_fdot2_f32_bf16(__builtin_bit_cast(bf16x2,a),__builtin_bit_cast(bf16x2,b),c,false); }
DI float swp_lo(float v){ auto r=__builtin_amdgcn_permlane32_swap(f2u(v),f2u(v),false,false); return u2f(r[0]); }
DI float swp_hi(float v){ auto r=__builtin_amdgcn_permlane32_swap(f2u(v),f2u(v),false,false); return u2f(r[1]); }
DI float swp_add(float v){ auto r=__builtin_amdgcn_permlane32_swap(f2u(v),f2u(v),false,false); return u2f(r[0])+u2f(r[1]); }
DI float swp_max(float v){ auto r=__builtin_amdgcn_permlane32_swap(f2u(v),f2u(v),false,false); return __builtin_fmaxf(u2f(r[0]),u2f(r[1])); }
DI float swp_min(float v){ auto r=__builtin_amdgcn_permlane32_swap(f2u(v),f2u(v),false,false); return __builtin_fminf(u2f(r[0]),u2f(r[1])); }
DI float red8(float v){ v+=u2f(__builtin_amdgcn_update_dpp(0u,f2u(v),0x128,0xf,0xf,false)); auto r=__builtin_amdgcn_permlane16_swap(f2u(v),f2u(v),false,false); return swp_add(u2f(r[0])+u2f(r[1])); }
DI u32x4 pk8(const f32x16& c,int b){ return u32x4{pk(c[b],c[b+1]),pk(c[b+2],c[b+3]),pk(c[b+4],c[b+5]),pk(c[b+6],c[b+7])}; }
DI void dsoftmax(const f32x16& p,float c,u32x4* ph){
  float mx=p[0],mn=p[0];
#pragma unroll
  for(int r=1;r<14;r++){ mx=__builtin_fmaxf(mx,p[r]); mn=__builtin_fminf(mn,p[r]); }
  mx=swp_max(mx); mn=swp_min(mn);
  const float a=-mx*c,b=mn*c; f32x16 e,g; float s=0,u=0;
#pragma unroll
  for(int r=0;r<14;r++){ e[r]=__builtin_amdgcn_exp2f(__builtin_fmaf(p[r],c,a)); g[r]=__builtin_amdgcn_exp2f(__builtin_fmaf(p[r],-c,b)); s+=e[r]; u+=g[r]; }
  s=__builtin_amdgcn_rcpf(swp_add(s)); u=__builtin_amdgcn_rcpf(swp_add(u));
#pragma unroll
  for(int r=0;r<14;r++){ e[r]*=s; g[r]*=u; }
  ph[0]=pk8(e,0); ph[1]=u32x4{pk(e[8],e[9]),pk(e[10],e[11]),pk(e[12],e[13]),0u};
  ph[2]=pk8(g,0); ph[3]=u32x4{pk(g[8],g[9]),pk(g[10],g[11]),pk(g[12],g[13]),0u};
}
DI void dmac(u32 l,u32 voff,rsrc_t r,u32 soff){ asm volatile("s_mov_b32 m0, %0\n\ts_nop 0\n\tbuffer_load_dwordx4 %1, %2, %3 offen sc1 lds"::"s"(l),"v"(voff),"s"(r),"s"(soff):"memory"); }
template<int O> DI void rld(u32x4& v,u32 voff,rsrc_t r,u32 soff){ asm volatile("buffer_load_dwordx4 %0, %1, %2, %3 offen offset:%4 sc1":"+v"(v):"v"(voff),"s"(r),"s"(soff),"n"(O):"memory"); }
template<int O> DI u32x4 ldc(u32 voff,rsrc_t r,u32 soff){ u32x4 v; asm volatile("buffer_load_dwordx4 %0, %1, %2, %3 offen offset:%4 sc1":"=v"(v):"v"(voff),"s"(r),"s"(soff),"n"(O):"memory"); return v; }
DI u32x4 ldp(u32 voff,rsrc_t r,u32 soff){ u32x4 v; asm volatile("buffer_load_dwordx4 %0, %1, %2, %3 offen":"=v"(v):"v"(voff),"s"(r),"s"(soff):"memory"); return v; }
template<int N> DI void waitv(){ asm volatile("s_waitcnt vmcnt(%0)"::"n"(N):"memory"); }
template<int O=0> DI void st16(u32x4 v,rsrc_t r,u32 voff,u32 soff){ asm volatile("buffer_store_dwordx4 %0, %1, %2, %3 offen offset:%4 sc1\n\ts_nop 1"::"v"(v),"v"(voff),"s"(r),"s"(soff),"n"(O):"memory"); }
DI u32 mx3(u32 a,u32 b,u32 c){ u32 r; asm("v_max3_u32 %0, %1, %2, %3":"=v"(r):"v"(a),"v"(b),"v"(c)); return r; }
DI u32 mx4(u32 m,u32x4 g){ return mx3(mx3(m,g[0],g[1]),g[2],g[3]); }
DI bool bad(u32 m){ return __builtin_amdgcn_ballot_w64(m==0xFFFFFFFFu)!=0; }
constexpr int SPIN=30000;
DI void lsig(lptr p){ asm volatile("s_mov_b64 exec, 1\n\tds_add_u32 %0, %1\n\ts_mov_b64 exec, -1"::"v"(p),"v"(1u):"memory"); }
DI void lwait(lptr p,u32 n){ NOUNR for(int it=0;it<SPIN*8;it++){ u32 v; asm volatile("ds_read_b32 %0, %1\n\ts_waitcnt lgkmcnt(0)":"=v"(v):"v"(p):"memory"); if(__builtin_amdgcn_readfirstlane(v)>=n) break; } }
DI float sum8(u32x4 f,float c){ c=dot2(f[0],0x3F803F80u,c); c=dot2(f[1],0x3F803F80u,c); c=dot2(f[2],0x3F803F80u,c); return dot2(f[3],0x3F803F80u,c); }
DI float sq8(u32x4 f,float c){ c=dot2(f[0],f[0],c); c=dot2(f[1],f[1],c); c=dot2(f[2],f[2],c); return dot2(f[3],f[3],c); }
DI void acc8(u32x2 id4,u32x4 g,f32x4& a,f32x4& b){ a=m44(id4,u32x2{g[0],g[1]},a); b=m44(id4,u32x2{g[2],g[3]},b); }
DI u32x4 pk44(f32x4 a,f32x4 b){ return u32x4{pk(a[0],a[1]),pk(a[2],a[3]),pk(b[0],b[1]),pk(b[2],b[3])}; }
DI bool chk(u32x4 g){ return bad(mx3(mx3(g[0],g[1],g[2]),g[3],g[3])); }
template<int N> DI void waitr(u32x4& v){ asm volatile("s_waitcnt vmcnt(%1)":"+v"(v):"n"(N):"memory"); }
template<int A,int B> DI void waitb(u32x4& v,int c){ asm volatile("s_cmp_lg_u32 %1, 0\n\ts_waitcnt vmcnt(%2)\n\ts_cbranch_scc1 1f\n\ts_waitcnt vmcnt(%3)\n1:":"+v"(v):"s"(c),"n"(A),"n"(B):"memory","scc"); }
DI void rldb(u32x4& v,u32 voff,rsrc_t r,u32 soff,int c){ asm volatile("s_cmp_eq_u32 %4, 0\n\ts_cbranch_scc1 1f\n\tbuffer_load_dwordx4 %0, %1, %2, %3 offen sc1\n1:":"+v"(v):"v"(voff),"s"(r),"s"(soff),"s"(c):"memory","scc"); }
DI u32x4 mkpz(){ u32x4 p={~0u,~0u,~0u,~0u}; PINW(p); return p; }
DI u32 wrap(u32 v,u32 n){ return v>=n?v-n:v; }
template<int N,class I,class W,class U,class X,class R> DI int chain(u32x4* g,I&& init,W&& wt,U&& use,X&& ex,R&& re){
  int it=0;
  NOUNR for(;it<SPIN;it++){
    init(); u32 m=0;
    sfor<0,N>([&](auto K){ wt(K); use(K); m=mx4(m,g[K.v]); PINW(m); ex(K); SB(); });
    if(!bad(m)) break;
    WAITV(0); sfor<0,N>(re); WAITV(0); }
  return it; }
#define PRIO(n) __builtin_amdgcn_s_setprio(n)

__global__ __launch_bounds__(512,2) void fused(const u16* __restrict__ x,const u16* __restrict__ wqkv,const u16* __restrict__ wphi,const u16* __restrict__ wo,
    const u16* __restrict__ w1,const u16* __restrict__ b1,const u16* __restrict__ w2,const u16* __restrict__ b2,u16* __restrict__ out,char* __restrict__ ws
    ){
  extern __shared__ __attribute__((aligned(16))) char smem[];
  const lptr L=(lptr)smem;
  const int tid=__builtin_amdgcn_workitem_id_x(),lane=tid&63,w=__builtin_amdgcn_readfirstlane(tid>>6),tt=w&3,ty=w>>2,t=lane&31,hf=lane>>5;
  const int b=__builtin_amdgcn_workgroup_id_x(),i3=(b>>3)&3,jq=b>>5,th=b&1,i=((b&7)>>1)*4+i3;
  const int j1=th*8+jq,odd=j1&1;
  const bool act=jq<7; const int j=th*7+jq,cs=jq;
  STAMP(0);
  asm volatile(""::"s"(x),"s"(wqkv),"s"(wphi),"s"(wo),"s"(w1),"s"(b1),"s"(w2),"s"(b2),"s"(out),"s"(ws));
  const u32 epoch=*(const __attribute__((address_space(4))) u32*)ws;
  const rsrc_t rw=mk(wqkv,448*2688), rx=mk(x+(size_t)i*128*448,128*896);
  const u32 vw=(lane>>2)*2688+(lane&3)*16, hoff=56*j1-8*odd+tt*16*2688, sW=hoff+(ty+1)*896, lW=L_WK+ty*28672+tt*1024;
  const u32 vx0=(lane>>3)*896+(((lane&7)^(lane>>4))<<4), vx1=vx0^64, sX=(tt*32+ty*16)*896, lX=tt*4096+ty*2048;
  const u32 rot=ROT?(u32)(j1+2*i3+XR*(i>>2))%7u:0u;
  auto cc=[&](int c){ return wrap(c+rot,7); };
  auto ch=[&](int c,int u){ const u32 q_=cc(c); if(u==0) dma(lW+c*4096,vw,rw,sW+q_*(64*2688)); else dma(xs(c)+lX+(u-1)*1024,u==1?vx0:vx1,rx,sX+(u-1)*7168+q_*128); };
#define SBAR() asm volatile("s_barrier":::"memory")
  if(w<2) dma(L_MISC+w*1024,tid*16,mk(wphi+j1*784,1568),0);
  const u32 l16=lane*16;
  const rsrc_t ro=mk(ws+WS_O+i*114688,114688), rkv=mk(ws+WS_KV+j1*65536,65536), rh=mk(ws+WS_H+i*114688,114688), rst=mk(ws+WS_ST+i*8192,8192);
  auto ost=[&](u32x4 o0,u32x4 o1){ const u32 ub=t*16+((7*j1)>>1)*512, so=tt*28672;
    st16(o0,ro,ub+(odd+2*hf)*512,so); st16(o1,ro,hf?OOB:ub+(odd+1)*512,so);
    __builtin_amdgcn_raw_buffer_store_b64(u32x2{o1[0],o1[1]},ro,hf?ub+(odd?8:1536):OOB,so,16); };
  STAMP(32);
  sfor<0,DEP>([&](auto C){ if constexpr(C.v>0) SBAR(); ch(C.v,0); ch(C.v,1); ch(C.v,2); STAMP(33+C.v); });
  auto poison=[&]{ const u32x4 PZ=mkpz();
    if(ty){ const int t1=j1>=7; const u32 so=(2*t1+(tt&1))*28672+(4*(j1-7*t1)+(tt>>1))*1024, v=j1<14?l16:OOB; st16(PZ,rh,v,so); st16<2048>(PZ,rh,v,so);
      st16(PZ,rst,lane<8?l16:OOB,(j1*4+tt)*128); }
    else { st16(PZ,rkv,l16,i*4096+tt*1024); ost(PZ,PZ); } };
  if(tid<3) LDS(u32x4,L+L_CNT+tid*16)=u32x4{0,0,0,0};
  if(tid<2) LDS(u32x2,L+L_ONE+tid*256)=u32x2{0x3F80u,0u};
  const int tt2=w&1, kq=w>>1;
  const int q4=(lane&15)>>2,mu=((lane>>4)&1)*4+(lane&3),sg=4*(mu&1)+(mu>>1);
  const int rowo=(8*hf+q4)*64, kslot=(mu+odd)&7;
  lptr aW[4],aQ[4],xb[4];
  { const int sw=hf^((t>>1)&7), sl=ty?(odd?(sg==7?0:((sg+1)%7)+1):sg):kslot;
#pragma unroll
    for(int u=0;u<4;u++){ const int r=(u+tt+2*ty)&3; aW[u]=mu==7?L+L_ONE:L+L_WK+ty*28672+rowo+sl*8+r*1024; aQ[u]=L+rowo+kslot*8+r*1024; xb[u]=L+(tt*32+t)*128+((sw^(r*2))<<4); PINW(aW[u]); PINW(xb[u]); } }
  u32 tag=epoch*0x9E3779B1u+0x7F4A7C15u; asm volatile("":"+s"(tag));
  const rsrc_t r1=mk(w1,act?448*3584:0), r2=mk(w2,act?1792*896:0), r0=mk(wo,act?448*896:0);
  const lptr cnt=L+L_CNT;
  const rsrc_t rf=mk(ws,1u<<20); const u32 vfl=lane<16?WS_FLAG+(j1*16+lane)*128:lane<32?WS_FLAG+((lane-16)*16+i)*128:OOB; u32 flv;
  STAMP(43);
  u32 stp=mu==7?0:4096; PINW(stp);
  const u32 tq2=(tt+2)&3, hq=56*j1-8*odd+tq2*16*2688;
  auto wq=[&](int u){ dma(tq2*1024+u*4096,vw,rw,hq+cc(u)*(64*2688)); };
  u32x4 xf[28],wp[2],fw[2][4]; f32x16 c1={}; float ss=0,s2=0,cz=0;
  auto rd=[&](auto C,int u){ constexpr int c=C.v; fw[c&1][u]=tr2(aW[u],aW[u]+256); xf[4*c+u]=LDS(u32x4,xb[u]+xs(c)); if(c<6) aW[u]+=stp; };
  auto md=[&](auto C,int u){ constexpr int c=C.v; c1=mfma(fw[c&1][u],xf[4*c+u],c1); PINW(c1); SB(); };
  auto dd=[&](auto C,int u){ constexpr int c=C.v;
    if(u==0){ cz=sum8(fw[c&1][0],cz); PINW(cz); } else if(u==1){ ss=sq8(xf[4*c],ss); PINW(ss); } else if(u==2){ s2=sq8(xf[4*c+1],s2); PINW(s2); } };
  sfor<0,7>([&](auto C){ constexpr int c=C.v;
    constexpr int pz=c>PZS&&c<PZS+DEP, fl=c>PZS+DEP;
    if(ty) waitv<yng(c)+3*pz+nqc(c-1)>(); else if(fl&&w==2) waitv<yng(c)+4*pz+1>(); else waitv<yng(c)+4*pz>();
    if constexpr(c<3) STAMP(44+c);
    if constexpr(c==0) BAR(); else SBAR();
    if constexpr(c==0) STAMP(1); else STAMP(25+c);
    if constexpr(c==PZS+DEP){ if(tid==128) __hip_atomic_store((u32*)(ws+WS_FLAG+(j1*16+i)*128),tag,__ATOMIC_RELAXED,__HIP_MEMORY_SCOPE_AGENT); }
    if constexpr(c==6){ if(!ty) PRIO(2); }
    if constexpr(c==PZS) poison();
    sfor<0,4>([&](auto U){ constexpr int u=U.v;
      if constexpr(c>0&&c<6) md(IC<c-1>{},u);
      rd(C,u);
      if constexpr(c+DEP<=6&&u<3) ch(c+DEP,u);
      if constexpr(u<nq(c)){ if(ty) wq(nqc(c-1)+u); }
      if constexpr(c>0&&c<6) dd(IC<c-1>{},u);
      SB(); });
    if constexpr(c==0){
      const bool mv=(t&26)!=26; const lptr p=L+L_MISC+(t<26?t:t-2)*2+hf*224; u32 e[16];
#pragma unroll
      for(int q=0;q<16;q++){ const int d=16*(q>>3)+8*((q>>2)&1)+(q&3); e[q]=LDS(u16,p+d*56); }
#pragma unroll
      for(int q=0;q<8;q++){ u32 v=e[2*q]|(e[2*q+1]<<16); v=(mv&&(q<6||hf==0))?v:0u; asm volatile("":"+v"(v)); wp[q>>2][q&3]=v; } SB(); }
    if constexpr(c==5) sfor<0,4>([&](auto U){ md(C,U.v); dd(C,U.v); SB(); });
  });
  md(IC<6>{},0); dd(IC<6>{},0); dd(IC<6>{},1); SB(); md(IC<6>{},1); dd(IC<6>{},2);
  LDS(float,L+L_KVX+w*256+lane*4)=ss+s2; LDS(float,L+L_KVX+2048+ty*1024+l16+tt*4)=cz; SB();
  md(IC<6>{},2); md(IC<6>{},3);
  BAR();
  { const f32x4 z=LDS(f32x4,L+L_KVX+2048+ty*1024+l16); const float so_=LDS(float,L+L_KVX+(w^4)*256+lane*4);
    ss=(ss+s2)+so_; cz=(z[0]+z[1])+(z[2]+z[3]); }
  const float nmean=swp_hi(c1[12])*(-1.f/448);
  c1=__builtin_amdgcn_mfma_f32_32x32x2f32(cz,nmean,c1,0,0,0);
  const float rstd=__builtin_amdgcn_rsqf(__builtin_fmaxf(__builtin_fmaf(swp_add(ss),1.f/448,-nmean*nmean),0.f)+1e-5f);
  STAMP(2);
  const int wro=(tt*4096+t*64),c0=((2*hf)^((t>>2)&3))<<4;
  const rsrc_t rhh=mk(ws+WS_HH+i*458752,act?458752:0);
  const u32 v8=(lane>>3), p8=((lane&7)^(lane>>2&4))<<4, vw2=v8*896+p8, sW2=w*224*896+cs*128;
  const u32 r7=ROT?(u32)(((7*i3)>>2)+jq+XR*(i>>3))%7u:0u, r14=ROT?(u32)(((7*i3)>>1)+R14J*jq+XR*(i>>2))%14u:0u;
#define W2D(l,n,o) do{ const u32 s_=sW2+(o)+wrap((n)+r14,14)*14336; dma((l),vw2,r2,s_); dma((l)+1024,vw2,r2,s_+7168); }while(0)
  const int nq=(mu&4)|((mu&1)<<1)|((mu>>1)&1);
  const int ao=(8*hf+q4)*128+(q4>>1)*64+nq*8; const lptr a0=L+ao,a1=L+(ao^64);
  auto mkid=[&]{ u32 l=l16; PINW(l); const u32 v=(l&16)?0x3F800000u:0x3F80u; return u32x2{(l&32)?0u:v,(l&32)?v:0u}; };
  const u32 vo=(lane&7)*896+v8*16, so_=((2*th+tt2)*32+kq*8)*896+cs*128, vf=v8*512+(lane&7)*16+kq*128;
  const u32 vw1=v8*3584+p8;
  auto w1d=[&](int n,int q){ dma(w1l(n)+q*1024,vw1,r1,(q&1)*8*3584+j*256+(q>>1)*128+(wrap((n>>2)+r7,7)*4+(n&3))*16*3584); };
  u32x4 phq[4],w1f[28],w2f[14][2],xr; u32 b1v;
  auto pzh=[&]{ const u32x4 PZ=mkpz(); const u32 sh=(8*j+2*tt)*1024;
    sfor<0,4>([&](auto U){ st16(PZ,rhh,l16,sh+U.v*114688); st16<1024>(PZ,rhh,l16,sh+U.v*114688); }); };
  auto misc=[&]{ xr=ldp(act?vo:OOB,rx,so_);
    const int tq=t>>2; asm volatile("buffer_load_ushort %0, %1, %2, %3 offen":"=v"(b1v):"v"((((tq&4)|((tq&1)<<1)|((tq>>1)&1))*4+(t&3))*2),"s"(mk(b1,act?3584:0)),"s"(j*256+tt*64):"memory"); };
  auto rdw1=[&](auto P){ lptr q[4]; constexpr int n0=P.v?12:0, n1=P.v?20:8, n2=P.v?28:12;
#pragma unroll
    for(int u=0;u<4;u++){ q[u]=L+w1l(n0)+((tt&1)?ao^64:ao)+(tt>>1)*2048+((u+tt)&3)*4096; PINW(q[u]); }
#pragma unroll
    for(int n=n0;n<n1;n++) w1f[n]=tr2(q[n&3]+(w1l(n&~3)-w1l(n0)),q[n&3]+(w1l(n&~3)-w1l(n0))+512);
#pragma unroll
    for(int u=0;u<4;u++){ q[u]+=P.v?-32768:32768; PINW(q[u]); }
#pragma unroll
    for(int n=n1;n<n2;n++) w1f[n]=tr2(q[n&3]+(w1l(n&~3)-w1l(n1)),q[n&3]+(w1l(n&~3)-w1l(n1))+512); };
  auto rdw1a=[&]{ lwait(cnt+20,4); rdw1(IC<0>{}); asm volatile("s_waitcnt lgkmcnt(0)":::"memory"); lsig(cnt+24); };
  if(ty==0){
    f32x16 p=mfma(wp[0],pk8(c1,0),f32x16{}); p=mfma(wp[1],pk8(c1,8),p);
    u32x4 ph[4]; dsoftmax(p,rstd*LOG2E,ph);
    LDS(u32x4,L+L_PHI+wro+c0)=ph[0]; LDS(u32x4,L+L_PHI+wro+(c0^16))=ph[1];
    LDS(u32x4,L+L_PHI+2048+wro+c0)=ph[2]; LDS(u32x4,L+L_PHI+2048+wro+(c0^16))=ph[3];
    lsig(cnt);
    STAMP(3);
    if(tt<2){
      lwait(cnt,8);
      const int ko=(8*hf+q4)*64+(sg&1)*8; const lptr k0=L+ko+(((sg>>1)^(2*hf))<<4),k1=L+ko+256+(((sg>>1)^(2*hf+1))<<4);
      f32x16 c={}; u32x4 fa[8],fb[8];
#pragma unroll
      for(int u=0;u<8;u++){ const int o=(u>>1)*4096+(u&1)*1024; fa[u]=tr2(k0+L_PHI+tt*2048+o,k1+L_PHI+tt*2048+o); fb[u]=tr2(k0+L_VI+o,k1+L_VI+o); }
      SB();
#pragma unroll
      for(int u=0;u<8;u++) c=mfma(fa[u],fb[u],c);
      st16(pk8(c,0),rkv,l16,i*4096+tt*2048); st16<1024>(pk8(c,8),rkv,l16,i*4096+tt*2048);
      lsig(cnt+8);
      STAMP(4);
    } else {
      if(tt==2) asm volatile("buffer_load_dword %0, %1, %2, 0 offen sc1":"=v"(flv):"v"(vfl),"s"(rf):"memory");
      sfor<W1E,20>([&](auto K){ w1d(K.v,2*(tt&1)); w1d(K.v,2*(tt&1)+1); });
      if(tt==2){
        asm volatile("s_waitcnt vmcnt(%1)":"+v"(flv):"n"(2*(20-W1E)):"memory");
        NOUNR for(int it=0;it<SPIN;it++){
          if(__builtin_amdgcn_ballot_w64(lane<32&&flv!=tag)==0) break;
          asm volatile("buffer_load_dword %0, %1, %2, 0 offen sc1\n\ts_waitcnt vmcnt(0)":"+v"(flv):"v"(vfl),"s"(rf):"memory"); }
        lsig(cnt+12); } }
    lwait(cnt+8,2);
    STAMP(5);
    sfor<20,28>([&](auto K){ w1d(K.v,tt); });
    if(tt<2){ WAITV(8); lsig(cnt+12); }
    STAMP(6);
    lwait(cnt+12,3);
    STAMP(8);
    u32x4 g[16]; const u32 rk=4*i3;
    sfor<0,16>([&](auto N){ g[N.v]=ldc<0>(l16,rkv,((N.v+rk)&15)*4096+tt*1024); });
    STAMP(42);
    rdw1a();
    WAITV(16); lsig(cnt+28);
    misc();
    STAMP(7);
    f32x4 s[4]; const u32x2 id4=mkid();
    const int rr0=chain<16>(g,[&]{ s[0]=s[1]=s[2]=s[3]=f32x4{}; },[&](auto N){ waitr<17-N.v>(g[N.v]); },[&](auto N){ acc8(id4,g[N.v],s[2*(N.v&1)],s[2*(N.v&1)+1]); PINW(s[2*(N.v&1)]); PINW(s[2*(N.v&1)+1]); },[](auto){},
      [&](auto N){ rld<0>(g[N.v],l16,rkv,((N.v+rk)&15)*4096+tt*1024); });
    CNT(37,rr0);
    LDS(u32x4,L+L_KVX+tt*1024+l16)=pk44(s[0]+s[2],s[1]+s[3]);
    PRIO(0);
  } else {
#pragma unroll
    for(int r=0;r<16;r++) c1[r]*=rstd;
    u32x4 v0=pk8(c1,0),v1=pk8(c1,8); if(hf){ v1[2]=0x3F80u; v1[3]=0u; }
    LDS(u32x4,L+L_VI+wro+c0)=v0; LDS(u32x4,L+L_VI+wro+(c0^16))=v1;
    lsig(cnt);
    STAMP(3);
    sfor<nqc(5),7>([&](auto U){ wq(U.v); });
    sfor<0,W1E>([&](auto K){ w1d(K.v,tt); });
    STAMP(40);
    waitv<W1E>(); lsig(cnt+4);
    u32x4 fq[28]; float cq=0;
#pragma unroll
    for(int u=0;u<7;u++) fq[4*u]=tr2(aQ[0]+u*4096,aQ[0]+u*4096+256);
    lwait(cnt+4,4);
    STAMP(4);
    auto ord=[](int s){ return s<7?4*s:((s-7)/3)*4+1+(s-7)%3; };
    f32x16 cQ={};
    sfor<0,28>([&](auto S){ constexpr int s=S.v;
      cQ=mfma(fq[ord(s)],xf[ord(s)],cQ); PINW(cQ); SB();
      if constexpr(s<21){ constexpr int n=ord(s+7); fq[n]=tr2(aQ[n&3]+(n>>2)*4096,aQ[n&3]+(n>>2)*4096+256); }
      if constexpr(s<7){ cq=sum8(fq[4*s],cq); PINW(cq); }
      if constexpr(s==7) LDS(float,L+L_MISC+l16+tt*4)=cq;
      if constexpr(s==22) lsig(cnt+16);
      SB(); });
    lwait(cnt+16,4);
    const f32x4 z=LDS(f32x4,L+L_MISC+l16);
    cQ=__builtin_amdgcn_mfma_f32_32x32x2f32((z[0]+z[1])+(z[2]+z[3]),nmean,cQ,0,0,0);
    f32x16 p=mfma(wp[0],pk8(cQ,0),f32x16{}); p=mfma(wp[1],pk8(cQ,8),p); PINW(p);
    STAMP(5);
    dsoftmax(p,rstd*LOG2E,phq);
    PINW(phq[0]); PINW(phq[1]); PINW(phq[2]); PINW(phq[3]);
    WAITV(0); lsig(cnt+20);
    STAMP(6);
    rdw1a();
    lwait(cnt+28,4); rdw1(IC<1>{});
    lwait(cnt+24,8);
    STAMP(42);
    misc();
    { const u32 rw_=ROT?((7*i3)>>1)+XR*(i>>2):0; sfor<0,14>([&](auto U){ const u32 u=tt*14+wrap(U.v+rw_,14); dma(L_WO+u*1024,vw2,r0,u*7168+cs*128); }); }
    STAMP(7);
    PRIO(2);
  }
  BAR();
  STAMP(9);
  if(ty){ f32x16 c={};
#pragma unroll
    for(int n=0;n<4;n++) c=mfma(LDS(u32x4,L+L_KVX+n*1024+l16),phq[n],c);
    const float rd=__builtin_amdgcn_rcpf(swp_hi(c[12])+1e-6f);
#pragma unroll
    for(int r=0;r<16;r++) c[r]*=rd;
    ost(pk8(c,0),pk8(c,8));
    PRIO(0);
    WAITV(3); lsig(cnt+32);
  }
  else rdw1(IC<1>{});
  STAMP(10);
  if(act){
  { const u32 so=(2*th+tt2)*28672+kq*7168;
    lwait(cnt+32,4);
    if(!ty){ pzh(); W2D(w*4096,0,0); W2D(w*4096+2048,1,0); W2D(w*4096+16384,0,4*224*896); W2D(w*4096+18432,1,4*224*896); }
    lptr p0=a0+L_WO+kq*14336,p1=a1+L_WO+kq*14336; PINW(p0); PINW(p1);
    u32x4 fa[7][2]; f32x16 c[2];
#pragma unroll
    for(int n=0;n<7;n++){ fa[n][0]=tr2(p0+n*2048,p0+n*2048+512); fa[n][1]=tr2(p1+n*2048,p1+n*2048+512); PINW(fa[n][0]); PINW(fa[n][1]); }
    SB();
    STAMP(11);
    if(ty) WAITV(0); else WAITV(16);
    asm volatile("":"+v"(xr),"+v"(b1v)::"memory");
    BAR();
    STAMP(12);
    u32x4 of[7];
    sfor<0,7>([&](auto N){ of[N.v]=ldc<(N.v&3)*1024>(l16,ro,so+(N.v>>2)*4096); });
    auto r2d=[&](int n){ dma(L_R2+(n>>1)*16384+w*2048+(n&1)*1024,vw2,r2,sW2+wrap(2+(n>>1)+r14,14)*14336+(n&1)*7168); };
    STAMP(41);
    const int rr1=chain<7>(of,[&]{ c[0]=c[1]=f32x16{}; },[&](auto N){ waitr<6>(of[N.v]); },[&](auto N){ c[0]=mfma(fa[N.v][0],of[N.v],c[0]); PINW(c[0]); c[1]=mfma(fa[N.v][1],of[N.v],c[1]); PINW(c[1]); },[&](auto N){ r2d(N.v); if(N.v==6) r2d(7); },
      [&](auto N){ rld<(N.v&3)*1024>(of[N.v],l16,ro,so+(N.v>>2)*4096); });
    SB();
    CNT(38,rr1);
    lptr e=L+L_PX+w*4096+l16;
    LDS(u32x4,e)=pk8(c[0],0); LDS(u32x4,e+1024)=pk8(c[0],8); LDS(u32x4,e+2048)=pk8(c[1],0); LDS(u32x4,e+3072)=pk8(c[1],8);
    STAMP(13);
    BAR();
    e=L+L_PX+tt2*4096+vf;
    const u32x2 id4=mkid(); f32x4 s[4]={}; acc8(id4,xr,s[0],s[1]);
#pragma unroll
    for(int q=0;q<4;q++) acc8(id4,LDS(u32x4,e+q*8192),s[2*(q&1)],s[2*(q&1)+1]);
    s[0]+=s[2]; s[1]+=s[3];
    st16(pk44(s[0],s[1]),rh,vf,(2*th+tt2)*28672+cs*4096);
    float s1=((s[0][0]+s[0][1])+(s[0][2]+s[0][3]))+((s[1][0]+s[1][1])+(s[1][2]+s[1][3])), s2=s[0][0]*s[0][0], s3=s[1][0]*s[1][0];
#pragma unroll
    for(int q=1;q<4;q++){ s2=__builtin_fmaf(s[0][q],s[0][q],s2); s3=__builtin_fmaf(s[1][q],s[1][q],s3); }
    s1=red8(s1); s2=red8(s2+s3);
    asm volatile("buffer_store_dwordx2 %0, %1, %2, %3 offen sc1"::"v"(f32x2{s1,s2}),"v"(lane<8?lane*64:OOB),"s"(rst),"s"(((2*th+tt2)*32+kq*8)*64+cs*8):"memory");
    SB(); }
  STAMP(14);
  if(b==0&&tid==0) __hip_atomic_store((u32*)ws,tag,__ATOMIC_RELAXED,__HIP_MEMORY_SCOPE_AGENT);
  const u32 bX=ty?tt*6144:L_S+tt*12288, bY=ty?28672+tt*6144:bX+6144;
  W2D(L_R3+w*2048,6,0);
  { lptr p0=a0+w*4096,p1=a1+w*4096;
#pragma unroll
    for(int n=0;n<2;n++){ w2f[n][0]=tr2(p0+n*2048,p0+n*2048+512); w2f[n][1]=tr2(p1+n*2048,p1+n*2048+512); } }
  WAITV(4);
  { lptr p0=a0+L_R2+w*2048,p1=a1+L_R2+w*2048; PINW(p0); PINW(p1);
#pragma unroll
    for(int n=0;n<4;n++){ w2f[2+n][0]=tr2(p0+n*16384,p0+n*16384+512); w2f[2+n][1]=tr2(p1+n*16384,p1+n*16384+512); }
    asm volatile("s_waitcnt lgkmcnt(0)":"+v"(w2f[4][0]),"+v"(w2f[4][1])::"memory"); }
  W2D(L_R4+w*2048,7,0);
  WAITV(4);
  BAR();
  STAMP(15);
  const u32 sH=ty*57344+tt*1024, lH0=ty*28672+tt*1024, lH1=(ty?57344:86016)+tt*1024;
  if(ty) __builtin_amdgcn_s_sleep(4);
  u32x4 hb[28],sa,sb; float ks[2][5]; const u32 vst=t*64+hf*32;
  auto dm1=[&](u32 l,int k,int u){ dmac(l+u*4096,l16,rh,sH+k*28672+wrap(u+r7,7)*4096); };
  auto dmas=[&](u32 l,int k){ sfor<0,7>([&](auto U){ dm1(l,k,U.v); }); };
  dmas(lH0,0); sa=ldc<0>(vst,rst,ty*4096); sb=ldc<16>(vst,rst,ty*4096);
  float cb=0;
  if(ty){
#pragma unroll
    for(int n=14;n<28;n++) cb=sum8(w1f[n],cb);
    asm volatile("; b":"+v"(cb));
  } else {
#pragma unroll
    for(int n=0;n<14;n++) cb=sum8(w1f[n],cb);
    asm volatile("; a":"+v"(cb)); }
  LDS(float,L+(ty?86016:57344)+tt*1024+lane*4)=cb; SB();
  STAMP(36);
  sfor<0,3>([&](auto U){ W2D(L_S+tt*12288+ty*6144+U.v*2048,8+3*ty+U.v,-ty*4*224*896); });
  WAITV(15);
  { lptr p0=a0+L_R4+w*2048,p1=a1+L_R4+w*2048; PINW(p0); PINW(p1); w2f[7][0]=tr2(p0,p0+512); w2f[7][1]=tr2(p1,p1+512);
    w2f[6][0]=tr2(p0+(L_R3-L_R4),p0+(L_R3-L_R4)+512); w2f[6][1]=tr2(p1+(L_R3-L_R4),p1+(L_R3-L_R4)+512); }
  auto vrd=[&](u32 l){ lptr p=L+l+l16; PINW(p);
#pragma unroll
    for(int n=0;n<7;n++) hb[4*n]=LDS(u32x4,p+n*4096);
    if(hf){ sb[2]=0; sb[3]=0; } };
  auto vbad=[&]{ u32 m=mx4(mx4(0,sa),sb);
#pragma unroll
    for(int n=0;n<7;n++) m=mx4(m,hb[4*n]);
    return bad(m); };
  auto vfix=[&](u32 l,int k){
    NOUNR for(int it=0;it<SPIN;it++){
      sfor<0,7>([&](auto U){ if(chk(hb[4*U.v])) dm1(l,k,U.v); });
      if(bad(mx4(mx4(0,sa),sb))){ rld<0>(sa,vst,rst,ty*4096+k*2048); rld<16>(sb,vst,rst,ty*4096+k*2048); }
      asm volatile("s_waitcnt vmcnt(0)":"+v"(sa),"+v"(sb)::"memory");
      vrd(l); if(!vbad()) break; } };
  auto vst_=[&](float* o){
    const float s1=swp_add((u2f(sa[0])+u2f(sa[2]))+(u2f(sb[0])+u2f(sb[2])))*(-1.f/448), s2=swp_add((u2f(sa[1])+u2f(sa[3]))+(u2f(sb[1])+u2f(sb[3])));
    const float g_=__builtin_fmaf(__builtin_fmaxf(__builtin_fmaf(s2,1.f/448,-s1*s1),0.f),1.00001f,1e-10f);
    const float rs_=__builtin_amdgcn_rsqf(g_); o[3]=0.5f*__builtin_amdgcn_logf(g_);
    o[0]=g_*rs_; o[1]=-2.302208198f*rs_; o[2]=-0.1029432397f*rs_*rs_*rs_; o[4]=hf?o[0]:s1; };
  auto vpin=[&](float* o){ PINW(o[0]); PINW(o[1]); PINW(o[2]); PINW(o[3]); PINW(o[4]); };
  if(ty) BAR();
  asm volatile("s_waitcnt vmcnt(6)":"+v"(sa),"+v"(sb)::"memory"); vrd(lH0); vst_(ks[0]); if(vbad()){ vfix(lH0,0); vst_(ks[0]); } vpin(ks[0]);
  if(!ty) BAR();
  STAMP(16);
  cb=swp_add(cb+LDS(float,L+lH1+lane*4)); cb=hf?u2f(b1v<<16):cb; PINW(cb);
  dmas(lH1,1); sa=ldc<0>(vst,rst,ty*4096+2048); sb=ldc<16>(vst,rst,ty*4096+2048);
  if(ty) BAR();
  sfor<0,2>([&](auto KK){ constexpr int k=KK.v;
    lptr q[4];
#pragma unroll
    for(int u=1;u<4;u++){ q[u]=L+(k?lH1:lH0)+(((u+tt)&3)-tt)*1024+l16; PINW(q[u]); }
    f32x16 c={};
    auto ord=[](int s){ return s<7?4*s:((s-7)/3)*4+1+(s-7)%3; };
    PRIO(1);
    sfor<0,28>([&](auto S){ constexpr int s=S.v;
      c=mfma(w1f[ord(s)],hb[ord(s)],c); PINW(c); SB();
      if constexpr(s+LA>=7&&s+LA<28) hb[ord(s+LA)]=LDS(u32x4,q[ord(s+LA)&3]+(ord(s+LA)>>2)*4096);
      if constexpr(k==0&&s%8==2&&s<24){ if(ty) W2D(bX+(s/8)*2048,8+s/8,0); }
      SB(); });
    c=__builtin_amdgcn_mfma_f32_32x32x2f32(cb,ks[k][4],c,0,0,0); PINW(c);
    PRIO(0);
    SB(); if(k==0||!ty) BAR(); PINW(c); SB();
    STAMP(17+2*k);
    if constexpr(k==0){ if(ty){ W2D(bY,11,0); W2D(bY+2048,12,0); W2D(bY+4096,13,0); if(tid==256) LDS(u32,L+L_SIG)=0; WAITV(12); } else WAITV(0);
      asm volatile("":"+v"(sa),"+v"(sb)::"memory"); vrd(lH1); vst_(ks[1]); }
    u32 g[8]; const float sg_=ks[k][0],K1=ks[k][1],K2=ks[k][2],Lg=ks[k][3];
#pragma unroll
    for(int p=0;p<8;p++){ float a=c[2*p],d=c[2*p+1];
      float u=__builtin_fmaf(a,__builtin_fmaf(a*a,K2,K1),Lg), v=__builtin_fmaf(d,__builtin_fmaf(d*d,K2,K1),Lg);
      u=__builtin_amdgcn_rcpf(__builtin_amdgcn_exp2f(u)+sg_); v=__builtin_amdgcn_rcpf(__builtin_amdgcn_exp2f(v)+sg_);
      g[p]=pk(a*u,d*v);
      if(p==3) st16(u32x4{g[0],g[1],g[2],g[3]},rhh,l16,(2*ty+k)*114688+(8*j+2*tt)*1024); }
    st16<1024>(u32x4{g[4],g[5],g[6],g[7]},rhh,l16,(2*ty+k)*114688+(8*j+2*tt)*1024);
    if constexpr(k==0){ if(vbad()){ vfix(lH1,1); vst_(ks[1]); } vpin(ks[1]);
      BAR(); STAMP(18); }
    SB();
  });
  { const u32 s5=th*229376+w*14336;
    auto su=[&](int n){ return s5+wrap(n+r14,14)*1024; };
    u32x4 hh[14],hp,q2;
    auto rdw2=[&]{ lptr p0=a0+bX,p1=a1+bX; PINW(p0); PINW(p1);
#pragma unroll
      for(int n=0;n<3;n++){ w2f[8+n][0]=tr2(p0+n*2048,p0+n*2048+512); w2f[8+n][1]=tr2(p1+n*2048,p1+n*2048+512); }
      p0=a0+bY; p1=a1+bY; PINW(p0); PINW(p1);
#pragma unroll
      for(int n=0;n<3;n++){ w2f[11+n][0]=tr2(p0+n*2048,p0+n*2048+512); w2f[11+n][1]=tr2(p1+n*2048,p1+n*2048+512); } };
    STAMP(20);
    sfor<0,14>([&](auto N){ hh[N.v]=ldc<0>(l16,rhh,su(N.v)); });
    WAITV(18); rdw2();
#pragma unroll
    for(int n=8;n<14;n++){ PINW(w2f[n][0]); PINW(w2f[n][1]); }
    if(ty){ WAITV(14); lsig(L+L_SIG); }
    STAMP(21);
    f32x16 c[2]; auto zc=[&]{ c[0]=c[1]=f32x16{}; };
    auto mm=[&](auto N){ c[0]=mfma(w2f[N.v][0],hh[N.v],c[0]); PINW(c[0]); c[1]=mfma(w2f[N.v][1],hh[N.v],c[1]); PINW(c[1]); };
    const int rr2=chain<14>(hh,zc,[&](auto N){ waitb<13,13-N.v>(hh[N.v],ty); },mm,[&](auto N){ rldb(hh[N.v],l16,rhh,su(N.v)+114688,ty); },
      [&](auto N){ rld<0>(hh[N.v],l16,rhh,su(N.v)); });
    STAMP(22); CNT(39,rr2);
    lptr e=L+bX+l16; PINW(e);
    LDS(u32x4,e)=pk8(c[0],0); LDS(u32x4,e+1024)=pk8(c[0],8); LDS(u32x4,e+2048)=pk8(c[1],0); LDS(u32x4,e+3072)=pk8(c[1],8);
    if(!ty){ lwait(L+L_SIG,4); sfor<0,14>([&](auto N){ rld<0>(hh[N.v],l16,rhh,su(N.v)+114688); }); }
    hp=ldc<0>(vf,rh,(2*th+tt2)*28672+cs*4096); q2=ldp(v8*16,mk(b2,896),cs*128);
    STAMP(23);
    const int rr3=chain<14>(hh,zc,[&](auto N){ waitr<15-N.v>(hh[N.v]); },mm,[](auto){},
      [&](auto N){ rld<0>(hh[N.v],l16,rhh,su(N.v)+114688); });
    STAMP(24); CNT(47,rr3);
    e=L+bY+l16; PINW(e);
    LDS(u32x4,e)=pk8(c[0],0); LDS(u32x4,e+1024)=pk8(c[0],8); LDS(u32x4,e+2048)=pk8(c[1],0); LDS(u32x4,e+3072)=pk8(c[1],8);
    asm volatile("s_waitcnt vmcnt(0)":"+v"(q2),"+v"(hp)::"memory");
    BAR();
    lptr e0=L+tt2*28672+vf,e1=L+L_S+tt2*6144+vf; PINW(e0); PINW(e1);
    const u32x2 id4=mkid(); f32x4 s[4]={}; acc8(id4,hp,s[0],s[1]); acc8(id4,q2,s[2],s[3]);
#pragma unroll
    for(int q=0;q<4;q++){ acc8(id4,LDS(u32x4,e0+q*6144),s[0],s[1]); acc8(id4,LDS(u32x4,e1+q*12288),s[2],s[3]); }
    __builtin_amdgcn_raw_buffer_store_b128(pk44(s[0]+s[2],s[1]+s[3]),mk(out+(size_t)i*128*448,128*896),vo,so_,0); }
  }
  STAMP(25);
}
}
#ifdef __HIP_DEVICE_COMPILE__
#pragma clang attribute pop
#endif
extern "C" void kernel_launch(void* const* d_in,const int* in_sizes,int n_in,void* d_out,int out_size,void* d_ws,size_t ws_size,hipStream_t stream){
  typedef const unsigned short* P;
  (void)hipFuncSetAttribute((const void*)fk::fused,hipFuncAttributeMaxDynamicSharedMemorySize,163840);
  fk::fused<<<256,512,163840,stream>>>((P)d_in[0],(P)d_in[1],(P)d_in[2],(P)d_in[3],(P)d_in[5],(P)d_in[6],(P)d_in[7],(P)d_in[8],(unsigned short*)d_out,(char*)d_ws);
}
```

```cpp
#define PROBE2 1
#define PBLK 0
#define PSEL 9
#define PMUL 4
#include <hip/hip_runtime.h>
#include <stdint.h>
#ifdef __HIP_DEVICE_COMPILE__
#pragma clang attribute push(__attribute__((target("no-packed-fp32-ops"))),apply_to=function)
#endif
namespace fk {
typedef unsigned short u16; typedef unsigned u32;
typedef __attribute__((ext_vector_type(4))) u32 u32x4;
typedef __attribute__((ext_vector_type(2))) u32 u32x2;
typedef __attribute__((ext_vector_type(8))) __bf16 bf16x8;
typedef __attribute__((ext_vector_type(2))) __bf16 bf16x2;
typedef __attribute__((ext_vector_type(2))) float f32x2;
typedef __attribute__((ext_vector_type(4))) float f32x4;
typedef __attribute__((ext_vector_type(16))) float f32x16;
typedef short v4i16_t __attribute__((ext_vector_type(4)));
typedef __attribute__((address_space(3))) char* lptr;
typedef __amdgpu_buffer_rsrc_t rsrc_t;
#define DI __device__ __forceinline__
#define LDS(T,a) (*(__attribute__((address_space(3))) T*)(a))
#define STAMP(n)
#define CNT(n,v)
#define CST(n)
constexpr u32 WS_FLAG=4096, WS_ST=512u<<10, WS_KV=1u<<20, WS_O=2u<<20, WS_H=4u<<20, WS_HH=8u<<20, OOB=0x80000000u;
constexpr int L_WK=98304, L_MISC=155648, L_CNT=L_MISC+2048, L_ONE=L_CNT+64, L_KVX=159744;
constexpr int L_PHI=32768, L_VI=49152, L_WO=98304, L_R2=32768, L_R3=98304, L_R4=65536, L_S=114688, L_SIG=53248, L_PX=131072;
constexpr int w1l(int ks){ return ks<12?98304+ks*4096:ks<20?65536+(ks-12)*4096:32768+(ks-20)*4096; }
constexpr int xs(int c){ return (c%6==0?4:c%6==5?5:c%6-1)*16384; }
constexpr float LOG2E=1.4426950408889634f;
#define DEP 3
#define PZS 3
#define NQE 4
#define W1E 12
#define LA 6
#define R14J 2
#define XR 0
#define ROT 1
constexpr int yng(int c){ return ((c-1+DEP<6?c-1+DEP:6)-c)*3; }
constexpr int nq(int s){ return s+DEP<=6||s<4?0:s==4?(NQE<4?NQE:4):s==5?(NQE>4?NQE-4:0):0; }
constexpr int nqc(int s){ return s<4?0:nqc(s-1)+nq(s); }
#define NOUNR _Pragma("clang loop unroll(disable)")
DI float u2f(u32 v){ return __builtin_bit_cast(float,v); }
DI u32 f2u(float v){ return __builtin_bit_cast(u32,v); }
template<int V> struct IC{ static constexpr int v=V; };
template<int I,int N,class F> DI void sfor(F&& f){ if constexpr(I<N){ f(IC<I>{}); sfor<I+1,N>(f); } }
DI rsrc_t mk(const void* p,u32 bytes){ return __builtin_amdgcn_make_buffer_rsrc((void*)p,0,bytes,0x00020000); }
DI void dma(u32 l,u32 voff,rsrc_t r,u32 soff){ asm volatile("s_mov_b32 m0, %0\n\ts_nop 0\n\tbuffer_load_dwordx4 %1, %2, %3 offen lds"::"s"(l),"v"(voff),"s"(r),"s"(soff):"memory"); }
#define WAITV(n) asm volatile("s_waitcnt vmcnt(" #n ")":::"memory")
#define BAR() asm volatile("s_waitcnt lgkmcnt(0)\n\ts_barrier":::"memory")
#define PIN(x) asm volatile(""::"v"(x))
#define PINW(x) asm volatile("":"+v"(x))
#define SB() __builtin_amdgcn_sched_barrier(0)
DI f32x16 mfma(u32x4 a,u32x4 b,f32x16 c){ return __builtin_amdgcn_mfma_f32_32x32x16_bf16(__builtin_bit_cast(bf16x8,a),__builtin_bit_cast(bf16x8,b),c,0,0,0); }
DI f32x4 m44(u32x2 a,u32x2 b,f32x4 c){ return __builtin_amdgcn_mfma_f32_4x4x4bf16_1k(__builtin_bit_cast(v4i16_t,a),__builtin_bit_cast(v4i16_t,b),c,0,0,0); }
DI u32x2 tr(lptr p){ return __builtin_bit_cast(u32x2,__builtin_amdgcn_ds_read_tr16_b64_v4i16((__attribute__((address_space(3))) v4i16_t*)p)); }
DI u32x4 tr2(lptr p0,lptr p1){ u32x2 a=tr(p0),b=tr(p1); return u32x4{a[0],a[1],b[0],b[1]}; }
DI u32 pk(float lo,float hi){ return __builtin_bit_cast(u32,__builtin_convertvector(f32x2{lo,hi},bf16x2)); }
DI float dot2(u32 a,u32 b,float c){ return __builtin_amdgcn_fdot2_f32_bf16(__builtin_bit_cast(bf16x2,a),__builtin_bit_cast(bf16x2,b),c,false); }
DI float swp_lo(float v){ auto r=__builtin_amdgcn_permlane32_swap(f2u(v),f2u(v),false,false); return u2f(r[0]); }
DI float swp_hi(float v){ auto r=__builtin_amdgcn_permlane32_swap(f2u(v),f2u(v),false,false); return u2f(r[1]); }
DI float swp_add(float v){ auto r=__builtin_amdgcn_permlane32_swap(f2u(v),f2u(v),false,false); return u2f(r[0])+u2f(r[1]); }
DI float swp_max(float v){ auto r=__builtin_amdgcn_permlane32_swap(f2u(v),f2u(v),false,false); return __builtin_fmaxf(u2f(r[0]),u2f(r[1])); }
DI float swp_min(float v){ auto r=__builtin_amdgcn_permlane32_swap(f2u(v),f2u(v),false,false); return __builtin_fminf(u2f(r[0]),u2f(r[1])); }
DI float red8(float v){ v+=u2f(__builtin_amdgcn_update_dpp(0u,f2u(v),0x128,0xf,0xf,false)); auto r=__builtin_amdgcn_permlane16_swap(f2u(v),f2u(v),false,false); return swp_add(u2f(r[0])+u2f(r[1])); }
DI u32x4 pk8(const f32x16& c,int b){ return u32x4{pk(c[b],c[b+1]),pk(c[b+2],c[b+3]),pk(c[b+4],c[b+5]),pk(c[b+6],c[b+7])}; }
DI void dsoftmax(const f32x16& p,float c,u32x4* ph){
  float mx=p[0],mn=p[0];
#pragma unroll
  for(int r=1;r<14;r++){ mx=__builtin_fmaxf(mx,p[r]); mn=__builtin_fminf(mn,p[r]); }
  mx=swp_max(mx); mn=swp_min(mn);
  const float a=-mx*c,b=mn*c; f32x16 e,g; float s=0,u=0;
#pragma unroll
  for(int r=0;r<14;r++){ e[r]=__builtin_amdgcn_exp2f(__builtin_fmaf(p[r],c,a)); g[r]=__builtin_amdgcn_exp2f(__builtin_fmaf(p[r],-c,b)); s+=e[r]; u+=g[r]; }
  s=__builtin_amdgcn_rcpf(swp_add(s)); u=__builtin_amdgcn_rcpf(swp_add(u));
#pragma unroll
  for(int r=0;r<14;r++){ e[r]*=s; g[r]*=u; }
  ph[0]=pk8(e,0); ph[1]=u32x4{pk(e[8],e[9]),pk(e[10],e[11]),pk(e[12],e[13]),0u};
  ph[2]=pk8(g,0); ph[3]=u32x4{pk(g[8],g[9]),pk(g[10],g[11]),pk(g[12],g[13]),0u};
}
DI void dmac(u32 l,u32 voff,rsrc_t r,u32 soff){ asm volatile("s_mov_b32 m0, %0\n\ts_nop 0\n\tbuffer_load_dwordx4 %1, %2, %3 offen sc1 lds"::"s"(l),"v"(voff),"s"(r),"s"(soff):"memory"); }
template<int O> DI void rld(u32x4& v,u32 voff,rsrc_t r,u32 soff){ asm volatile("buffer_load_dwordx4 %0, %1, %2, %3 offen offset:%4 sc1":"+v"(v):"v"(voff),"s"(r),"s"(soff),"n"(O):"memory"); }
template<int O> DI u32x4 ldc(u32 voff,rsrc_t r,u32 soff){ u32x4 v; asm volatile("buffer_load_dwordx4 %0, %1, %2, %3 offen offset:%4 sc1":"=v"(v):"v"(voff),"s"(r),"s"(soff),"n"(O):"memory"); return v; }
DI u32x4 ldp(u32 voff,rsrc_t r,u32 soff){ u32x4 v; asm volatile("buffer_load_dwordx4 %0, %1, %2, %3 offen":"=v"(v):"v"(voff),"s"(r),"s"(soff):"memory"); return v; }
template<int N> DI void waitv(){ asm volatile("s_waitcnt vmcnt(%0)"::"n"(N):"memory"); }
template<int O=0> DI void st16(u32x4 v,rsrc_t r,u32 voff,u32 soff){ asm volatile("buffer_store_dwordx4 %0, %1, %2, %3 offen offset:%4 sc1\n\ts_nop 1"::"v"(v),"v"(voff),"s"(r),"s"(soff),"n"(O):"memory"); }
DI u32 mx3(u32 a,u32 b,u32 c){ u32 r; asm("v_max3_u32 %0, %1, %2, %3":"=v"(r):"v"(a),"v"(b),"v"(c)); return r; }
DI u32 mx4(u32 m,u32x4 g){ return mx3(mx3(m,g[0],g[1]),g[2],g[3]); }
DI bool bad(u32 m){ return __builtin_amdgcn_ballot_w64(m==0xFFFFFFFFu)!=0; }
constexpr int SPIN=30000;
DI void lsig(lptr p){ asm volatile("s_mov_b64 exec, 1\n\tds_add_u32 %0, %1\n\ts_mov_b64 exec, -1"::"v"(p),"v"(1u):"memory"); }
DI void lwait(lptr p,u32 n){ NOUNR for(int it=0;it<SPIN*8;it++){ u32 v; asm volatile("ds_read_b32 %0, %1\n\ts_waitcnt lgkmcnt(0)":"=v"(v):"v"(p):"memory"); if(__builtin_amdgcn_readfirstlane(v)>=n) break; } }
DI float sum8(u32x4 f,float c){ c=dot2(f[0],0x3F803F80u,c); c=dot2(f[1],0x3F803F80u,c); c=dot2(f[2],0x3F803F80u,c); return dot2(f[3],0x3F803F80u,c); }
DI float sq8(u32x4 f,float c){ c=dot2(f[0],f[0],c); c=dot2(f[1],f[1],c); c=dot2(f[2],f[2],c); return dot2(f[3],f[3],c); }
DI void acc8(u32x2 id4,u32x4 g,f32x4& a,f32x4& b){ a=m44(id4,u32x2{g[0],g[1]},a); b=m44(id4,u32x2{g[2],g[3]},b); }
DI u32x4 pk44(f32x4 a,f32x4 b){ return u32x4{pk(a[0],a[1]),pk(a[2],a[3]),pk(b[0],b[1]),pk(b[2],b[3])}; }
DI bool chk(u32x4 g){ return bad(mx3(mx3(g[0],g[1],g[2]),g[3],g[3])); }
template<int N> DI void waitr(u32x4& v){ asm volatile("s_waitcnt vmcnt(%1)":"+v"(v):"n"(N):"memory"); }
template<int A,int B> DI void waitb(u32x4& v,int c){ asm volatile("s_cmp_lg_u32 %1, 0\n\ts_waitcnt vmcnt(%2)\n\ts_cbranch_scc1 1f\n\ts_waitcnt vmcnt(%3)\n1:":"+v"(v):"s"(c),"n"(A),"n"(B):"memory","scc"); }
DI void rldb(u32x4& v,u32 voff,rsrc_t r,u32 soff,int c){ asm volatile("s_cmp_eq_u32 %4, 0\n\ts_cbranch_scc1 1f\n\tbuffer_load_dwordx4 %0, %1, %2, %3 offen sc1\n1:":"+v"(v):"v"(voff),"s"(r),"s"(soff),"s"(c):"memory","scc"); }
DI u32x4 mkpz(){ u32x4 p={~0u,~0u,~0u,~0u}; PINW(p); return p; }
DI u32 wrap(u32 v,u32 n){ return v>=n?v-n:v; }
template<int N,class I,class W,class U,class X,class R> DI int chain(u32x4* g,I&& init,W&& wt,U&& use,X&& ex,R&& re){
  int it=0;
  NOUNR for(;it<SPIN;it++){
    init(); u32 m=0;
    sfor<0,N>([&](auto K){ wt(K); use(K); m=mx4(m,g[K.v]); PINW(m); ex(K); SB(); });
    if(!bad(m)) break;
    WAITV(0); sfor<0,N>(re); WAITV(0); }
  return it; }
#define PRIO(n) __builtin_amdgcn_s_setprio(n)

__global__ __launch_bounds__(512,2) void fused(const u16* __restrict__ x,const u16* __restrict__ wqkv,const u16* __restrict__ wphi,const u16* __restrict__ wo,
    const u16* __restrict__ w1,const u16* __restrict__ b1,const u16* __restrict__ w2,const u16* __restrict__ b2,u16* __restrict__ out,char* __restrict__ ws
    ){
  extern __shared__ __attribute__((aligned(16))) char smem[];
  const lptr L=(lptr)smem;
  const int tid=__builtin_amdgcn_workitem_id_x(),lane=tid&63,w=__builtin_amdgcn_readfirstlane(tid>>6),tt=w&3,ty=w>>2,t=lane&31,hf=lane>>5;
  const int b=__builtin_amdgcn_workgroup_id_x(),i3=(b>>3)&3,jq=b>>5,th=b&1,i=((b&7)>>1)*4+i3;
  const int j1=th*8+jq,odd=j1&1;
  const bool act=jq<7; const int j=th*7+jq,cs=jq;
  STAMP(0);
  asm volatile(""::"s"(x),"s"(wqkv),"s"(wphi),"s"(wo),"s"(w1),"s"(b1),"s"(w2),"s"(b2),"s"(out),"s"(ws));
  { uint64_t t_; asm volatile("s_memrealtime %0\n\ts_waitcnt lgkmcnt(0)":"=s"(t_)::"memory"); if(tid==0) __hip_atomic_store((u32*)(ws+(32u<<20)+b*4),(u32)t_,__ATOMIC_RELAXED,__HIP_MEMORY_SCOPE_AGENT); }
  const u32 epoch=*(const __attribute__((address_space(4))) u32*)ws;
  const rsrc_t rw=mk(wqkv,448*2688), rx=mk(x+(size_t)i*128*448,128*896);
  const u32 vw=(lane>>2)*2688+(lane&3)*16, hoff=56*j1-8*odd+tt*16*2688, sW=hoff+(ty+1)*896, lW=L_WK+ty*28672+tt*1024;
  const u32 vx0=(lane>>3)*896+(((lane&7)^(lane>>4))<<4), vx1=vx0^64, sX=(tt*32+ty*16)*896, lX=tt*4096+ty*2048;
  const u32 rot=ROT?(u32)(j1+2*i3+XR*(i>>2))%7u:0u;
  auto cc=[&](int c){ return wrap(c+rot,7); };
  auto ch=[&](int c,int u){ const u32 q_=cc(c); if(u==0) dma(lW+c*4096,vw,rw,sW+q_*(64*2688)); else dma(xs(c)+lX+(u-1)*1024,u==1?vx0:vx1,rx,sX+(u-1)*7168+q_*128); };
#define SBAR() asm volatile("s_barrier":::"memory")
  if(w<2) dma(L_MISC+w*1024,tid*16,mk(wphi+j1*784,1568),0);
  const u32 l16=lane*16;
  const rsrc_t ro=mk(ws+WS_O+i*114688,114688), rkv=mk(ws+WS_KV+j1*65536,65536), rh=mk(ws+WS_H+i*114688,114688), rst=mk(ws+WS_ST+i*8192,8192);
  auto ost=[&](u32x4 o0,u32x4 o1){ const u32 ub=t*16+((7*j1)>>1)*512, so=tt*28672;
    st16(o0,ro,ub+(odd+2*hf)*512,so); st16(o1,ro,hf?OOB:ub+(odd+1)*512,so);
    __builtin_amdgcn_raw_buffer_store_b64(u32x2{o1[0],o1[1]},ro,hf?ub+(odd?8:1536):OOB,so,16); };
  STAMP(32);
  sfor<0,DEP>([&](auto C){ if constexpr(C.v>0) SBAR(); ch(C.v,0); ch(C.v,1); ch(C.v,2); STAMP(33+C.v); });
  auto poison=[&]{ const u32x4 PZ=mkpz();
    if(ty){ const int t1=j1>=7; const u32 so=(2*t1+(tt&1))*28672+(4*(j1-7*t1)+(tt>>1))*1024, v=j1<14?l16:OOB; st16(PZ,rh,v,so); st16<2048>(PZ,rh,v,so);
      st16(PZ,rst,lane<8?l16:OOB,(j1*4+tt)*128); }
    else { st16(PZ,rkv,l16,i*4096+tt*1024); ost(PZ,PZ); } };
  if(tid<3) LDS(u32x4,L+L_CNT+tid*16)=u32x4{0,0,0,0};
  if(tid<2) LDS(u32x2,L+L_ONE+tid*256)=u32x2{0x3F80u,0u};
  const int tt2=w&1, kq=w>>1;
  const int q4=(lane&15)>>2,mu=((lane>>4)&1)*4+(lane&3),sg=4*(mu&1)+(mu>>1);
  const int rowo=(8*hf+q4)*64, kslot=(mu+odd)&7;
  lptr aW[4],aQ[4],xb[4];
  { const int sw=hf^((t>>1)&7), sl=ty?(odd?(sg==7?0:((sg+1)%7)+1):sg):kslot;
#pragma unroll
    for(int u=0;u<4;u++){ const int r=(u+tt+2*ty)&3; aW[u]=mu==7?L+L_ONE:L+L_WK+ty*28672+rowo+sl*8+r*1024; aQ[u]=L+rowo+kslot*8+r*1024; xb[u]=L+(tt*32+t)*128+((sw^(r*2))<<4); PINW(aW[u]); PINW(xb[u]); } }
  u32 tag=epoch*0x9E3779B1u+0x7F4A7C15u; asm volatile("":"+s"(tag));
  const rsrc_t r1=mk(w1,act?448*3584:0), r2=mk(w2,act?1792*896:0), r0=mk(wo,act?448*896:0);
  const lptr cnt=L+L_CNT;
  const rsrc_t rf=mk(ws,1u<<20); const u32 vfl=lane<16?WS_FLAG+(j1*16+lane)*128:lane<32?WS_FLAG+((lane-16)*16+i)*128:OOB; u32 flv;
  STAMP(43);
  u32 stp=mu==7?0:4096; PINW(stp);
  const u32 tq2=(tt+2)&3, hq=56*j1-8*odd+tq2*16*2688;
  auto wq=[&](int u){ dma(tq2*1024+u*4096,vw,rw,hq+cc(u)*(64*2688)); };
  u32x4 xf[28],wp[2],fw[2][4]; f32x16 c1={}; float ss=0,s2=0,cz=0;
  auto rd=[&](auto C,int u){ constexpr int c=C.v; fw[c&1][u]=tr2(aW[u],aW[u]+256); xf[4*c+u]=LDS(u32x4,xb[u]+xs(c)); if(c<6) aW[u]+=stp; };
  auto md=[&](auto C,int u){ constexpr int c=C.v; c1=mfma(fw[c&1][u],xf[4*c+u],c1); PINW(c1); SB(); };
  auto dd=[&](auto C,int u){ constexpr int c=C.v;
    if(u==0){ cz=sum8(fw[c&1][0],cz); PINW(cz); } else if(u==1){ ss=sq8(xf[4*c],ss); PINW(ss); } else if(u==2){ s2=sq8(xf[4*c+1],s2); PINW(s2); } };
  sfor<0,7>([&](auto C){ constexpr int c=C.v;
    constexpr int pz=c>PZS&&c<PZS+DEP, fl=c>PZS+DEP;
    if(ty) waitv<yng(c)+3*pz+nqc(c-1)>(); else if(fl&&w==2) waitv<yng(c)+4*pz+1>(); else waitv<yng(c)+4*pz>();
    if constexpr(c<3) STAMP(44+c);
    if constexpr(c==0) BAR(); else SBAR();
    if constexpr(c==0) STAMP(1); else STAMP(25+c);
    if constexpr(c==PZS+DEP){ if(tid==128) __hip_atomic_store((u32*)(ws+WS_FLAG+(j1*16+i)*128),tag,__ATOMIC_RELAXED,__HIP_MEMORY_SCOPE_AGENT); }
    if constexpr(c==6){ if(!ty) PRIO(2); }
    if constexpr(c==PZS) poison();
    sfor<0,4>([&](auto U){ constexpr int u=U.v;
      if constexpr(c>0&&c<6) md(IC<c-1>{},u);
      rd(C,u);
      if constexpr(c+DEP<=6&&u<3) ch(c+DEP,u);
      if constexpr(u<nq(c)){ if(ty) wq(nqc(c-1)+u); }
      if constexpr(c>0&&c<6) dd(IC<c-1>{},u);
      SB(); });
    if constexpr(c==0){
      const bool mv=(t&26)!=26; const lptr p=L+L_MISC+(t<26?t:t-2)*2+hf*224; u32 e[16];
#pragma unroll
      for(int q=0;q<16;q++){ const int d=16*(q>>3)+8*((q>>2)&1)+(q&3); e[q]=LDS(u16,p+d*56); }
#pragma unroll
      for(int q=0;q<8;q++){ u32 v=e[2*q]|(e[2*q+1]<<16); v=(mv&&(q<6||hf==0))?v:0u; asm volatile("":"+v"(v)); wp[q>>2][q&3]=v; } SB(); }
    if constexpr(c==5) sfor<0,4>([&](auto U){ md(C,U.v); dd(C,U.v); SB(); });
  });
  md(IC<6>{},0); dd(IC<6>{},0); dd(IC<6>{},1); SB(); md(IC<6>{},1); dd(IC<6>{},2);
  LDS(float,L+L_KVX+w*256+lane*4)=ss+s2; LDS(float,L+L_KVX+2048+ty*1024+l16+tt*4)=cz; SB();
  md(IC<6>{},2); md(IC<6>{},3);
  BAR();
  { const f32x4 z=LDS(f32x4,L+L_KVX+2048+ty*1024+l16); const float so_=LDS(float,L+L_KVX+(w^4)*256+lane*4);
    ss=(ss+s2)+so_; cz=(z[0]+z[1])+(z[2]+z[3]); }
  const float nmean=swp_hi(c1[12])*(-1.f/448);
  c1=__builtin_amdgcn_mfma_f32_32x32x2f32(cz,nmean,c1,0,0,0);
  const float rstd=__builtin_amdgcn_rsqf(__builtin_fmaxf(__builtin_fmaf(swp_add(ss),1.f/448,-nmean*nmean),0.f)+1e-5f);
  STAMP(2);
  const int wro=(tt*4096+t*64),c0=((2*hf)^((t>>2)&3))<<4;
  const rsrc_t rhh=mk(ws+WS_HH+i*458752,act?458752:0);
  const u32 v8=(lane>>3), p8=((lane&7)^(lane>>2&4))<<4, vw2=v8*896+p8, sW2=w*224*896+cs*128;
  const u32 r7=ROT?(u32)(((7*i3)>>2)+jq+XR*(i>>3))%7u:0u, r14=ROT?(u32)(((7*i3)>>1)+R14J*jq+XR*(i>>2))%14u:0u;
#define W2D(l,n,o) do{ const u32 s_=sW2+(o)+wrap((n)+r14,14)*14336; dma((l),vw2,r2,s_); dma((l)+1024,vw2,r2,s_+7168); }while(0)
  const int nq=(mu&4)|((mu&1)<<1)|((mu>>1)&1);
  const int ao=(8*hf+q4)*128+(q4>>1)*64+nq*8; const lptr a0=L+ao,a1=L+(ao^64);
  auto mkid=[&]{ u32 l=l16; PINW(l); const u32 v=(l&16)?0x3F800000u:0x3F80u; return u32x2{(l&32)?0u:v,(l&32)?v:0u}; };
  const u32 vo=(lane&7)*896+v8*16, so_=((2*th+tt2)*32+kq*8)*896+cs*128, vf=v8*512+(lane&7)*16+kq*128;
  const u32 vw1=v8*3584+p8;
  auto w1d=[&](int n,int q){ dma(w1l(n)+q*1024,vw1,r1,(q&1)*8*3584+j*256+(q>>1)*128+(wrap((n>>2)+r7,7)*4+(n&3))*16*3584); };
  u32x4 phq[4],w1f[28],w2f[14][2],xr; u32 b1v;
  auto pzh=[&]{ const u32x4 PZ=mkpz(); const u32 sh=(8*j+2*tt)*1024;
    sfor<0,4>([&](auto U){ st16(PZ,rhh,l16,sh+U.v*114688); st16<1024>(PZ,rhh,l16,sh+U.v*114688); }); };
  auto misc=[&]{ xr=ldp(act?vo:OOB,rx,so_);
    const int tq=t>>2; asm volatile("buffer_load_ushort %0, %1, %2, %3 offen":"=v"(b1v):"v"((((tq&4)|((tq&1)<<1)|((tq>>1)&1))*4+(t&3))*2),"s"(mk(b1,act?3584:0)),"s"(j*256+tt*64):"memory"); };
  auto rdw1=[&](auto P){ lptr q[4]; constexpr int n0=P.v?12:0, n1=P.v?20:8, n2=P.v?28:12;
#pragma unroll
    for(int u=0;u<4;u++){ q[u]=L+w1l(n0)+((tt&1)?ao^64:ao)+(tt>>1)*2048+((u+tt)&3)*4096; PINW(q[u]); }
#pragma unroll
    for(int n=n0;n<n1;n++) w1f[n]=tr2(q[n&3]+(w1l(n&~3)-w1l(n0)),q[n&3]+(w1l(n&~3)-w1l(n0))+512);
#pragma unroll
    for(int u=0;u<4;u++){ q[u]+=P.v?-32768:32768; PINW(q[u]); }
#pragma unroll
    for(int n=n1;n<n2;n++) w1f[n]=tr2(q[n&3]+(w1l(n&~3)-w1l(n1)),q[n&3]+(w1l(n&~3)-w1l(n1))+512); };
  auto rdw1a=[&]{ lwait(cnt+20,4); rdw1(IC<0>{}); asm volatile("s_waitcnt lgkmcnt(0)":::"memory"); lsig(cnt+24); };
  if(ty==0){
    f32x16 p=mfma(wp[0],pk8(c1,0),f32x16{}); p=mfma(wp[1],pk8(c1,8),p);
    u32x4 ph[4]; dsoftmax(p,rstd*LOG2E,ph);
    LDS(u32x4,L+L_PHI+wro+c0)=ph[0]; LDS(u32x4,L+L_PHI+wro+(c0^16))=ph[1];
    LDS(u32x4,L+L_PHI+2048+wro+c0)=ph[2]; LDS(u32x4,L+L_PHI+2048+wro+(c0^16))=ph[3];
    lsig(cnt);
    STAMP(3);
    if(tt<2){
      lwait(cnt,8);
      const int ko=(8*hf+q4)*64+(sg&1)*8; const lptr k0=L+ko+(((sg>>1)^(2*hf))<<4),k1=L+ko+256+(((sg>>1)^(2*hf+1))<<4);
      f32x16 c={}; u32x4 fa[8],fb[8];
#pragma unroll
      for(int u=0;u<8;u++){ const int o=(u>>1)*4096+(u&1)*1024; fa[u]=tr2(k0+L_PHI+tt*2048+o,k1+L_PHI+tt*2048+o); fb[u]=tr2(k0+L_VI+o,k1+L_VI+o); }
      SB();
#pragma unroll
      for(int u=0;u<8;u++) c=mfma(fa[u],fb[u],c);
      st16(pk8(c,0),rkv,l16,i*4096+tt*2048); st16<1024>(pk8(c,8),rkv,l16,i*4096+tt*2048);
      lsig(cnt+8);
      STAMP(4);
    } else {
      if(tt==2) asm volatile("buffer_load_dword %0, %1, %2, 0 offen sc1":"=v"(flv):"v"(vfl),"s"(rf):"memory");
      sfor<W1E,20>([&](auto K){ w1d(K.v,2*(tt&1)); w1d(K.v,2*(tt&1)+1); });
      if(tt==2){
        asm volatile("s_waitcnt vmcnt(%1)":"+v"(flv):"n"(2*(20-W1E)):"memory");
        NOUNR for(int it=0;it<SPIN;it++){
          if(__builtin_amdgcn_ballot_w64(lane<32&&flv!=tag)==0) break;
          asm volatile("buffer_load_dword %0, %1, %2, 0 offen sc1\n\ts_waitcnt vmcnt(0)":"+v"(flv):"v"(vfl),"s"(rf):"memory"); }
        lsig(cnt+12); } }
    lwait(cnt+8,2);
    STAMP(5);
    sfor<20,28>([&](auto K){ w1d(K.v,tt); });
    if(tt<2){ WAITV(8); lsig(cnt+12); }
    STAMP(6);
    lwait(cnt+12,3);
    STAMP(8);
    u32x4 g[16]; const u32 rk=4*i3;
    sfor<0,16>([&](auto N){ g[N.v]=ldc<0>(l16,rkv,((N.v+rk)&15)*4096+tt*1024); });
    STAMP(42);
    rdw1a();
    WAITV(16); lsig(cnt+28);
    misc();
    STAMP(7);
    f32x4 s[4]; const u32x2 id4=mkid();
    const int rr0=chain<16>(g,[&]{ s[0]=s[1]=s[2]=s[3]=f32x4{}; },[&](auto N){ waitr<17-N.v>(g[N.v]); },[&](auto N){ acc8(id4,g[N.v],s[2*(N.v&1)],s[2*(N.v&1)+1]); PINW(s[2*(N.v&1)]); PINW(s[2*(N.v&1)+1]); },[](auto){},
      [&](auto N){ rld<0>(g[N.v],l16,rkv,((N.v+rk)&15)*4096+tt*1024); });
    CNT(37,rr0);
    LDS(u32x4,L+L_KVX+tt*1024+l16)=pk44(s[0]+s[2],s[1]+s[3]);
    PRIO(0);
  } else {
#pragma unroll
    for(int r=0;r<16;r++) c1[r]*=rstd;
    u32x4 v0=pk8(c1,0),v1=pk8(c1,8); if(hf){ v1[2]=0x3F80u; v1[3]=0u; }
    LDS(u32x4,L+L_VI+wro+c0)=v0; LDS(u32x4,L+L_VI+wro+(c0^16))=v1;
    lsig(cnt);
    STAMP(3);
    sfor<nqc(5),7>([&](auto U){ wq(U.v); });
    sfor<0,W1E>([&](auto K){ w1d(K.v,tt); });
    STAMP(40);
    waitv<W1E>(); lsig(cnt+4);
    u32x4 fq[28]; float cq=0;
#pragma unroll
    for(int u=0;u<7;u++) fq[4*u]=tr2(aQ[0]+u*4096,aQ[0]+u*4096+256);
    lwait(cnt+4,4);
    STAMP(4);
    auto ord=[](int s){ return s<7?4*s:((s-7)/3)*4+1+(s-7)%3; };
    f32x16 cQ={};
    sfor<0,28>([&](auto S){ constexpr int s=S.v;
      cQ=mfma(fq[ord(s)],xf[ord(s)],cQ); PINW(cQ); SB();
      if constexpr(s<21){ constexpr int n=ord(s+7); fq[n]=tr2(aQ[n&3]+(n>>2)*4096,aQ[n&3]+(n>>2)*4096+256); }
      if constexpr(s<7){ cq=sum8(fq[4*s],cq); PINW(cq); }
      if constexpr(s==7) LDS(float,L+L_MISC+l16+tt*4)=cq;
      if constexpr(s==22) lsig(cnt+16);
      SB(); });
    lwait(cnt+16,4);
    const f32x4 z=LDS(f32x4,L+L_MISC+l16);
    cQ=__builtin_amdgcn_mfma_f32_32x32x2f32((z[0]+z[1])+(z[2]+z[3]),nmean,cQ,0,0,0);
    f32x16 p=mfma(wp[0],pk8(cQ,0),f32x16{}); p=mfma(wp[1],pk8(cQ,8),p); PINW(p);
    STAMP(5);
    dsoftmax(p,rstd*LOG2E,phq);
    PINW(phq[0]); PINW(phq[1]); PINW(phq[2]); PINW(phq[3]);
    WAITV(0); lsig(cnt+20);
    STAMP(6);
    rdw1a();
    lwait(cnt+28,4); rdw1(IC<1>{});
    lwait(cnt+24,8);
    STAMP(42);
    misc();
    { const u32 rw_=ROT?((7*i3)>>1)+XR*(i>>2):0; sfor<0,14>([&](auto U){ const u32 u=tt*14+wrap(U.v+rw_,14); dma(L_WO+u*1024,vw2,r0,u*7168+cs*128); }); }
    STAMP(7);
    PRIO(2);
  }
  BAR();
  STAMP(9);
  if(ty){ f32x16 c={};
#pragma unroll
    for(int n=0;n<4;n++) c=mfma(LDS(u32x4,L+L_KVX+n*1024+l16),phq[n],c);
    const float rd=__builtin_amdgcn_rcpf(swp_hi(c[12])+1e-6f);
#pragma unroll
    for(int r=0;r<16;r++) c[r]*=rd;
    ost(pk8(c,0),pk8(c,8));
    PRIO(0);
    WAITV(3); lsig(cnt+32);
  }
  else rdw1(IC<1>{});
  STAMP(10);
  if(act){
  { const u32 so=(2*th+tt2)*28672+kq*7168;
    lwait(cnt+32,4);
    if(!ty){ pzh(); W2D(w*4096,0,0); W2D(w*4096+2048,1,0); W2D(w*4096+16384,0,4*224*896); W2D(w*4096+18432,1,4*224*896); }
    lptr p0=a0+L_WO+kq*14336,p1=a1+L_WO+kq*14336; PINW(p0); PINW(p1);
    u32x4 fa[7][2]; f32x16 c[2];
#pragma unroll
    for(int n=0;n<7;n++){ fa[n][0]=tr2(p0+n*2048,p0+n*2048+512); fa[n][1]=tr2(p1+n*2048,p1+n*2048+512); PINW(fa[n][0]); PINW(fa[n][1]); }
    SB();
    STAMP(11);
    if(ty) WAITV(0); else WAITV(16);
    asm volatile("":"+v"(xr),"+v"(b1v)::"memory");
    BAR();
    STAMP(12);
    u32x4 of[7];
    sfor<0,7>([&](auto N){ of[N.v]=ldc<(N.v&3)*1024>(l16,ro,so+(N.v>>2)*4096); });
    auto r2d=[&](int n){ dma(L_R2+(n>>1)*16384+w*2048+(n&1)*1024,vw2,r2,sW2+wrap(2+(n>>1)+r14,14)*14336+(n&1)*7168); };
    STAMP(41);
    const int rr1=chain<7>(of,[&]{ c[0]=c[1]=f32x16{}; },[&](auto N){ waitr<6>(of[N.v]); },[&](auto N){ c[0]=mfma(fa[N.v][0],of[N.v],c[0]); PINW(c[0]); c[1]=mfma(fa[N.v][1],of[N.v],c[1]); PINW(c[1]); },[&](auto N){ r2d(N.v); if(N.v==6) r2d(7); },
      [&](auto N){ rld<(N.v&3)*1024>(of[N.v],l16,ro,so+(N.v>>2)*4096); });
    SB();
    CNT(38,rr1);
    lptr e=L+L_PX+w*4096+l16;
    LDS(u32x4,e)=pk8(c[0],0); LDS(u32x4,e+1024)=pk8(c[0],8); LDS(u32x4,e+2048)=pk8(c[1],0); LDS(u32x4,e+3072)=pk8(c[1],8);
    STAMP(13);
    BAR();
    e=L+L_PX+tt2*4096+vf;
    const u32x2 id4=mkid(); f32x4 s[4]={}; acc8(id4,xr,s[0],s[1]);
#pragma unroll
    for(int q=0;q<4;q++) acc8(id4,LDS(u32x4,e+q*8192),s[2*(q&1)],s[2*(q&1)+1]);
    s[0]+=s[2]; s[1]+=s[3];
    st16(pk44(s[0],s[1]),rh,vf,(2*th+tt2)*28672+cs*4096);
    float s1=((s[0][0]+s[0][1])+(s[0][2]+s[0][3]))+((s[1][0]+s[1][1])+(s[1][2]+s[1][3])), s2=s[0][0]*s[0][0], s3=s[1][0]*s[1][0];
#pragma unroll
    for(int q=1;q<4;q++){ s2=__builtin_fmaf(s[0][q],s[0][q],s2); s3=__builtin_fmaf(s[1][q],s[1][q],s3); }
    s1=red8(s1); s2=red8(s2+s3);
    asm volatile("buffer_store_dwordx2 %0, %1, %2, %3 offen sc1"::"v"(f32x2{s1,s2}),"v"(lane<8?lane*64:OOB),"s"(rst),"s"(((2*th+tt2)*32+kq*8)*64+cs*8):"memory");
    SB(); }
  STAMP(14);
  if(b==0&&tid==0) __hip_atomic_store((u32*)ws,tag,__ATOMIC_RELAXED,__HIP_MEMORY_SCOPE_AGENT);
  const u32 bX=ty?tt*6144:L_S+tt*12288, bY=ty?28672+tt*6144:bX+6144;
  W2D(L_R3+w*2048,6,0);
  { lptr p0=a0+w*4096,p1=a1+w*4096;
#pragma unroll
    for(int n=0;n<2;n++){ w2f[n][0]=tr2(p0+n*2048,p0+n*2048+512); w2f[n][1]=tr2(p1+n*2048,p1+n*2048+512); } }
  WAITV(4);
  { lptr p0=a0+L_R2+w*2048,p1=a1+L_R2+w*2048; PINW(p0); PINW(p1);
#pragma unroll
    for(int n=0;n<4;n++){ w2f[2+n][0]=tr2(p0+n*16384,p0+n*16384+512); w2f[2+n][1]=tr2(p1+n*16384,p1+n*16384+512); }
    asm volatile("s_waitcnt lgkmcnt(0)":"+v"(w2f[4][0]),"+v"(w2f[4][1])::"memory"); }
  W2D(L_R4+w*2048,7,0);
  WAITV(4);
  BAR();
  STAMP(15);
  const u32 sH=ty*57344+tt*1024, lH0=ty*28672+tt*1024, lH1=(ty?57344:86016)+tt*1024;
  if(ty) __builtin_amdgcn_s_sleep(4);
  u32x4 hb[28],sa,sb; float ks[2][5]; const u32 vst=t*64+hf*32;
  auto dm1=[&](u32 l,int k,int u){ dmac(l+u*4096,l16,rh,sH+k*28672+wrap(u+r7,7)*4096); };
  auto dmas=[&](u32 l,int k){ sfor<0,7>([&](auto U){ dm1(l,k,U.v); }); };
  dmas(lH0,0); sa=ldc<0>(vst,rst,ty*4096); sb=ldc<16>(vst,rst,ty*4096);
  float cb=0;
  if(ty){
#pragma unroll
    for(int n=14;n<28;n++) cb=sum8(w1f[n],cb);
    asm volatile("; b":"+v"(cb));
  } else {
#pragma unroll
    for(int n=0;n<14;n++) cb=sum8(w1f[n],cb);
    asm volatile("; a":"+v"(cb)); }
  LDS(float,L+(ty?86016:57344)+tt*1024+lane*4)=cb; SB();
  STAMP(36);
  sfor<0,3>([&](auto U){ W2D(L_S+tt*12288+ty*6144+U.v*2048,8+3*ty+U.v,-ty*4*224*896); });
  WAITV(15);
  { lptr p0=a0+L_R4+w*2048,p1=a1+L_R4+w*2048; PINW(p0); PINW(p1); w2f[7][0]=tr2(p0,p0+512); w2f[7][1]=tr2(p1,p1+512);
    w2f[6][0]=tr2(p0+(L_R3-L_R4),p0+(L_R3-L_R4)+512); w2f[6][1]=tr2(p1+(L_R3-L_R4),p1+(L_R3-L_R4)+512); }
  auto vrd=[&](u32 l){ lptr p=L+l+l16; PINW(p);
#pragma unroll
    for(int n=0;n<7;n++) hb[4*n]=LDS(u32x4,p+n*4096);
    if(hf){ sb[2]=0; sb[3]=0; } };
  auto vbad=[&]{ u32 m=mx4(mx4(0,sa),sb);
#pragma unroll
    for(int n=0;n<7;n++) m=mx4(m,hb[4*n]);
    return bad(m); };
  auto vfix=[&](u32 l,int k){
    NOUNR for(int it=0;it<SPIN;it++){
      sfor<0,7>([&](auto U){ if(chk(hb[4*U.v])) dm1(l,k,U.v); });
      if(bad(mx4(mx4(0,sa),sb))){ rld<0>(sa,vst,rst,ty*4096+k*2048); rld<16>(sb,vst,rst,ty*4096+k*2048); }
      asm volatile("s_waitcnt vmcnt(0)":"+v"(sa),"+v"(sb)::"memory");
      vrd(l); if(!vbad()) break; } };
  auto vst_=[&](float* o){
    const float s1=swp_add((u2f(sa[0])+u2f(sa[2]))+(u2f(sb[0])+u2f(sb[2])))*(-1.f/448), s2=swp_add((u2f(sa[1])+u2f(sa[3]))+(u2f(sb[1])+u2f(sb[3])));
    const float g_=__builtin_fmaf(__builtin_fmaxf(__builtin_fmaf(s2,1.f/448,-s1*s1),0.f),1.00001f,1e-10f);
    const float rs_=__builtin_amdgcn_rsqf(g_); o[3]=0.5f*__builtin_amdgcn_logf(g_);
    o[0]=g_*rs_; o[1]=-2.302208198f*rs_; o[2]=-0.1029432397f*rs_*rs_*rs_; o[4]=hf?o[0]:s1; };
  auto vpin=[&](float* o){ PINW(o[0]); PINW(o[1]); PINW(o[2]); PINW(o[3]); PINW(o[4]); };
  if(ty) BAR();
  asm volatile("s_waitcnt vmcnt(6)":"+v"(sa),"+v"(sb)::"memory"); vrd(lH0); vst_(ks[0]); if(vbad()){ vfix(lH0,0); vst_(ks[0]); } vpin(ks[0]);
  if(!ty) BAR();
  STAMP(16);
  cb=swp_add(cb+LDS(float,L+lH1+lane*4)); cb=hf?u2f(b1v<<16):cb; PINW(cb);
  dmas(lH1,1); sa=ldc<0>(vst,rst,ty*4096+2048); sb=ldc<16>(vst,rst,ty*4096+2048);
  if(ty) BAR();
  sfor<0,2>([&](auto KK){ constexpr int k=KK.v;
    lptr q[4];
#pragma unroll
    for(int u=1;u<4;u++){ q[u]=L+(k?lH1:lH0)+(((u+tt)&3)-tt)*1024+l16; PINW(q[u]); }
    f32x16 c={};
    auto ord=[](int s){ return s<7?4*s:((s-7)/3)*4+1+(s-7)%3; };
    PRIO(1);
    sfor<0,28>([&](auto S){ constexpr int s=S.v;
      c=mfma(w1f[ord(s)],hb[ord(s)],c); PINW(c); SB();
      if constexpr(s+LA>=7&&s+LA<28) hb[ord(s+LA)]=LDS(u32x4,q[ord(s+LA)&3]+(ord(s+LA)>>2)*4096);
      if constexpr(k==0&&s%8==2&&s<24){ if(ty) W2D(bX+(s/8)*2048,8+s/8,0); }
      SB(); });
    c=__builtin_amdgcn_mfma_f32_32x32x2f32(cb,ks[k][4],c,0,0,0); PINW(c);
    PRIO(0);
    SB(); if(k==0||!ty) BAR(); PINW(c); SB();
    STAMP(17+2*k);
    if constexpr(k==0){ if(ty){ W2D(bY,11,0); W2D(bY+2048,12,0); W2D(bY+4096,13,0); if(tid==256) LDS(u32,L+L_SIG)=0; WAITV(12); } else WAITV(0);
      asm volatile("":"+v"(sa),"+v"(sb)::"memory"); vrd(lH1); vst_(ks[1]); }
    u32 g[8]; const float sg_=ks[k][0],K1=ks[k][1],K2=ks[k][2],Lg=ks[k][3];
#pragma unroll
    for(int p=0;p<8;p++){ float a=c[2*p],d=c[2*p+1];
      float u=__builtin_fmaf(a,__builtin_fmaf(a*a,K2,K1),Lg), v=__builtin_fmaf(d,__builtin_fmaf(d*d,K2,K1),Lg);
      u=__builtin_amdgcn_rcpf(__builtin_amdgcn_exp2f(u)+sg_); v=__builtin_amdgcn_rcpf(__builtin_amdgcn_exp2f(v)+sg_);
      g[p]=pk(a*u,d*v);
      if(p==3) st16(u32x4{g[0],g[1],g[2],g[3]},rhh,l16,(2*ty+k)*114688+(8*j+2*tt)*1024); }
    st16<1024>(u32x4{g[4],g[5],g[6],g[7]},rhh,l16,(2*ty+k)*114688+(8*j+2*tt)*1024);
    if constexpr(k==0){ if(vbad()){ vfix(lH1,1); vst_(ks[1]); } vpin(ks[1]);
      BAR(); STAMP(18); }
    SB();
  });
  { const u32 s5=th*229376+w*14336;
    auto su=[&](int n){ return s5+wrap(n+r14,14)*1024; };
    u32x4 hh[14],hp,q2;
    auto rdw2=[&]{ lptr p0=a0+bX,p1=a1+bX; PINW(p0); PINW(p1);
#pragma unroll
      for(int n=0;n<3;n++){ w2f[8+n][0]=tr2(p0+n*2048,p0+n*2048+512); w2f[8+n][1]=tr2(p1+n*2048,p1+n*2048+512); }
      p0=a0+bY; p1=a1+bY; PINW(p0); PINW(p1);
#pragma unroll
      for(int n=0;n<3;n++){ w2f[11+n][0]=tr2(p0+n*2048,p0+n*2048+512); w2f[11+n][1]=tr2(p1+n*2048,p1+n*2048+512); } };
    STAMP(20);
    sfor<0,14>([&](auto N){ hh[N.v]=ldc<0>(l16,rhh,su(N.v)); });
    WAITV(18); rdw2();
#pragma unroll
    for(int n=8;n<14;n++){ PINW(w2f[n][0]); PINW(w2f[n][1]); }
    if(ty){ WAITV(14); lsig(L+L_SIG); }
    STAMP(21);
    f32x16 c[2]; auto zc=[&]{ c[0]=c[1]=f32x16{}; };
    auto mm=[&](auto N){ c[0]=mfma(w2f[N.v][0],hh[N.v],c[0]); PINW(c[0]); c[1]=mfma(w2f[N.v][1],hh[N.v],c[1]); PINW(c[1]); };
    const int rr2=chain<14>(hh,zc,[&](auto N){ waitb<13,13-N.v>(hh[N.v],ty); },mm,[&](auto N){ rldb(hh[N.v],l16,rhh,su(N.v)+114688,ty); },
      [&](auto N){ rld<0>(hh[N.v],l16,rhh,su(N.v)); });
    STAMP(22); CNT(39,rr2);
    lptr e=L+bX+l16; PINW(e);
    LDS(u32x4,e)=pk8(c[0],0); LDS(u32x4,e+1024)=pk8(c[0],8); LDS(u32x4,e+2048)=pk8(c[1],0); LDS(u32x4,e+3072)=pk8(c[1],8);
    if(!ty){ lwait(L+L_SIG,4); sfor<0,14>([&](auto N){ rld<0>(hh[N.v],l16,rhh,su(N.v)+114688); }); }
    hp=ldc<0>(vf,rh,(2*th+tt2)*28672+cs*4096); q2=ldp(v8*16,mk(b2,896),cs*128);
    STAMP(23);
    const int rr3=chain<14>(hh,zc,[&](auto N){ waitr<15-N.v>(hh[N.v]); },mm,[](auto){},
      [&](auto N){ rld<0>(hh[N.v],l16,rhh,su(N.v)+114688); });
    STAMP(24); CNT(47,rr3);
    e=L+bY+l16; PINW(e);
    LDS(u32x4,e)=pk8(c[0],0); LDS(u32x4,e+1024)=pk8(c[0],8); LDS(u32x4,e+2048)=pk8(c[1],0); LDS(u32x4,e+3072)=pk8(c[1],8);
    asm volatile("s_waitcnt vmcnt(0)":"+v"(q2),"+v"(hp)::"memory");
    BAR();
    lptr e0=L+tt2*28672+vf,e1=L+L_S+tt2*6144+vf; PINW(e0); PINW(e1);
    const u32x2 id4=mkid(); f32x4 s[4]={}; acc8(id4,hp,s[0],s[1]); acc8(id4,q2,s[2],s[3]);
#pragma unroll
    for(int q=0;q<4;q++){ acc8(id4,LDS(u32x4,e0+q*6144),s[0],s[1]); acc8(id4,LDS(u32x4,e1+q*12288),s[2],s[3]); }
    __builtin_amdgcn_raw_buffer_store_b128(pk44(s[0]+s[2],s[1]+s[3]),mk(out+(size_t)i*128*448,128*896),vo,so_,0); }
  }
  STAMP(25);
  if(b==PBLK&&w==0){ uint64_t t0_,t1_; asm volatile("s_memrealtime %0\n\ts_waitcnt lgkmcnt(0)":"=s"(t0_)::"memory");
    u32 mn=~0u,mx=0,ms=~0u; const u32 ref=(u32)t0_-100000u;
    for(int q=0;q<256;q++){ u32 v=__hip_atomic_load((u32*)(ws+(32u<<20)+q*4),__ATOMIC_RELAXED,__HIP_MEMORY_SCOPE_AGENT)-ref; v=__builtin_amdgcn_readfirstlane(v); mn=v<mn?v:mn; mx=v>mx?v:mx; if((q&7)==PSEL) ms=v<ms?v:ms; }
    u32 d=PSEL<0?mx-mn:PSEL<8?ms-mn:PSEL==8?100000u-mn:0u; d=d*PMUL+10000u; if(d>40000) d=40000;
    do{ asm volatile("s_memrealtime %0\n\ts_waitcnt lgkmcnt(0)":"=s"(t1_)::"memory"); }while((u32)t1_-(u32)t0_<d); }
}
}
#ifdef __HIP_DEVICE_COMPILE__
#pragma clang attribute pop
#endif
extern "C" void kernel_launch(void* const* d_in,const int* in_sizes,int n_in,void* d_out,int out_size,void* d_ws,size_t ws_size,hipStream_t stream){
  typedef const unsigned short* P;
  (void)hipFuncSetAttribute((const void*)fk::fused,hipFuncAttributeMaxDynamicSharedMemorySize,163840);
  fk::fused<<<256,512,163840,stream>>>((P)d_in[0],(P)d_in[1],(P)d_in[2],(P)d_in[3],(P)d_in[5],(P)d_in[6],(P)d_in[7],(P)d_in[8],(unsigned short*)d_out,(char*)d_ws);
}
```

```cpp
#define OAUX 16
#include <hip/hip_runtime.h>
#include <stdint.h>
#ifdef __HIP_DEVICE_COMPILE__
#pragma clang attribute push(__attribute__((target("no-packed-fp32-ops"))),apply_to=function)
#endif
namespace fk {
typedef unsigned short u16; typedef unsigned u32;
typedef __attribute__((ext_vector_type(4))) u32 u32x4;
typedef __attribute__((ext_vector_type(2))) u32 u32x2;
typedef __attribute__((ext_vector_type(8))) __bf16 bf16x8;
typedef __attribute__((ext_vector_type(2))) __bf16 bf16x2;
typedef __attribute__((ext_vector_type(2))) float f32x2;
typedef __attribute__((ext_vector_type(4))) float f32x4;
typedef __attribute__((ext_vector_type(16))) float f32x16;
typedef short v4i16_t __attribute__((ext_vector_type(4)));
typedef __attribute__((address_space(3))) char* lptr;
typedef __amdgpu_buffer_rsrc_t rsrc_t;
#define DI __device__ __forceinline__
#define LDS(T,a) (*(__attribute__((address_space(3))) T*)(a))
#define STAMP(n)
#define CNT(n,v)
#define CST(n)
constexpr u32 WS_FLAG=4096, WS_ST=512u<<10, WS_KV=1u<<20, WS_O=2u<<20, WS_H=4u<<20, WS_HH=8u<<20, OOB=0x80000000u;
constexpr int L_WK=98304, L_MISC=155648, L_CNT=L_MISC+2048, L_ONE=L_CNT+64, L_KVX=159744;
constexpr int L_PHI=32768, L_VI=49152, L_WO=98304, L_R2=32768, L_R3=98304, L_R4=65536, L_S=114688, L_SIG=53248, L_PX=131072;
constexpr int w1l(int ks){ return ks<12?98304+ks*4096:ks<20?65536+(ks-12)*4096:32768+(ks-20)*4096; }
constexpr int xs(int c){ return (c%6==0?4:c%6==5?5:c%6-1)*16384; }
constexpr float LOG2E=1.4426950408889634f;
#define DEP 3
#define PZS 3
#define NQE 4
#define W1E 12
#define LA 6
#define R14J 2
#define XR 0
#define ROT 1
constexpr int yng(int c){ return ((c-1+DEP<6?c-1+DEP:6)-c)*3; }
constexpr int nq(int s){ return s+DEP<=6||s<4?0:s==4?(NQE<4?NQE:4):s==5?(NQE>4?NQE-4:0):0; }
constexpr int nqc(int s){ return s<4?0:nqc(s-1)+nq(s); }
#define NOUNR _Pragma("clang loop unroll(disable)")
DI float u2f(u32 v){ return __builtin_bit_cast(float,v); }
DI u32 f2u(float v){ return __builtin_bit_cast(u32,v); }
template<int V> struct IC{ static constexpr int v=V; };
template<int I,int N,class F> DI void sfor(F&& f){ if constexpr(I<N){ f(IC<I>{}); sfor<I+1,N>(f); } }
DI rsrc_t mk(const void* p,u32 bytes){ return __builtin_amdgcn_make_buffer_rsrc((void*)p,0,bytes,0x00020000); }
DI void dma(u32 l,u32 voff,rsrc_t r,u32 soff){ asm volatile("s_mov_b32 m0, %0\n\ts_nop 0\n\tbuffer_load_dwordx4 %1, %2, %3 offen lds"::"s"(l),"v"(voff),"s"(r),"s"(soff):"memory"); }
#define WAITV(n) asm volatile("s_waitcnt vmcnt(" #n ")":::"memory")
#define BAR() asm volatile("s_waitcnt lgkmcnt(0)\n\ts_barrier":::"memory")
#define PIN(x) asm volatile(""::"v"(x))
#define PINW(x) asm volatile("":"+v"(x))
#define SB() __builtin_amdgcn_sched_barrier(0)
DI f32x16 mfma(u32x4 a,u32x4 b,f32x16 c){ return __builtin_amdgcn_mfma_f32_32x32x16_bf16(__builtin_bit_cast(bf16x8,a),__builtin_bit_cast(bf16x8,b),c,0,0,0); }
DI f32x4 m44(u32x2 a,u32x2 b,f32x4 c){ return __builtin_amdgcn_mfma_f32_4x4x4bf16_1k(__builtin_bit_cast(v4i16_t,a),__builtin_bit_cast(v4i16_t,b),c,0,0,0); }
DI u32x2 tr(lptr p){ return __builtin_bit_cast(u32x2,__builtin_amdgcn_ds_read_tr16_b64_v4i16((__attribute__((address_space(3))) v4i16_t*)p)); }
DI u32x4 tr2(lptr p0,lptr p1){ u32x2 a=tr(p0),b=tr(p1); return u32x4{a[0],a[1],b[0],b[1]}; }
DI u32 pk(float lo,float hi){ return __builtin_bit_cast(u32,__builtin_convertvector(f32x2{lo,hi},bf16x2)); }
DI float dot2(u32 a,u32 b,float c){ return __builtin_amdgcn_fdot2_f32_bf16(__builtin_bit_cast(bf16x2,a),__builtin_bit_cast(bf16x2,b),c,false); }
DI float swp_lo(float v){ auto r=__builtin_amdgcn_permlane32_swap(f2u(v),f2u(v),false,false); return u2f(r[0]); }
DI float swp_hi(float v){ auto r=__builtin_amdgcn_permlane32_swap(f2u(v),f2u(v),false,false); return u2f(r[1]); }
DI float swp_add(float v){ auto r=__builtin_amdgcn_permlane32_swap(f2u(v),f2u(v),false,false); return u2f(r[0])+u2f(r[1]); }
DI float swp_max(float v){ auto r=__builtin_amdgcn_permlane32_swap(f2u(v),f2u(v),false,false); return __builtin_fmaxf(u2f(r[0]),u2f(r[1])); }
DI float swp_min(float v){ auto r=__builtin_amdgcn_permlane32_swap(f2u(v),f2u(v),false,false); return __builtin_fminf(u2f(r[0]),u2f(r[1])); }
DI float red8(float v){ v+=u2f(__builtin_amdgcn_update_dpp(0u,f2u(v),0x128,0xf,0xf,false)); auto r=__builtin_amdgcn_permlane16_swap(f2u(v),f2u(v),false,false); return swp_add(u2f(r[0])+u2f(r[1])); }
DI u32x4 pk8(const f32x16& c,int b){ return u32x4{pk(c[b],c[b+1]),pk(c[b+2],c[b+3]),pk(c[b+4],c[b+5]),pk(c[b+6],c[b+7])}; }
DI void dsoftmax(const f32x16& p,float c,u32x4* ph){
  float mx=p[0],mn=p[0];
#pragma unroll
  for(int r=1;r<14;r++){ mx=__builtin_fmaxf(mx,p[r]); mn=__builtin_fminf(mn,p[r]); }
  mx=swp_max(mx); mn=swp_min(mn);
  const float a=-mx*c,b=mn*c; f32x16 e,g; float s=0,u=0;
#pragma unroll
  for(int r=0;r<14;r++){ e[r]=__builtin_amdgcn_exp2f(__builtin_fmaf(p[r],c,a)); g[r]=__builtin_amdgcn_exp2f(__builtin_fmaf(p[r],-c,b)); s+=e[r]; u+=g[r]; }
  s=__builtin_amdgcn_rcpf(swp_add(s)); u=__builtin_amdgcn_rcpf(swp_add(u));
#pragma unroll
  for(int r=0;r<14;r++){ e[r]*=s; g[r]*=u; }
  ph[0]=pk8(e,0); ph[1]=u32x4{pk(e[8],e[9]),pk(e[10],e[11]),pk(e[12],e[13]),0u};
  ph[2]=pk8(g,0); ph[3]=u32x4{pk(g[8],g[9]),pk(g[10],g[11]),pk(g[12],g[13]),0u};
}
DI void dmac(u32 l,u32 voff,rsrc_t r,u32 soff){ asm volatile("s_mov_b32 m0, %0\n\ts_nop 0\n\tbuffer_load_dwordx4 %1, %2, %3 offen sc1 lds"::"s"(l),"v"(voff),"s"(r),"s"(soff):"memory"); }
template<int O> DI void rld(u32x4& v,u32 voff,rsrc_t r,u32 soff){ asm volatile("buffer_load_dwordx4 %0, %1, %2, %3 offen offset:%4 sc1":"+v"(v):"v"(voff),"s"(r),"s"(soff),"n"(O):"memory"); }
template<int O> DI u32x4 ldc(u32 voff,rsrc_t r,u32 soff){ u32x4 v; asm volatile("buffer_load_dwordx4 %0, %1, %2, %3 offen offset:%4 sc1":"=v"(v):"v"(voff),"s"(r),"s"(soff),"n"(O):"memory"); return v; }
DI u32x4 ldp(u32 voff,rsrc_t r,u32 soff){ u32x4 v; asm volatile("buffer_load_dwordx4 %0, %1, %2, %3 offen":"=v"(v):"v"(voff),"s"(r),"s"(soff):"memory"); return v; }
template<int N> DI void waitv(){ asm volatile("s_waitcnt vmcnt(%0)"::"n"(N):"memory"); }
template<int O=0> DI void st16(u32x4 v,rsrc_t r,u32 voff,u32 soff){ asm volatile("buffer_store_dwordx4 %0, %1, %2, %3 offen offset:%4 sc1\n\ts_nop 1"::"v"(v),"v"(voff),"s"(r),"s"(soff),"n"(O):"memory"); }
DI u32 mx3(u32 a,u32 b,u32 c){ u32 r; asm("v_max3_u32 %0, %1, %2, %3":"=v"(r):"v"(a),"v"(b),"v"(c)); return r; }
DI u32 mx4(u32 m,u32x4 g){ return mx3(mx3(m,g[0],g[1]),g[2],g[3]); }
DI bool bad(u32 m){ return __builtin_amdgcn_ballot_w64(m==0xFFFFFFFFu)!=0; }
constexpr int SPIN=30000;
DI void lsig(lptr p){ asm volatile("s_mov_b64 exec, 1\n\tds_add_u32 %0, %1\n\ts_mov_b64 exec, -1"::"v"(p),"v"(1u):"memory"); }
DI void lwait(lptr p,u32 n){ NOUNR for(int it=0;it<SPIN*8;it++){ u32 v; asm volatile("ds_read_b32 %0, %1\n\ts_waitcnt lgkmcnt(0)":"=v"(v):"v"(p):"memory"); if(__builtin_amdgcn_readfirstlane(v)>=n) break; } }
DI float sum8(u32x4 f,float c){ c=dot2(f[0],0x3F803F80u,c); c=dot2(f[1],0x3F803F80u,c); c=dot2(f[2],0x3F803F80u,c); return dot2(f[3],0x3F803F80u,c); }
DI float sq8(u32x4 f,float c){ c=dot2(f[0],f[0],c); c=dot2(f[1],f[1],c); c=dot2(f[2],f[2],c); return dot2(f[3],f[3],c); }
DI void acc8(u32x2 id4,u32x4 g,f32x4& a,f32x4& b){ a=m44(id4,u32x2{g[0],g[1]},a); b=m44(id4,u32x2{g[2],g[3]},b); }
DI u32x4 pk44(f32x4 a,f32x4 b){ return u32x4{pk(a[0],a[1]),pk(a[2],a[3]),pk(b[0],b[1]),pk(b[2],b[3])}; }
DI bool chk(u32x4 g){ return bad(mx3(mx3(g[0],g[1],g[2]),g[3],g[3])); }
template<int N> DI void waitr(u32x4& v){ asm volatile("s_waitcnt vmcnt(%1)":"+v"(v):"n"(N):"memory"); }
template<int A,int B> DI void waitb(u32x4& v,int c){ asm volatile("s_cmp_lg_u32 %1, 0\n\ts_waitcnt vmcnt(%2)\n\ts_cbranch_scc1 1f\n\ts_waitcnt vmcnt(%3)\n1:":"+v"(v):"s"(c),"n"(A),"n"(B):"memory","scc"); }
DI void rldb(u32x4& v,u32 voff,rsrc_t r,u32 soff,int c){ asm volatile("s_cmp_eq_u32 %4, 0\n\ts_cbranch_scc1 1f\n\tbuffer_load_dwordx4 %0, %1, %2, %3 offen sc1\n1:":"+v"(v):"v"(voff),"s"(r),"s"(soff),"s"(c):"memory","scc"); }
DI u32x4 mkpz(){ u32x4 p={~0u,~0u,~0u,~0u}; PINW(p); return p; }
DI u32 wrap(u32 v,u32 n){ return v>=n?v-n:v; }
template<int N,class I,class W,class U,class X,class R> DI int chain(u32x4* g,I&& init,W&& wt,U&& use,X&& ex,R&& re){
  int it=0;
  NOUNR for(;it<SPIN;it++){
    init(); u32 m=0;
    sfor<0,N>([&](auto K){ wt(K); use(K); m=mx4(m,g[K.v]); PINW(m); ex(K); SB(); });
    if(!bad(m)) break;
    WAITV(0); sfor<0,N>(re); WAITV(0); }
  return it; }
#define PRIO(n) __builtin_amdgcn_s_setprio(n)

__global__ __launch_bounds__(512,2) void fused(const u16* __restrict__ x,const u16* __restrict__ wqkv,const u16* __restrict__ wphi,const u16* __restrict__ wo,
    const u16* __restrict__ w1,const u16* __restrict__ b1,const u16* __restrict__ w2,const u16* __restrict__ b2,u16* __restrict__ out,char* __restrict__ ws
    ){
  extern __shared__ __attribute__((aligned(16))) char smem[];
  const lptr L=(lptr)smem;
  const int tid=__builtin_amdgcn_workitem_id_x(),lane=tid&63,w=__builtin_amdgcn_readfirstlane(tid>>6),tt=w&3,ty=w>>2,t=lane&31,hf=lane>>5;
  const int b=__builtin_amdgcn_workgroup_id_x(),i3=(b>>3)&3,jq=b>>5,th=b&1,i=((b&7)>>1)*4+i3;
  const int j1=th*8+jq,odd=j1&1;
  const bool act=jq<7; const int j=th*7+jq,cs=jq;
  STAMP(0);
  asm volatile(""::"s"(x),"s"(wqkv),"s"(wphi),"s"(wo),"s"(w1),"s"(b1),"s"(w2),"s"(b2),"s"(out),"s"(ws));
  const u32 epoch=*(const __attribute__((address_space(4))) u32*)ws;
  const rsrc_t rw=mk(wqkv,448*2688), rx=mk(x+(size_t)i*128*448,128*896);
  const u32 vw=(lane>>2)*2688+(lane&3)*16, hoff=56*j1-8*odd+tt*16*2688, sW=hoff+(ty+1)*896, lW=L_WK+ty*28672+tt*1024;
  const u32 vx0=(lane>>3)*896+(((lane&7)^(lane>>4))<<4), vx1=vx0^64, sX=(tt*32+ty*16)*896, lX=tt*4096+ty*2048;
  const u32 rot=ROT?(u32)(j1+2*i3+XR*(i>>2))%7u:0u;
  auto cc=[&](int c){ return wrap(c+rot,7); };
  auto ch=[&](int c,int u){ const u32 q_=cc(c); if(u==0) dma(lW+c*4096,vw,rw,sW+q_*(64*2688)); else dma(xs(c)+lX+(u-1)*1024,u==1?vx0:vx1,rx,sX+(u-1)*7168+q_*128); };
#define SBAR() asm volatile("s_barrier":::"memory")
  if(w<2) dma(L_MISC+w*1024,tid*16,mk(wphi+j1*784,1568),0);
  const u32 l16=lane*16;
  const rsrc_t ro=mk(ws+WS_O+i*114688,114688), rkv=mk(ws+WS_KV+j1*65536,65536), rh=mk(ws+WS_H+i*114688,114688), rst=mk(ws+WS_ST+i*8192,8192);
  auto ost=[&](u32x4 o0,u32x4 o1){ const u32 ub=t*16+((7*j1)>>1)*512, so=tt*28672;
    st16(o0,ro,ub+(odd+2*hf)*512,so); st16(o1,ro,hf?OOB:ub+(odd+1)*512,so);
    __builtin_amdgcn_raw_buffer_store_b64(u32x2{o1[0],o1[1]},ro,hf?ub+(odd?8:1536):OOB,so,16); };
  STAMP(32);
  sfor<0,DEP>([&](auto C){ if constexpr(C.v>0) SBAR(); ch(C.v,0); ch(C.v,1); ch(C.v,2); STAMP(33+C.v); });
  auto poison=[&]{ const u32x4 PZ=mkpz();
    if(ty){ const int t1=j1>=7; const u32 so=(2*t1+(tt&1))*28672+(4*(j1-7*t1)+(tt>>1))*1024, v=j1<14?l16:OOB; st16(PZ,rh,v,so); st16<2048>(PZ,rh,v,so);
      st16(PZ,rst,lane<8?l16:OOB,(j1*4+tt)*128); }
    else { st16(PZ,rkv,l16,i*4096+tt*1024); ost(PZ,PZ); } };
  if(tid<3) LDS(u32x4,L+L_CNT+tid*16)=u32x4{0,0,0,0};
  if(tid<2) LDS(u32x2,L+L_ONE+tid*256)=u32x2{0x3F80u,0u};
  const int tt2=w&1, kq=w>>1;
  const int q4=(lane&15)>>2,mu=((lane>>4)&1)*4+(lane&3),sg=4*(mu&1)+(mu>>1);
  const int rowo=(8*hf+q4)*64, kslot=(mu+odd)&7;
  lptr aW[4],aQ[4],xb[4];
  { const int sw=hf^((t>>1)&7), sl=ty?(odd?(sg==7?0:((sg+1)%7)+1):sg):kslot;
#pragma unroll
    for(int u=0;u<4;u++){ const int r=(u+tt+2*ty)&3; aW[u]=mu==7?L+L_ONE:L+L_WK+ty*28672+rowo+sl*8+r*1024; aQ[u]=L+rowo+kslot*8+r*1024; xb[u]=L+(tt*32+t)*128+((sw^(r*2))<<4); PINW(aW[u]); PINW(xb[u]); } }
  u32 tag=epoch*0x9E3779B1u+0x7F4A7C15u; asm volatile("":"+s"(tag));
  const rsrc_t r1=mk(w1,act?448*3584:0), r2=mk(w2,act?1792*896:0), r0=mk(wo,act?448*896:0);
  const lptr cnt=L+L_CNT;
  const rsrc_t rf=mk(ws,1u<<20); const u32 vfl=lane<16?WS_FLAG+(j1*16+lane)*128:lane<32?WS_FLAG+((lane-16)*16+i)*128:OOB; u32 flv;
  STAMP(43);
  u32 stp=mu==7?0:4096; PINW(stp);
  const u32 tq2=(tt+2)&3, hq=56*j1-8*odd+tq2*16*2688;
  auto wq=[&](int u){ dma(tq2*1024+u*4096,vw,rw,hq+cc(u)*(64*2688)); };
  u32x4 xf[28],wp[2],fw[2][4]; f32x16 c1={}; float ss=0,s2=0,cz=0;
  auto rd=[&](auto C,int u){ constexpr int c=C.v; fw[c&1][u]=tr2(aW[u],aW[u]+256); xf[4*c+u]=LDS(u32x4,xb[u]+xs(c)); if(c<6) aW[u]+=stp; };
  auto md=[&](auto C,int u){ constexpr int c=C.v; c1=mfma(fw[c&1][u],xf[4*c+u],c1); PINW(c1); SB(); };
  auto dd=[&](auto C,int u){ constexpr int c=C.v;
    if(u==0){ cz=sum8(fw[c&1][0],cz); PINW(cz); } else if(u==1){ ss=sq8(xf[4*c],ss); PINW(ss); } else if(u==2){ s2=sq8(xf[4*c+1],s2); PINW(s2); } };
  sfor<0,7>([&](auto C){ constexpr int c=C.v;
    constexpr int pz=c>PZS&&c<PZS+DEP, fl=c>PZS+DEP;
    if(ty) waitv<yng(c)+3*pz+nqc(c-1)>(); else if(fl&&w==2) waitv<yng(c)+4*pz+1>(); else waitv<yng(c)+4*pz>();
    if constexpr(c<3) STAMP(44+c);
    if constexpr(c==0) BAR(); else SBAR();
    if constexpr(c==0) STAMP(1); else STAMP(25+c);
    if constexpr(c==PZS+DEP){ if(tid==128) __hip_atomic_store((u32*)(ws+WS_FLAG+(j1*16+i)*128),tag,__ATOMIC_RELAXED,__HIP_MEMORY_SCOPE_AGENT); }
    if constexpr(c==6){ if(!ty) PRIO(2); }
    if constexpr(c==PZS) poison();
    sfor<0,4>([&](auto U){ constexpr int u=U.v;
      if constexpr(c>0&&c<6) md(IC<c-1>{},u);
      rd(C,u);
      if constexpr(c+DEP<=6&&u<3) ch(c+DEP,u);
      if constexpr(u<nq(c)){ if(ty) wq(nqc(c-1)+u); }
      if constexpr(c>0&&c<6) dd(IC<c-1>{},u);
      SB(); });
    if constexpr(c==0){
      const bool mv=(t&26)!=26; const lptr p=L+L_MISC+(t<26?t:t-2)*2+hf*224; u32 e[16];
#pragma unroll
      for(int q=0;q<16;q++){ const int d=16*(q>>3)+8*((q>>2)&1)+(q&3); e[q]=LDS(u16,p+d*56); }
#pragma unroll
      for(int q=0;q<8;q++){ u32 v=e[2*q]|(e[2*q+1]<<16); v=(mv&&(q<6||hf==0))?v:0u; asm volatile("":"+v"(v)); wp[q>>2][q&3]=v; } SB(); }
    if constexpr(c==5) sfor<0,4>([&](auto U){ md(C,U.v); dd(C,U.v); SB(); });
  });
  md(IC<6>{},0); dd(IC<6>{},0); dd(IC<6>{},1); SB(); md(IC<6>{},1); dd(IC<6>{},2);
  LDS(float,L+L_KVX+w*256+lane*4)=ss+s2; LDS(float,L+L_KVX+2048+ty*1024+l16+tt*4)=cz; SB();
  md(IC<6>{},2); md(IC<6>{},3);
  BAR();
  { const f32x4 z=LDS(f32x4,L+L_KVX+2048+ty*1024+l16); const float so_=LDS(float,L+L_KVX+(w^4)*256+lane*4);
    ss=(ss+s2)+so_; cz=(z[0]+z[1])+(z[2]+z[3]); }
  const float nmean=swp_hi(c1[12])*(-1.f/448);
  c1=__builtin_amdgcn_mfma_f32_32x32x2f32(cz,nmean,c1,0,0,0);
  const float rstd=__builtin_amdgcn_rsqf(__builtin_fmaxf(__builtin_fmaf(swp_add(ss),1.f/448,-nmean*nmean),0.f)+1e-5f);
  STAMP(2);
  const int wro=(tt*4096+t*64),c0=((2*hf)^((t>>2)&3))<<4;
  const rsrc_t rhh=mk(ws+WS_HH+i*458752,act?458752:0);
  const u32 v8=(lane>>3), p8=((lane&7)^(lane>>2&4))<<4, vw2=v8*896+p8, sW2=w*224*896+cs*128;
  const u32 r7=ROT?(u32)(((7*i3)>>2)+jq+XR*(i>>3))%7u:0u, r14=ROT?(u32)(((7*i3)>>1)+R14J*jq+XR*(i>>2))%14u:0u;
#define W2D(l,n,o) do{ const u32 s_=sW2+(o)+wrap((n)+r14,14)*14336; dma((l),vw2,r2,s_); dma((l)+1024,vw2,r2,s_+7168); }while(0)
  const int nq=(mu&4)|((mu&1)<<1)|((mu>>1)&1);
  const int ao=(8*hf+q4)*128+(q4>>1)*64+nq*8; const lptr a0=L+ao,a1=L+(ao^64);
  auto mkid=[&]{ u32 l=l16; PINW(l); const u32 v=(l&16)?0x3F800000u:0x3F80u; return u32x2{(l&32)?0u:v,(l&32)?v:0u}; };
  const u32 vo=(lane&7)*896+v8*16, so_=((2*th+tt2)*32+kq*8)*896+cs*128, vf=v8*512+(lane&7)*16+kq*128;
  const u32 vw1=v8*3584+p8;
  auto w1d=[&](int n,int q){ dma(w1l(n)+q*1024,vw1,r1,(q&1)*8*3584+j*256+(q>>1)*128+(wrap((n>>2)+r7,7)*4+(n&3))*16*3584); };
  u32x4 phq[4],w1f[28],w2f[14][2],xr; u32 b1v;
  auto pzh=[&]{ const u32x4 PZ=mkpz(); const u32 sh=(8*j+2*tt)*1024;
    sfor<0,4>([&](auto U){ st16(PZ,rhh,l16,sh+U.v*114688); st16<1024>(PZ,rhh,l16,sh+U.v*114688); }); };
  auto misc=[&]{ xr=ldp(act?vo:OOB,rx,so_);
    const int tq=t>>2; asm volatile("buffer_load_ushort %0, %1, %2, %3 offen":"=v"(b1v):"v"((((tq&4)|((tq&1)<<1)|((tq>>1)&1))*4+(t&3))*2),"s"(mk(b1,act?3584:0)),"s"(j*256+tt*64):"memory"); };
  auto rdw1=[&](auto P){ lptr q[4]; constexpr int n0=P.v?12:0, n1=P.v?20:8, n2=P.v?28:12;
#pragma unroll
    for(int u=0;u<4;u++){ q[u]=L+w1l(n0)+((tt&1)?ao^64:ao)+(tt>>1)*2048+((u+tt)&3)*4096; PINW(q[u]); }
#pragma unroll
    for(int n=n0;n<n1;n++) w1f[n]=tr2(q[n&3]+(w1l(n&~3)-w1l(n0)),q[n&3]+(w1l(n&~3)-w1l(n0))+512);
#pragma unroll
    for(int u=0;u<4;u++){ q[u]+=P.v?-32768:32768; PINW(q[u]); }
#pragma unroll
    for(int n=n1;n<n2;n++) w1f[n]=tr2(q[n&3]+(w1l(n&~3)-w1l(n1)),q[n&3]+(w1l(n&~3)-w1l(n1))+512); };
  auto rdw1a=[&]{ lwait(cnt+20,4); rdw1(IC<0>{}); asm volatile("s_waitcnt lgkmcnt(0)":::"memory"); lsig(cnt+24); };
  if(ty==0){
    f32x16 p=mfma(wp[0],pk8(c1,0),f32x16{}); p=mfma(wp[1],pk8(c1,8),p);
    u32x4 ph[4]; dsoftmax(p,rstd*LOG2E,ph);
    LDS(u32x4,L+L_PHI+wro+c0)=ph[0]; LDS(u32x4,L+L_PHI+wro+(c0^16))=ph[1];
    LDS(u32x4,L+L_PHI+2048+wro+c0)=ph[2]; LDS(u32x4,L+L_PHI+2048+wro+(c0^16))=ph[3];
    lsig(cnt);
    STAMP(3);
    if(tt<2){
      lwait(cnt,8);
      const int ko=(8*hf+q4)*64+(sg&1)*8; const lptr k0=L+ko+(((sg>>1)^(2*hf))<<4),k1=L+ko+256+(((sg>>1)^(2*hf+1))<<4);
      f32x16 c={}; u32x4 fa[8],fb[8];
#pragma unroll
      for(int u=0;u<8;u++){ const int o=(u>>1)*4096+(u&1)*1024; fa[u]=tr2(k0+L_PHI+tt*2048+o,k1+L_PHI+tt*2048+o); fb[u]=tr2(k0+L_VI+o,k1+L_VI+o); }
      SB();
#pragma unroll
      for(int u=0;u<8;u++) c=mfma(fa[u],fb[u],c);
      st16(pk8(c,0),rkv,l16,i*4096+tt*2048); st16<1024>(pk8(c,8),rkv,l16,i*4096+tt*2048);
      lsig(cnt+8);
      STAMP(4);
    } else {
      if(tt==2) asm volatile("buffer_load_dword %0, %1, %2, 0 offen sc1":"=v"(flv):"v"(vfl),"s"(rf):"memory");
      sfor<W1E,20>([&](auto K){ w1d(K.v,2*(tt&1)); w1d(K.v,2*(tt&1)+1); });
      if(tt==2){
        asm volatile("s_waitcnt vmcnt(%1)":"+v"(flv):"n"(2*(20-W1E)):"memory");
        NOUNR for(int it=0;it<SPIN;it++){
          if(__builtin_amdgcn_ballot_w64(lane<32&&flv!=tag)==0) break;
          asm volatile("buffer_load_dword %0, %1, %2, 0 offen sc1\n\ts_waitcnt vmcnt(0)":"+v"(flv):"v"(vfl),"s"(rf):"memory"); }
        lsig(cnt+12); } }
    lwait(cnt+8,2);
    STAMP(5);
    sfor<20,28>([&](auto K){ w1d(K.v,tt); });
    if(tt<2){ WAITV(8); lsig(cnt+12); }
    STAMP(6);
    lwait(cnt+12,3);
    STAMP(8);
    u32x4 g[16]; const u32 rk=4*i3;
    sfor<0,16>([&](auto N){ g[N.v]=ldc<0>(l16,rkv,((N.v+rk)&15)*4096+tt*1024); });
    STAMP(42);
    rdw1a();
    WAITV(16); lsig(cnt+28);
    misc();
    STAMP(7);
    f32x4 s[4]; const u32x2 id4=mkid();
    const int rr0=chain<16>(g,[&]{ s[0]=s[1]=s[2]=s[3]=f32x4{}; },[&](auto N){ waitr<17-N.v>(g[N.v]); },[&](auto N){ acc8(id4,g[N.v],s[2*(N.v&1)],s[2*(N.v&1)+1]); PINW(s[2*(N.v&1)]); PINW(s[2*(N.v&1)+1]); },[](auto){},
      [&](auto N){ rld<0>(g[N.v],l16,rkv,((N.v+rk)&15)*4096+tt*1024); });
    CNT(37,rr0);
    LDS(u32x4,L+L_KVX+tt*1024+l16)=pk44(s[0]+s[2],s[1]+s[3]);
    PRIO(0);
  } else {
#pragma unroll
    for(int r=0;r<16;r++) c1[r]*=rstd;
    u32x4 v0=pk8(c1,0),v1=pk8(c1,8); if(hf){ v1[2]=0x3F80u; v1[3]=0u; }
    LDS(u32x4,L+L_VI+wro+c0)=v0; LDS(u32x4,L+L_VI+wro+(c0^16))=v1;
    lsig(cnt);
    STAMP(3);
    sfor<nqc(5),7>([&](auto U){ wq(U.v); });
    sfor<0,W1E>([&](auto K){ w1d(K.v,tt); });
    STAMP(40);
    waitv<W1E>(); lsig(cnt+4);
    u32x4 fq[28]; float cq=0;
#pragma unroll
    for(int u=0;u<7;u++) fq[4*u]=tr2(aQ[0]+u*4096,aQ[0]+u*4096+256);
    lwait(cnt+4,4);
    STAMP(4);
    auto ord=[](int s){ return s<7?4*s:((s-7)/3)*4+1+(s-7)%3; };
    f32x16 cQ={};
    sfor<0,28>([&](auto S){ constexpr int s=S.v;
      cQ=mfma(fq[ord(s)],xf[ord(s)],cQ); PINW(cQ); SB();
      if constexpr(s<21){ constexpr int n=ord(s+7); fq[n]=tr2(aQ[n&3]+(n>>2)*4096,aQ[n&3]+(n>>2)*4096+256); }
      if constexpr(s<7){ cq=sum8(fq[4*s],cq); PINW(cq); }
      if constexpr(s==7) LDS(float,L+L_MISC+l16+tt*4)=cq;
      if constexpr(s==22) lsig(cnt+16);
      SB(); });
    lwait(cnt+16,4);
    const f32x4 z=LDS(f32x4,L+L_MISC+l16);
    cQ=__builtin_amdgcn_mfma_f32_32x32x2f32((z[0]+z[1])+(z[2]+z[3]),nmean,cQ,0,0,0);
    f32x16 p=mfma(wp[0],pk8(cQ,0),f32x16{}); p=mfma(wp[1],pk8(cQ,8),p); PINW(p);
    STAMP(5);
    dsoftmax(p,rstd*LOG2E,phq);
    PINW(phq[0]); PINW(phq[1]); PINW(phq[2]); PINW(phq[3]);
    WAITV(0); lsig(cnt+20);
    STAMP(6);
    rdw1a();
    lwait(cnt+28,4); rdw1(IC<1>{});
    lwait(cnt+24,8);
    STAMP(42);
    misc();
    { const u32 rw_=ROT?((7*i3)>>1)+XR*(i>>2):0; sfor<0,14>([&](auto U){ const u32 u=tt*14+wrap(U.v+rw_,14); dma(L_WO+u*1024,vw2,r0,u*7168+cs*128); }); }
    STAMP(7);
    PRIO(2);
  }
  BAR();
  STAMP(9);
  if(ty){ f32x16 c={};
#pragma unroll
    for(int n=0;n<4;n++) c=mfma(LDS(u32x4,L+L_KVX+n*1024+l16),phq[n],c);
    const float rd=__builtin_amdgcn_rcpf(swp_hi(c[12])+1e-6f);
#pragma unroll
    for(int r=0;r<16;r++) c[r]*=rd;
    ost(pk8(c,0),pk8(c,8));
    PRIO(0);
    WAITV(3); lsig(cnt+32);
  }
  else rdw1(IC<1>{});
  STAMP(10);
  if(act){
  { const u32 so=(2*th+tt2)*28672+kq*7168;
    lwait(cnt+32,4);
    if(!ty){ pzh(); W2D(w*4096,0,0); W2D(w*4096+2048,1,0); W2D(w*4096+16384,0,4*224*896); W2D(w*4096+18432,1,4*224*896); }
    lptr p0=a0+L_WO+kq*14336,p1=a1+L_WO+kq*14336; PINW(p0); PINW(p1);
    u32x4 fa[7][2]; f32x16 c[2];
#pragma unroll
    for(int n=0;n<7;n++){ fa[n][0]=tr2(p0+n*2048,p0+n*2048+512); fa[n][1]=tr2(p1+n*2048,p1+n*2048+512); PINW(fa[n][0]); PINW(fa[n][1]); }
    SB();
    STAMP(11);
    if(ty) WAITV(0); else WAITV(16);
    asm volatile("":"+v"(xr),"+v"(b1v)::"memory");
    BAR();
    STAMP(12);
    u32x4 of[7];
    sfor<0,7>([&](auto N){ of[N.v]=ldc<(N.v&3)*1024>(l16,ro,so+(N.v>>2)*4096); });
    auto r2d=[&](int n){ dma(L_R2+(n>>1)*16384+w*2048+(n&1)*1024,vw2,r2,sW2+wrap(2+(n>>1)+r14,14)*14336+(n&1)*7168); };
    STAMP(41);
    const int rr1=chain<7>(of,[&]{ c[0]=c[1]=f32x16{}; },[&](auto N){ waitr<6>(of[N.v]); },[&](auto N){ c[0]=mfma(fa[N.v][0],of[N.v],c[0]); PINW(c[0]); c[1]=mfma(fa[N.v][1],of[N.v],c[1]); PINW(c[1]); },[&](auto N){ r2d(N.v); if(N.v==6) r2d(7); },
      [&](auto N){ rld<(N.v&3)*1024>(of[N.v],l16,ro,so+(N.v>>2)*4096); });
    SB();
    CNT(38,rr1);
    lptr e=L+L_PX+w*4096+l16;
    LDS(u32x4,e)=pk8(c[0],0); LDS(u32x4,e+1024)=pk8(c[0],8); LDS(u32x4,e+2048)=pk8(c[1],0); LDS(u32x4,e+3072)=pk8(c[1],8);
    STAMP(13);
    BAR();
    e=L+L_PX+tt2*4096+vf;
    const u32x2 id4=mkid(); f32x4 s[4]={}; acc8(id4,xr,s[0],s[1]);
#pragma unroll
    for(int q=0;q<4;q++) acc8(id4,LDS(u32x4,e+q*8192),s[2*(q&1)],s[2*(q&1)+1]);
    s[0]+=s[2]; s[1]+=s[3];
    st16(pk44(s[0],s[1]),rh,vf,(2*th+tt2)*28672+cs*4096);
    float s1=((s[0][0]+s[0][1])+(s[0][2]+s[0][3]))+((s[1][0]+s[1][1])+(s[1][2]+s[1][3])), s2=s[0][0]*s[0][0], s3=s[1][0]*s[1][0];
#pragma unroll
    for(int q=1;q<4;q++){ s2=__builtin_fmaf(s[0][q],s[0][q],s2); s3=__builtin_fmaf(s[1][q],s[1][q],s3); }
    s1=red8(s1); s2=red8(s2+s3);
    asm volatile("buffer_store_dwordx2 %0, %1, %2, %3 offen sc1"::"v"(f32x2{s1,s2}),"v"(lane<8?lane*64:OOB),"s"(rst),"s"(((2*th+tt2)*32+kq*8)*64+cs*8):"memory");
    SB(); }
  STAMP(14);
  if(b==0&&tid==0) __hip_atomic_store((u32*)ws,tag,__ATOMIC_RELAXED,__HIP_MEMORY_SCOPE_AGENT);
  const u32 bX=ty?tt*6144:L_S+tt*12288, bY=ty?28672+tt*6144:bX+6144;
  W2D(L_R3+w*2048,6,0);
  { lptr p0=a0+w*4096,p1=a1+w*4096;
#pragma unroll
    for(int n=0;n<2;n++){ w2f[n][0]=tr2(p0+n*2048,p0+n*2048+512); w2f[n][1]=tr2(p1+n*2048,p1+n*2048+512); } }
  WAITV(4);
  { lptr p0=a0+L_R2+w*2048,p1=a1+L_R2+w*2048; PINW(p0); PINW(p1);
#pragma unroll
    for(int n=0;n<4;n++){ w2f[2+n][0]=tr2(p0+n*16384,p0+n*16384+512); w2f[2+n][1]=tr2(p1+n*16384,p1+n*16384+512); }
    asm volatile("s_waitcnt lgkmcnt(0)":"+v"(w2f[4][0]),"+v"(w2f[4][1])::"memory"); }
  W2D(L_R4+w*2048,7,0);
  WAITV(4);
  BAR();
  STAMP(15);
  const u32 sH=ty*57344+tt*1024, lH0=ty*28672+tt*1024, lH1=(ty?57344:86016)+tt*1024;
  if(ty) __builtin_amdgcn_s_sleep(4);
  u32x4 hb[28],sa,sb; float ks[2][5]; const u32 vst=t*64+hf*32;
  auto dm1=[&](u32 l,int k,int u){ dmac(l+u*4096,l16,rh,sH+k*28672+wrap(u+r7,7)*4096); };
  auto dmas=[&](u32 l,int k){ sfor<0,7>([&](auto U){ dm1(l,k,U.v); }); };
  dmas(lH0,0); sa=ldc<0>(vst,rst,ty*4096); sb=ldc<16>(vst,rst,ty*4096);
  float cb=0;
  if(ty){
#pragma unroll
    for(int n=14;n<28;n++) cb=sum8(w1f[n],cb);
    asm volatile("; b":"+v"(cb));
  } else {
#pragma unroll
    for(int n=0;n<14;n++) cb=sum8(w1f[n],cb);
    asm volatile("; a":"+v"(cb)); }
  LDS(float,L+(ty?86016:57344)+tt*1024+lane*4)=cb; SB();
  STAMP(36);
  sfor<0,3>([&](auto U){ W2D(L_S+tt*12288+ty*6144+U.v*2048,8+3*ty+U.v,-ty*4*224*896); });
  WAITV(15);
  { lptr p0=a0+L_R4+w*2048,p1=a1+L_R4+w*2048; PINW(p0); PINW(p1); w2f[7][0]=tr2(p0,p0+512); w2f[7][1]=tr2(p1,p1+512);
    w2f[6][0]=tr2(p0+(L_R3-L_R4),p0+(L_R3-L_R4)+512); w2f[6][1]=tr2(p1+(L_R3-L_R4),p1+(L_R3-L_R4)+512); }
  auto vrd=[&](u32 l){ lptr p=L+l+l16; PINW(p);
#pragma unroll
    for(int n=0;n<7;n++) hb[4*n]=LDS(u32x4,p+n*4096);
    if(hf){ sb[2]=0; sb[3]=0; } };
  auto vbad=[&]{ u32 m=mx4(mx4(0,sa),sb);
#pragma unroll
    for(int n=0;n<7;n++) m=mx4(m,hb[4*n]);
    return bad(m); };
  auto vfix=[&](u32 l,int k){
    NOUNR for(int it=0;it<SPIN;it++){
      sfor<0,7>([&](auto U){ if(chk(hb[4*U.v])) dm1(l,k,U.v); });
      if(bad(mx4(mx4(0,sa),sb))){ rld<0>(sa,vst,rst,ty*4096+k*2048); rld<16>(sb,vst,rst,ty*4096+k*2048); }
      asm volatile("s_waitcnt vmcnt(0)":"+v"(sa),"+v"(sb)::"memory");
      vrd(l); if(!vbad()) break; } };
  auto vst_=[&](float* o){
    const float s1=swp_add((u2f(sa[0])+u2f(sa[2]))+(u2f(sb[0])+u2f(sb[2])))*(-1.f/448), s2=swp_add((u2f(sa[1])+u2f(sa[3]))+(u2f(sb[1])+u2f(sb[3])));
    const float g_=__builtin_fmaf(__builtin_fmaxf(__builtin_fmaf(s2,1.f/448,-s1*s1),0.f),1.00001f,1e-10f);
    const float rs_=__builtin_amdgcn_rsqf(g_); o[3]=0.5f*__builtin_amdgcn_logf(g_);
    o[0]=g_*rs_; o[1]=-2.302208198f*rs_; o[2]=-0.1029432397f*rs_*rs_*rs_; o[4]=hf?o[0]:s1; };
  auto vpin=[&](float* o){ PINW(o[0]); PINW(o[1]); PINW(o[2]); PINW(o[3]); PINW(o[4]); };
  if(ty) BAR();
  asm volatile("s_waitcnt vmcnt(6)":"+v"(sa),"+v"(sb)::"memory"); vrd(lH0); vst_(ks[0]); if(vbad()){ vfix(lH0,0); vst_(ks[0]); } vpin(ks[0]);
  if(!ty) BAR();
  STAMP(16);
  cb=swp_add(cb+LDS(float,L+lH1+lane*4)); cb=hf?u2f(b1v<<16):cb; PINW(cb);
  dmas(lH1,1); sa=ldc<0>(vst,rst,ty*4096+2048); sb=ldc<16>(vst,rst,ty*4096+2048);
  if(ty) BAR();
  sfor<0,2>([&](auto KK){ constexpr int k=KK.v;
    lptr q[4];
#pragma unroll
    for(int u=1;u<4;u++){ q[u]=L+(k?lH1:lH0)+(((u+tt)&3)-tt)*1024+l16; PINW(q[u]); }
    f32x16 c={};
    auto ord=[](int s){ return s<7?4*s:((s-7)/3)*4+1+(s-7)%3; };
    PRIO(1);
    sfor<0,28>([&](auto S){ constexpr int s=S.v;
      c=mfma(w1f[ord(s)],hb[ord(s)],c); PINW(c); SB();
      if constexpr(s+LA>=7&&s+LA<28) hb[ord(s+LA)]=LDS(u32x4,q[ord(s+LA)&3]+(ord(s+LA)>>2)*4096);
      if constexpr(k==0&&s%8==2&&s<24){ if(ty) W2D(bX+(s/8)*2048,8+s/8,0); }
      SB(); });
    c=__builtin_amdgcn_mfma_f32_32x32x2f32(cb,ks[k][4],c,0,0,0); PINW(c);
    PRIO(0);
    SB(); if(k==0||!ty) BAR(); PINW(c); SB();
    STAMP(17+2*k);
    if constexpr(k==0){ if(ty){ W2D(bY,11,0); W2D(bY+2048,12,0); W2D(bY+4096,13,0); if(tid==256) LDS(u32,L+L_SIG)=0; WAITV(12); } else WAITV(0);
      asm volatile("":"+v"(sa),"+v"(sb)::"memory"); vrd(lH1); vst_(ks[1]); }
    u32 g[8]; const float sg_=ks[k][0],K1=ks[k][1],K2=ks[k][2],Lg=ks[k][3];
#pragma unroll
    for(int p=0;p<8;p++){ float a=c[2*p],d=c[2*p+1];
      float u=__builtin_fmaf(a,__builtin_fmaf(a*a,K2,K1),Lg), v=__builtin_fmaf(d,__builtin_fmaf(d*d,K2,K1),Lg);
      u=__builtin_amdgcn_rcpf(__builtin_amdgcn_exp2f(u)+sg_); v=__builtin_amdgcn_rcpf(__builtin_amdgcn_exp2f(v)+sg_);
      g[p]=pk(a*u,d*v);
      if(p==3) st16(u32x4{g[0],g[1],g[2],g[3]},rhh,l16,(2*ty+k)*114688+(8*j+2*tt)*1024); }
    st16<1024>(u32x4{g[4],g[5],g[6],g[7]},rhh,l16,(2*ty+k)*114688+(8*j+2*tt)*1024);
    if constexpr(k==0){ if(vbad()){ vfix(lH1,1); vst_(ks[1]); } vpin(ks[1]);
      BAR(); STAMP(18); }
    SB();
  });
  { const u32 s5=th*229376+w*14336;
    auto su=[&](int n){ return s5+wrap(n+r14,14)*1024; };
    u32x4 hh[14],hp,q2;
    auto rdw2=[&]{ lptr p0=a0+bX,p1=a1+bX; PINW(p0); PINW(p1);
#pragma unroll
      for(int n=0;n<3;n++){ w2f[8+n][0]=tr2(p0+n*2048,p0+n*2048+512); w2f[8+n][1]=tr2(p1+n*2048,p1+n*2048+512); }
      p0=a0+bY; p1=a1+bY; PINW(p0); PINW(p1);
#pragma unroll
      for(int n=0;n<3;n++){ w2f[11+n][0]=tr2(p0+n*2048,p0+n*2048+512); w2f[11+n][1]=tr2(p1+n*2048,p1+n*2048+512); } };
    STAMP(20);
    sfor<0,14>([&](auto N){ hh[N.v]=ldc<0>(l16,rhh,su(N.v)); });
    WAITV(18); rdw2();
#pragma unroll
    for(int n=8;n<14;n++){ PINW(w2f[n][0]); PINW(w2f[n][1]); }
    if(ty){ WAITV(14); lsig(L+L_SIG); }
    STAMP(21);
    f32x16 c[2]; auto zc=[&]{ c[0]=c[1]=f32x16{}; };
    auto mm=[&](auto N){ c[0]=mfma(w2f[N.v][0],hh[N.v],c[0]); PINW(c[0]); c[1]=mfma(w2f[N.v][1],hh[N.v],c[1]); PINW(c[1]); };
    const int rr2=chain<14>(hh,zc,[&](auto N){ waitb<13,13-N.v>(hh[N.v],ty); },mm,[&](auto N){ rldb(hh[N.v],l16,rhh,su(N.v)+114688,ty); },
      [&](auto N){ rld<0>(hh[N.v],l16,rhh,su(N.v)); });
    STAMP(22); CNT(39,rr2);
    lptr e=L+bX+l16; PINW(e);
    LDS(u32x4,e)=pk8(c[0],0); LDS(u32x4,e+1024)=pk8(c[0],8); LDS(u32x4,e+2048)=pk8(c[1],0); LDS(u32x4,e+3072)=pk8(c[1],8);
    if(!ty){ lwait(L+L_SIG,4); sfor<0,14>([&](auto N){ rld<0>(hh[N.v],l16,rhh,su(N.v)+114688); }); }
    hp=ldc<0>(vf,rh,(2*th+tt2)*28672+cs*4096); q2=ldp(v8*16,mk(b2,896),cs*128);
    STAMP(23);
    const int rr3=chain<14>(hh,zc,[&](auto N){ waitr<15-N.v>(hh[N.v]); },mm,[](auto){},
      [&](auto N){ rld<0>(hh[N.v],l16,rhh,su(N.v)+114688); });
    STAMP(24); CNT(47,rr3);
    e=L+bY+l16; PINW(e);
    LDS(u32x4,e)=pk8(c[0],0); LDS(u32x4,e+1024)=pk8(c[0],8); LDS(u32x4,e+2048)=pk8(c[1],0); LDS(u32x4,e+3072)=pk8(c[1],8);
    asm volatile("s_waitcnt vmcnt(0)":"+v"(q2),"+v"(hp)::"memory");
    BAR();
    lptr e0=L+tt2*28672+vf,e1=L+L_S+tt2*6144+vf; PINW(e0); PINW(e1);
    const u32x2 id4=mkid(); f32x4 s[4]={}; acc8(id4,hp,s[0],s[1]); acc8(id4,q2,s[2],s[3]);
#pragma unroll
    for(int q=0;q<4;q++){ acc8(id4,LDS(u32x4,e0+q*6144),s[0],s[1]); acc8(id4,LDS(u32x4,e1+q*12288),s[2],s[3]); }
    __builtin_amdgcn_raw_buffer_store_b128(pk44(s[0]+s[2],s[1]+s[3]),mk(out+(size_t)i*128*448,128*896),vo,so_,OAUX); }
  }
  STAMP(25);
}
}
#ifdef __HIP_DEVICE_COMPILE__
#pragma clang attribute pop
#endif
extern "C" void kernel_launch(void* const* d_in,const int* in_sizes,int n_in,void* d_out,int out_size,void* d_ws,size_t ws_size,hipStream_t stream){
  typedef const unsigned short* P;
  (void)hipFuncSetAttribute((const void*)fk::fused,hipFuncAttributeMaxDynamicSharedMemorySize,163840);
  fk::fused<<<256,512,163840,stream>>>((P)d_in[0],(P)d_in[1],(P)d_in[2],(P)d_in[3],(P)d_in[5],(P)d_in[6],(P)d_in[7],(P)d_in[8],(unsigned short*)d_out,(char*)d_ws);
}
```

```cpp
#include <hip/hip_runtime.h>
#include <stdint.h>
#ifdef __HIP_DEVICE_COMPILE__
#pragma clang attribute push(__attribute__((target("no-packed-fp32-ops"))),apply_to=function)
#endif
namespace fk {
typedef unsigned short u16; typedef unsigned u32;
typedef __attribute__((ext_vector_type(4))) u32 u32x4;
typedef __attribute__((ext_vector_type(2))) u32 u32x2;
typedef __attribute__((ext_vector_type(8))) __bf16 bf16x8;
typedef __attribute__((ext_vector_type(2))) __bf16 bf16x2;
typedef __attribute__((ext_vector_type(2))) float f32x2;
typedef __attribute__((ext_vector_type(4))) float f32x4;
typedef __attribute__((ext_vector_type(16))) float f32x16;
typedef short v4i16_t __attribute__((ext_vector_type(4)));
typedef __attribute__((address_space(3))) char* lptr;
typedef __amdgpu_buffer_rsrc_t rsrc_t;
#define DI __device__ __forceinline__
#define LDS(T,a) (*(__attribute__((address_space(3))) T*)(a))
#define STAMP(n)
#define CNT(n,v)
#define CST(n)
constexpr u32 WS_FLAG=4096, WS_ST=512u<<10, WS_KV=1u<<20, WS_O=2u<<20, WS_H=4u<<20, WS_HH=8u<<20, OOB=0x80000000u;
constexpr int L_WK=98304, L_MISC=155648, L_CNT=L_MISC+2048, L_ONE=L_CNT+64, L_KVX=159744;
constexpr int L_PHI=32768, L_VI=49152, L_WO=98304, L_R2=32768, L_R3=98304, L_R4=65536, L_S=114688, L_SIG=53248, L_PX=131072;
constexpr int w1l(int ks){ return ks<12?98304+ks*4096:ks<20?65536+(ks-12)*4096:32768+(ks-20)*4096; }
constexpr int xs(int c){ return (c%6==0?4:c%6==5?5:c%6-1)*16384; }
constexpr float LOG2E=1.4426950408889634f;
#define DEP 3
#define PZS 3
#define NQE 4
#define W1E 12
#define LA 6
#define R14J 2
#define XR 0
#define XS 0
#define ROT 1
constexpr int yng(int c){ return ((c-1+DEP<6?c-1+DEP:6)-c)*3; }
constexpr int nq(int s){ return s+DEP<=6||s<4?0:s==4?(NQE<4?NQE:4):s==5?(NQE>4?NQE-4:0):0; }
constexpr int nqc(int s){ return s<4?0:nqc(s-1)+nq(s); }
#define NOUNR _Pragma("clang loop unroll(disable)")
DI float u2f(u32 v){ return __builtin_bit_cast(float,v); }
DI u32 f2u(float v){ return __builtin_bit_cast(u32,v); }
template<int V> struct IC{ static constexpr int v=V; };
template<int I,int N,class F> DI void sfor(F&& f){ if constexpr(I<N){ f(IC<I>{}); sfor<I+1,N>(f); } }
DI rsrc_t mk(const void* p,u32 bytes){ return __builtin_amdgcn_make_buffer_rsrc((void*)p,0,bytes,0x00020000); }
DI void dma(u32 l,u32 voff,rsrc_t r,u32 soff){ asm volatile("s_mov_b32 m0, %0\n\ts_nop 0\n\tbuffer_load_dwordx4 %1, %2, %3 offen lds"::"s"(l),"v"(voff),"s"(r),"s"(soff):"memory"); }
#define WAITV(n) asm volatile("s_waitcnt vmcnt(" #n ")":::"memory")
#define BAR() asm volatile("s_waitcnt lgkmcnt(0)\n\ts_barrier":::"memory")
#define PIN(x) asm volatile(""::"v"(x))
#define PINW(x) asm volatile("":"+v"(x))
#define SB() __builtin_amdgcn_sched_barrier(0)
DI f32x16 mfma(u32x4 a,u32x4 b,f32x16 c){ return __builtin_amdgcn_mfma_f32_32x32x16_bf16(__builtin_bit_cast(bf16x8,a),__builtin_bit_cast(bf16x8,b),c,0,0,0); }
DI f32x4 m44(u32x2 a,u32x2 b,f32x4 c){ return __builtin_amdgcn_mfma_f32_4x4x4bf16_1k(__builtin_bit_cast(v4i16_t,a),__builtin_bit_cast(v4i16_t,b),c,0,0,0); }
DI u32x2 tr(lptr p){ return __builtin_bit_cast(u32x2,__builtin_amdgcn_ds_read_tr16_b64_v4i16((__attribute__((address_space(3))) v4i16_t*)p)); }
DI u32x4 tr2(lptr p0,lptr p1){ u32x2 a=tr(p0),b=tr(p1); return u32x4{a[0],a[1],b[0],b[1]}; }
DI u32 pk(float lo,float hi){ return __builtin_bit_cast(u32,__builtin_convertvector(f32x2{lo,hi},bf16x2)); }
DI float dot2(u32 a,u32 b,float c){ return __builtin_amdgcn_fdot2_f32_bf16(__builtin_bit_cast(bf16x2,a),__builtin_bit_cast(bf16x2,b),c,false); }
DI float swp_lo(float v){ auto r=__builtin_amdgcn_permlane32_swap(f2u(v),f2u(v),false,false); return u2f(r[0]); }
DI float swp_hi(float v){ auto r=__builtin_amdgcn_permlane32_swap(f2u(v),f2u(v),false,false); return u2f(r[1]); }
DI float swp_add(float v){ auto r=__builtin_amdgcn_permlane32_swap(f2u(v),f2u(v),false,false); return u2f(r[0])+u2f(r[1]); }
DI float swp_max(float v){ auto r=__builtin_amdgcn_permlane32_swap(f2u(v),f2u(v),false,false); return __builtin_fmaxf(u2f(r[0]),u2f(r[1])); }
DI float swp_min(float v){ auto r=__builtin_amdgcn_permlane32_swap(f2u(v),f2u(v),false,false); return __builtin_fminf(u2f(r[0]),u2f(r[1])); }
DI float red8(float v){ v+=u2f(__builtin_amdgcn_update_dpp(0u,f2u(v),0x128,0xf,0xf,false)); auto r=__builtin_amdgcn_permlane16_swap(f2u(v),f2u(v),false,false); return swp_add(u2f(r[0])+u2f(r[1])); }
DI u32x4 pk8(const f32x16& c,int b){ return u32x4{pk(c[b],c[b+1]),pk(c[b+2],c[b+3]),pk(c[b+4],c[b+5]),pk(c[b+6],c[b+7])}; }
DI void dsoftmax(const f32x16& p,float c,u32x4* ph){
  float mx=p[0],mn=p[0];
#pragma unroll
  for(int r=1;r<14;r++){ mx=__builtin_fmaxf(mx,p[r]); mn=__builtin_fminf(mn,p[r]); }
  mx=swp_max(mx); mn=swp_min(mn);
  const float a=-mx*c,b=mn*c; f32x16 e,g; float s=0,u=0;
#pragma unroll
  for(int r=0;r<14;r++){ e[r]=__builtin_amdgcn_exp2f(__builtin_fmaf(p[r],c,a)); g[r]=__builtin_amdgcn_exp2f(__builtin_fmaf(p[r],-c,b)); s+=e[r]; u+=g[r]; }
  s=__builtin_amdgcn_rcpf(swp_add(s)); u=__builtin_amdgcn_rcpf(swp_add(u));
#pragma unroll
  for(int r=0;r<14;r++){ e[r]*=s; g[r]*=u; }
  ph[0]=pk8(e,0); ph[1]=u32x4{pk(e[8],e[9]),pk(e[10],e[11]),pk(e[12],e[13]),0u};
  ph[2]=pk8(g,0); ph[3]=u32x4{pk(g[8],g[9]),pk(g[10],g[11]),pk(g[12],g[13]),0u};
}
DI void dmac(u32 l,u32 voff,rsrc_t r,u32 soff){ asm volatile("s_mov_b32 m0, %0\n\ts_nop 0\n\tbuffer_load_dwordx4 %1, %2, %3 offen sc1 lds"::"s"(l),"v"(voff),"s"(r),"s"(soff):"memory"); }
template<int O> DI void rld(u32x4& v,u32 voff,rsrc_t r,u32 soff){ asm volatile("buffer_load_dwordx4 %0, %1, %2, %3 offen offset:%4 sc1":"+v"(v):"v"(voff),"s"(r),"s"(soff),"n"(O):"memory"); }
template<int O> DI u32x4 ldc(u32 voff,rsrc_t r,u32 soff){ u32x4 v; asm volatile("buffer_load_dwordx4 %0, %1, %2, %3 offen offset:%4 sc1":"=v"(v):"v"(voff),"s"(r),"s"(soff),"n"(O):"memory"); return v; }
DI u32x4 ldp(u32 voff,rsrc_t r,u32 soff){ u32x4 v; asm volatile("buffer_load_dwordx4 %0, %1, %2, %3 offen":"=v"(v):"v"(voff),"s"(r),"s"(soff):"memory"); return v; }
template<int N> DI void waitv(){ asm volatile("s_waitcnt vmcnt(%0)"::"n"(N):"memory"); }
template<int O=0> DI void st16(u32x4 v,rsrc_t r,u32 voff,u32 soff){ asm volatile("buffer_store_dwordx4 %0, %1, %2, %3 offen offset:%4 sc1\n\ts_nop 1"::"v"(v),"v"(voff),"s"(r),"s"(soff),"n"(O):"memory"); }
DI u32 mx3(u32 a,u32 b,u32 c){ u32 r; asm("v_max3_u32 %0, %1, %2, %3":"=v"(r):"v"(a),"v"(b),"v"(c)); return r; }
DI u32 mx4(u32 m,u32x4 g){ return mx3(mx3(m,g[0],g[1]),g[2],g[3]); }
DI bool bad(u32 m){ return __builtin_amdgcn_ballot_w64(m==0xFFFFFFFFu)!=0; }
constexpr int SPIN=30000;
DI void lsig(lptr p){ asm volatile("s_mov_b64 exec, 1\n\tds_add_u32 %0, %1\n\ts_mov_b64 exec, -1"::"v"(p),"v"(1u):"memory"); }
DI void lwait(lptr p,u32 n){ NOUNR for(int it=0;it<SPIN*8;it++){ u32 v; asm volatile("ds_read_b32 %0, %1\n\ts_waitcnt lgkmcnt(0)":"=v"(v):"v"(p):"memory"); if(__builtin_amdgcn_readfirstlane(v)>=n) break; } }
DI float sum8(u32x4 f,float c){ c=dot2(f[0],0x3F803F80u,c); c=dot2(f[1],0x3F803F80u,c); c=dot2(f[2],0x3F803F80u,c); return dot2(f[3],0x3F803F80u,c); }
DI float sq8(u32x4 f,float c){ c=dot2(f[0],f[0],c); c=dot2(f[1],f[1],c); c=dot2(f[2],f[2],c); return dot2(f[3],f[3],c); }
DI void acc8(u32x2 id4,u32x4 g,f32x4& a,f32x4& b){ a=m44(id4,u32x2{g[0],g[1]},a); b=m44(id4,u32x2{g[2],g[3]},b); }
DI u32x4 pk44(f32x4 a,f32x4 b){ return u32x4{pk(a[0],a[1]),pk(a[2],a[3]),pk(b[0],b[1]),pk(b[2],b[3])}; }
DI bool chk(u32x4 g){ return bad(mx3(mx3(g[0],g[1],g[2]),g[3],g[3])); }
template<int N> DI void waitr(u32x4& v){ asm volatile("s_waitcnt vmcnt(%1)":"+v"(v):"n"(N):"memory"); }
template<int A,int B> DI void waitb(u32x4& v,int c){ asm volatile("s_cmp_lg_u32 %1, 0\n\ts_waitcnt vmcnt(%2)\n\ts_cbranch_scc1 1f\n\ts_waitcnt vmcnt(%3)\n1:":"+v"(v):"s"(c),"n"(A),"n"(B):"memory","scc"); }
DI void rldb(u32x4& v,u32 voff,rsrc_t r,u32 soff,int c){ asm volatile("s_cmp_eq_u32 %4, 0\n\ts_cbranch_scc1 1f\n\tbuffer_load_dwordx4 %0, %1, %2, %3 offen sc1\n1:":"+v"(v):"v"(voff),"s"(r),"s"(soff),"s"(c):"memory","scc"); }
DI u32x4 mkpz(){ u32x4 p={~0u,~0u,~0u,~0u}; PINW(p); return p; }
DI u32 wrap(u32 v,u32 n){ return v>=n?v-n:v; }
template<int N,class I,class W,class U,class X,class R> DI int chain(u32x4* g,I&& init,W&& wt,U&& use,X&& ex,R&& re){
  int it=0;
  NOUNR for(;it<SPIN;it++){
    init(); u32 m=0;
    sfor<0,N>([&](auto K){ wt(K); use(K); m=mx4(m,g[K.v]); PINW(m); ex(K); SB(); });
    if(!bad(m)) break;
    WAITV(0); sfor<0,N>(re); WAITV(0); }
  return it; }
#define PRIO(n) __builtin_amdgcn_s_setprio(n)

__global__ __launch_bounds__(512,2) void fused(const u16* __restrict__ x,const u16* __restrict__ wqkv,const u16* __restrict__ wphi,const u16* __restrict__ wo,
    const u16* __restrict__ w1,const u16* __restrict__ b1,const u16* __restrict__ w2,const u16* __restrict__ b2,u16* __restrict__ out,char* __restrict__ ws
    ){
  extern __shared__ __attribute__((aligned(16))) char smem[];
  const lptr L=(lptr)smem;
  const int tid=__builtin_amdgcn_workitem_id_x(),lane=tid&63,w=__builtin_amdgcn_readfirstlane(tid>>6),tt=w&3,ty=w>>2,t=lane&31,hf=lane>>5;
  const int b=__builtin_amdgcn_workgroup_id_x(),i3=(b>>3)&3,jq=b>>5,th=b&1,i=((b&7)>>1)*4+i3;
  const int j1=th*8+jq,odd=j1&1;
  const bool act=jq<7; const int j=th*7+jq,cs=jq;
  STAMP(0);
  asm volatile(""::"s"(x),"s"(wqkv),"s"(wphi),"s"(wo),"s"(w1),"s"(b1),"s"(w2),"s"(b2),"s"(out),"s"(ws));
  const u32 epoch=*(const __attribute__((address_space(4))) u32*)ws;
  const rsrc_t rw=mk(wqkv,448*2688), rx=mk(x+(size_t)i*128*448,128*896);
  const u32 vw=(lane>>2)*2688+(lane&3)*16, hoff=56*j1-8*odd+tt*16*2688, sW=hoff+(ty+1)*896, lW=L_WK+ty*28672+tt*1024;
  const u32 vx0=(lane>>3)*896+(((lane&7)^(lane>>4))<<4), vx1=vx0^64, sX=(tt*32+ty*16)*896, lX=tt*4096+ty*2048;
  const u32 rot=ROT?(u32)(j1+2*i3+XR*(i>>2))%7u:0u;
  auto cc=[&](int c){ return wrap(c+rot,7); };
  auto ch=[&](int c,int u){ const u32 q_=cc(c); if(u==0) dma(lW+c*4096,vw,rw,sW+q_*(64*2688)); else dma(xs(c)+lX+(u-1)*1024,u==1?vx0:vx1,rx,sX+(u-1)*7168+q_*128); };
#define SBAR() asm volatile("s_barrier":::"memory")
  if(w<2) dma(L_MISC+w*1024,tid*16,mk(wphi+j1*784,1568),0);
  const u32 l16=lane*16;
  const rsrc_t ro=mk(ws+WS_O+i*114688,114688), rkv=mk(ws+WS_KV+j1*65536,65536), rh=mk(ws+WS_H+i*114688,114688), rst=mk(ws+WS_ST+i*8192,8192);
  auto ost=[&](u32x4 o0,u32x4 o1){ const u32 ub=t*16+((7*j1)>>1)*512, so=tt*28672;
    st16(o0,ro,ub+(odd+2*hf)*512,so); st16(o1,ro,hf?OOB:ub+(odd+1)*512,so);
    __builtin_amdgcn_raw_buffer_store_b64(u32x2{o1[0],o1[1]},ro,hf?ub+(odd?8:1536):OOB,so,16); };
  STAMP(32);
  sfor<0,DEP>([&](auto C){ if constexpr(C.v>0) SBAR(); ch(C.v,0); ch(C.v,1); ch(C.v,2); STAMP(33+C.v); });
  auto poison=[&]{ const u32x4 PZ=mkpz();
    if(ty){ const int t1=j1>=7; const u32 so=(2*t1+(tt&1))*28672+(4*(j1-7*t1)+(tt>>1))*1024, v=j1<14?l16:OOB; st16(PZ,rh,v,so); st16<2048>(PZ,rh,v,so);
      st16(PZ,rst,lane<8?l16:OOB,(j1*4+tt)*128); }
    else { st16(PZ,rkv,l16,i*4096+tt*1024); ost(PZ,PZ); } };
  if(tid<3) LDS(u32x4,L+L_CNT+tid*16)=u32x4{0,0,0,0};
  if(tid<2) LDS(u32x2,L+L_ONE+tid*256)=u32x2{0x3F80u,0u};
  const int tt2=w&1, kq=w>>1;
  const int q4=(lane&15)>>2,mu=((lane>>4)&1)*4+(lane&3),sg=4*(mu&1)+(mu>>1);
  const int rowo=(8*hf+q4)*64, kslot=(mu+odd)&7;
  lptr aW[4],aQ[4],xb[4];
  { const int sw=hf^((t>>1)&7), sl=ty?(odd?(sg==7?0:((sg+1)%7)+1):sg):kslot;
#pragma unroll
    for(int u=0;u<4;u++){ const int r=(u+tt+2*ty)&3; aW[u]=mu==7?L+L_ONE:L+L_WK+ty*28672+rowo+sl*8+r*1024; aQ[u]=L+rowo+kslot*8+r*1024; xb[u]=L+(tt*32+t)*128+((sw^(r*2))<<4); PINW(aW[u]); PINW(xb[u]); } }
  u32 tag=epoch*0x9E3779B1u+0x7F4A7C15u; asm volatile("":"+s"(tag));
  const rsrc_t r1=mk(w1,act?448*3584:0), r2=mk(w2,act?1792*896:0), r0=mk(wo,act?448*896:0);
  const lptr cnt=L+L_CNT;
  const rsrc_t rf=mk(ws,1u<<20); const u32 vfl=lane<16?WS_FLAG+(j1*16+lane)*128:lane<32?WS_FLAG+((lane-16)*16+i)*128:OOB; u32 flv;
  STAMP(43);
  u32 stp=mu==7?0:4096; PINW(stp);
  const u32 tq2=(tt+2)&3, hq=56*j1-8*odd+tq2*16*2688;
  auto wq=[&](int u){ dma(tq2*1024+u*4096,vw,rw,hq+cc(u)*(64*2688)); };
  u32x4 xf[28],wp[2],fw[2][4]; f32x16 c1={}; float ss=0,s2=0,cz=0;
  auto rd=[&](auto C,int u){ constexpr int c=C.v; fw[c&1][u]=tr2(aW[u],aW[u]+256); xf[4*c+u]=LDS(u32x4,xb[u]+xs(c)); if(c<6) aW[u]+=stp; };
  auto md=[&](auto C,int u){ constexpr int c=C.v; c1=mfma(fw[c&1][u],xf[4*c+u],c1); PINW(c1); SB(); };
  auto dd=[&](auto C,int u){ constexpr int c=C.v;
    if(u==0){ cz=sum8(fw[c&1][0],cz); PINW(cz); } else if(u==1){ ss=sq8(xf[4*c],ss); PINW(ss); } else if(u==2){ s2=sq8(xf[4*c+1],s2); PINW(s2); } };
  sfor<0,7>([&](auto C){ constexpr int c=C.v;
    constexpr int pz=c>PZS&&c<PZS+DEP, fl=c>PZS+DEP;
    if(ty) waitv<yng(c)+3*pz+nqc(c-1)>(); else if(fl&&w==2) waitv<yng(c)+4*pz+1>(); else waitv<yng(c)+4*pz>();
    if constexpr(c<3) STAMP(44+c);
    if constexpr(c==0) BAR(); else SBAR();
    if constexpr(c==0) STAMP(1); else if constexpr(!XS) STAMP(25+c);
    if constexpr(c==PZS+DEP){ if(tid==128) __hip_atomic_store((u32*)(ws+WS_FLAG+(j1*16+i)*128),tag,__ATOMIC_RELAXED,__HIP_MEMORY_SCOPE_AGENT); }
    if constexpr(c==6){ if(!ty) PRIO(2); }
    if constexpr(c==PZS) poison();
    sfor<0,4>([&](auto U){ constexpr int u=U.v;
      if constexpr(c>0&&c<6) md(IC<c-1>{},u);
      rd(C,u);
      if constexpr(c+DEP<=6&&u<3) ch(c+DEP,u);
      if constexpr(u<nq(c)){ if(ty) wq(nqc(c-1)+u); }
      if constexpr(c>0&&c<6) dd(IC<c-1>{},u);
      SB(); });
    if constexpr(c==0){
      const bool mv=(t&26)!=26; const lptr p=L+L_MISC+(t<26?t:t-2)*2+hf*224; u32 e[16];
#pragma unroll
      for(int q=0;q<16;q++){ const int d=16*(q>>3)+8*((q>>2)&1)+(q&3); e[q]=LDS(u16,p+d*56); }
#pragma unroll
      for(int q=0;q<8;q++){ u32 v=e[2*q]|(e[2*q+1]<<16); v=(mv&&(q<6||hf==0))?v:0u; asm volatile("":"+v"(v)); wp[q>>2][q&3]=v; } SB(); }
    if constexpr(c==5) sfor<0,4>([&](auto U){ md(C,U.v); dd(C,U.v); SB(); });
  });
  md(IC<6>{},0); dd(IC<6>{},0); dd(IC<6>{},1); SB(); md(IC<6>{},1); dd(IC<6>{},2);
  LDS(float,L+L_KVX+w*256+lane*4)=ss+s2; LDS(float,L+L_KVX+2048+ty*1024+l16+tt*4)=cz; SB();
  md(IC<6>{},2); md(IC<6>{},3);
  BAR();
  { const f32x4 z=LDS(f32x4,L+L_KVX+2048+ty*1024+l16); const float so_=LDS(float,L+L_KVX+(w^4)*256+lane*4);
    ss=(ss+s2)+so_; cz=(z[0]+z[1])+(z[2]+z[3]); }
  const float nmean=swp_hi(c1[12])*(-1.f/448);
  c1=__builtin_amdgcn_mfma_f32_32x32x2f32(cz,nmean,c1,0,0,0);
  const float rstd=__builtin_amdgcn_rsqf(__builtin_fmaxf(__builtin_fmaf(swp_add(ss),1.f/448,-nmean*nmean),0.f)+1e-5f);
  STAMP(2);
  const int wro=(tt*4096+t*64),c0=((2*hf)^((t>>2)&3))<<4;
  const rsrc_t rhh=mk(ws+WS_HH+i*458752,act?458752:0);
  const u32 v8=(lane>>3), p8=((lane&7)^(lane>>2&4))<<4, vw2=v8*896+p8, sW2=w*224*896+cs*128;
  const u32 r7=ROT?(u32)(((7*i3)>>2)+jq+XR*(i>>3))%7u:0u, r14=ROT?(u32)(((7*i3)>>1)+R14J*jq+XR*(i>>2))%14u:0u;
#define W2D(l,n,o) do{ const u32 s_=sW2+(o)+wrap((n)+r14,14)*14336; dma((l),vw2,r2,s_); dma((l)+1024,vw2,r2,s_+7168); }while(0)
  const int nq=(mu&4)|((mu&1)<<1)|((mu>>1)&1);
  const int ao=(8*hf+q4)*128+(q4>>1)*64+nq*8; const lptr a0=L+ao,a1=L+(ao^64);
  auto mkid=[&]{ u32 l=l16; PINW(l); const u32 v=(l&16)?0x3F800000u:0x3F80u; return u32x2{(l&32)?0u:v,(l&32)?v:0u}; };
  const u32 vo=(lane&7)*896+v8*16, so_=((2*th+tt2)*32+kq*8)*896+cs*128, vf=v8*512+(lane&7)*16+kq*128;
  const u32 vw1=v8*3584+p8;
  auto w1d=[&](int n,int q){ dma(w1l(n)+q*1024,vw1,r1,(q&1)*8*3584+j*256+(q>>1)*128+(wrap((n>>2)+r7,7)*4+(n&3))*16*3584); };
  u32x4 phq[4],w1f[28],w2f[14][2],xr; u32 b1v;
  auto pzh=[&]{ const u32x4 PZ=mkpz(); const u32 sh=(8*j+2*tt)*1024;
    sfor<0,4>([&](auto U){ st16(PZ,rhh,l16,sh+U.v*114688); st16<1024>(PZ,rhh,l16,sh+U.v*114688); }); };
  auto misc=[&]{ xr=ldp(act?vo:OOB,rx,so_);
    const int tq=t>>2; asm volatile("buffer_load_ushort %0, %1, %2, %3 offen":"=v"(b1v):"v"((((tq&4)|((tq&1)<<1)|((tq>>1)&1))*4+(t&3))*2),"s"(mk(b1,act?3584:0)),"s"(j*256+tt*64):"memory"); };
  auto rdw1=[&](auto P){ lptr q[4]; constexpr int n0=P.v?12:0, n1=P.v?20:8, n2=P.v?28:12;
#pragma unroll
    for(int u=0;u<4;u++){ q[u]=L+w1l(n0)+((tt&1)?ao^64:ao)+(tt>>1)*2048+((u+tt)&3)*4096; PINW(q[u]); }
#pragma unroll
    for(int n=n0;n<n1;n++) w1f[n]=tr2(q[n&3]+(w1l(n&~3)-w1l(n0)),q[n&3]+(w1l(n&~3)-w1l(n0))+512);
#pragma unroll
    for(int u=0;u<4;u++){ q[u]+=P.v?-32768:32768; PINW(q[u]); }
#pragma unroll
    for(int n=n1;n<n2;n++) w1f[n]=tr2(q[n&3]+(w1l(n&~3)-w1l(n1)),q[n&3]+(w1l(n&~3)-w1l(n1))+512); };
  auto rdw1a=[&]{ lwait(cnt+20,4); rdw1(IC<0>{}); asm volatile("s_waitcnt lgkmcnt(0)":::"memory"); lsig(cnt+24); };
  if(ty==0){
    f32x16 p=mfma(wp[0],pk8(c1,0),f32x16{}); p=mfma(wp[1],pk8(c1,8),p);
    u32x4 ph[4]; dsoftmax(p,rstd*LOG2E,ph);
    LDS(u32x4,L+L_PHI+wro+c0)=ph[0]; LDS(u32x4,L+L_PHI+wro+(c0^16))=ph[1];
    LDS(u32x4,L+L_PHI+2048+wro+c0)=ph[2]; LDS(u32x4,L+L_PHI+2048+wro+(c0^16))=ph[3];
    lsig(cnt);
    STAMP(3);
    if(tt<2){
      lwait(cnt,8);
      const int ko=(8*hf+q4)*64+(sg&1)*8; const lptr k0=L+ko+(((sg>>1)^(2*hf))<<4),k1=L+ko+256+(((sg>>1)^(2*hf+1))<<4);
      f32x16 c={}; u32x4 fa[8],fb[8];
#pragma unroll
      for(int u=0;u<8;u++){ const int o=(u>>1)*4096+(u&1)*1024; fa[u]=tr2(k0+L_PHI+tt*2048+o,k1+L_PHI+tt*2048+o); fb[u]=tr2(k0+L_VI+o,k1+L_VI+o); }
      SB();
#pragma unroll
      for(int u=0;u<8;u++) c=mfma(fa[u],fb[u],c);
      st16(pk8(c,0),rkv,l16,i*4096+tt*2048); st16<1024>(pk8(c,8),rkv,l16,i*4096+tt*2048);
      lsig(cnt+8);
      STAMP(4);
    } else {
      if(tt==2) asm volatile("buffer_load_dword %0, %1, %2, 0 offen sc1":"=v"(flv):"v"(vfl),"s"(rf):"memory");
      sfor<W1E,20>([&](auto K){ w1d(K.v,2*(tt&1)); w1d(K.v,2*(tt&1)+1); });
      if(tt==2){
        asm volatile("s_waitcnt vmcnt(%1)":"+v"(flv):"n"(2*(20-W1E)):"memory");
        NOUNR for(int it=0;it<SPIN;it++){
          if(__builtin_amdgcn_ballot_w64(lane<32&&flv!=tag)==0) break;
          asm volatile("buffer_load_dword %0, %1, %2, 0 offen sc1\n\ts_waitcnt vmcnt(0)":"+v"(flv):"v"(vfl),"s"(rf):"memory"); }
        lsig(cnt+12); } }
    lwait(cnt+8,2);
    STAMP(5);
    sfor<20,28>([&](auto K){ w1d(K.v,tt); });
    if(tt<2){ WAITV(8); lsig(cnt+12); }
    STAMP(6);
    lwait(cnt+12,3);
    STAMP(8);
    u32x4 g[16]; const u32 rk=4*i3;
    sfor<0,16>([&](auto N){ g[N.v]=ldc<0>(l16,rkv,((N.v+rk)&15)*4096+tt*1024); });
    STAMP(42);
    rdw1a();
    WAITV(16); lsig(cnt+28);
    misc();
    STAMP(7);
    f32x4 s[4]; const u32x2 id4=mkid();
    const int rr0=chain<16>(g,[&]{ s[0]=s[1]=s[2]=s[3]=f32x4{}; },[&](auto N){ waitr<17-N.v>(g[N.v]); },[&](auto N){ acc8(id4,g[N.v],s[2*(N.v&1)],s[2*(N.v&1)+1]); PINW(s[2*(N.v&1)]); PINW(s[2*(N.v&1)+1]); },[](auto){},
      [&](auto N){ rld<0>(g[N.v],l16,rkv,((N.v+rk)&15)*4096+tt*1024); });
    CNT(37,rr0);
    LDS(u32x4,L+L_KVX+tt*1024+l16)=pk44(s[0]+s[2],s[1]+s[3]);
    PRIO(0);
  } else {
#pragma unroll
    for(int r=0;r<16;r++) c1[r]*=rstd;
    u32x4 v0=pk8(c1,0),v1=pk8(c1,8); if(hf){ v1[2]=0x3F80u; v1[3]=0u; }
    LDS(u32x4,L+L_VI+wro+c0)=v0; LDS(u32x4,L+L_VI+wro+(c0^16))=v1;
    lsig(cnt);
    STAMP(3);
    sfor<nqc(5),7>([&](auto U){ wq(U.v); });
    sfor<0,W1E>([&](auto K){ w1d(K.v,tt); });
    STAMP(40);
    waitv<W1E>(); lsig(cnt+4);
    u32x4 fq[28]; float cq=0;
#pragma unroll
    for(int u=0;u<7;u++) fq[4*u]=tr2(aQ[0]+u*4096,aQ[0]+u*4096+256);
    lwait(cnt+4,4);
    STAMP(4);
    auto ord=[](int s){ return s<7?4*s:((s-7)/3)*4+1+(s-7)%3; };
    f32x16 cQ={};
    sfor<0,28>([&](auto S){ constexpr int s=S.v;
      cQ=mfma(fq[ord(s)],xf[ord(s)],cQ); PINW(cQ); SB();
      if constexpr(s<21){ constexpr int n=ord(s+7); fq[n]=tr2(aQ[n&3]+(n>>2)*4096,aQ[n&3]+(n>>2)*4096+256); }
      if constexpr(s<7){ cq=sum8(fq[4*s],cq); PINW(cq); }
      if constexpr(s==7) LDS(float,L+L_MISC+l16+tt*4)=cq;
      if constexpr(s==22) lsig(cnt+16);
      SB(); });
    lwait(cnt+16,4);
    const f32x4 z=LDS(f32x4,L+L_MISC+l16);
    cQ=__builtin_amdgcn_mfma_f32_32x32x2f32((z[0]+z[1])+(z[2]+z[3]),nmean,cQ,0,0,0);
    f32x16 p=mfma(wp[0],pk8(cQ,0),f32x16{}); p=mfma(wp[1],pk8(cQ,8),p); PINW(p);
    STAMP(5);
    dsoftmax(p,rstd*LOG2E,phq);
    PINW(phq[0]); PINW(phq[1]); PINW(phq[2]); PINW(phq[3]);
    WAITV(0); lsig(cnt+20);
    STAMP(6);
    rdw1a();
    lwait(cnt+28,4); rdw1(IC<1>{});
    lwait(cnt+24,8);
    STAMP(42);
    misc();
    { const u32 rw_=ROT?((7*i3)>>1)+XR*(i>>2):0; sfor<0,14>([&](auto U){ const u32 u=tt*14+wrap(U.v+rw_,14); dma(L_WO+u*1024,vw2,r0,u*7168+cs*128); }); }
    STAMP(7);
    PRIO(2);
  }
  BAR();
  STAMP(9);
  if(ty){ f32x16 c={};
#pragma unroll
    for(int n=0;n<4;n++) c=mfma(LDS(u32x4,L+L_KVX+n*1024+l16),phq[n],c);
    const float rd=__builtin_amdgcn_rcpf(swp_hi(c[12])+1e-6f);
#pragma unroll
    for(int r=0;r<16;r++) c[r]*=rd;
    ost(pk8(c,0),pk8(c,8));
    PRIO(0);
    WAITV(3); lsig(cnt+32);
  }
  else rdw1(IC<1>{});
  STAMP(10);
  if(!act&&w==0) __builtin_amdgcn_fence(__ATOMIC_RELEASE,"");
  if(act){
  { const u32 so=(2*th+tt2)*28672+kq*7168;
    lwait(cnt+32,4);
    if(!ty){ pzh(); W2D(w*4096,0,0); W2D(w*4096+2048,1,0); W2D(w*4096+16384,0,4*224*896); W2D(w*4096+18432,1,4*224*896); }
    lptr p0=a0+L_WO+kq*14336,p1=a1+L_WO+kq*14336; PINW(p0); PINW(p1);
    u32x4 fa[7][2]; f32x16 c[2];
#pragma unroll
    for(int n=0;n<7;n++){ fa[n][0]=tr2(p0+n*2048,p0+n*2048+512); fa[n][1]=tr2(p1+n*2048,p1+n*2048+512); PINW(fa[n][0]); PINW(fa[n][1]); }
    SB();
    STAMP(11);
    if(ty) WAITV(0); else WAITV(16);
    asm volatile("":"+v"(xr),"+v"(b1v)::"memory");
    BAR();
    STAMP(12);
    u32x4 of[7];
    sfor<0,7>([&](auto N){ of[N.v]=ldc<(N.v&3)*1024>(l16,ro,so+(N.v>>2)*4096); });
    auto r2d=[&](int n){ dma(L_R2+(n>>1)*16384+w*2048+(n&1)*1024,vw2,r2,sW2+wrap(2+(n>>1)+r14,14)*14336+(n&1)*7168); };
    STAMP(41);
    const int rr1=chain<7>(of,[&]{ c[0]=c[1]=f32x16{}; },[&](auto N){ waitr<6>(of[N.v]); },[&](auto N){ c[0]=mfma(fa[N.v][0],of[N.v],c[0]); PINW(c[0]); c[1]=mfma(fa[N.v][1],of[N.v],c[1]); PINW(c[1]); },[&](auto N){ r2d(N.v); if(N.v==6) r2d(7); },
      [&](auto N){ rld<(N.v&3)*1024>(of[N.v],l16,ro,so+(N.v>>2)*4096); });
    SB();
    CNT(38,rr1);
    lptr e=L+L_PX+w*4096+l16;
    LDS(u32x4,e)=pk8(c[0],0); LDS(u32x4,e+1024)=pk8(c[0],8); LDS(u32x4,e+2048)=pk8(c[1],0); LDS(u32x4,e+3072)=pk8(c[1],8);
    STAMP(13);
    BAR();
    e=L+L_PX+tt2*4096+vf;
    const u32x2 id4=mkid(); f32x4 s[4]={}; acc8(id4,xr,s[0],s[1]);
#pragma unroll
    for(int q=0;q<4;q++) acc8(id4,LDS(u32x4,e+q*8192),s[2*(q&1)],s[2*(q&1)+1]);
    s[0]+=s[2]; s[1]+=s[3];
    st16(pk44(s[0],s[1]),rh,vf,(2*th+tt2)*28672+cs*4096);
    float s1=((s[0][0]+s[0][1])+(s[0][2]+s[0][3]))+((s[1][0]+s[1][1])+(s[1][2]+s[1][3])), s2=s[0][0]*s[0][0], s3=s[1][0]*s[1][0];
#pragma unroll
    for(int q=1;q<4;q++){ s2=__builtin_fmaf(s[0][q],s[0][q],s2); s3=__builtin_fmaf(s[1][q],s[1][q],s3); }
    s1=red8(s1); s2=red8(s2+s3);
    asm volatile("buffer_store_dwordx2 %0, %1, %2, %3 offen sc1"::"v"(f32x2{s1,s2}),"v"(lane<8?lane*64:OOB),"s"(rst),"s"(((2*th+tt2)*32+kq*8)*64+cs*8):"memory");
    SB(); }
  STAMP(14);
  if(b==0&&tid==0) __hip_atomic_store((u32*)ws,tag,__ATOMIC_RELAXED,__HIP_MEMORY_SCOPE_AGENT);
  const u32 bX=ty?tt*6144:L_S+tt*12288, bY=ty?28672+tt*6144:bX+6144;
  W2D(L_R3+w*2048,6,0);
  { lptr p0=a0+w*4096,p1=a1+w*4096;
#pragma unroll
    for(int n=0;n<2;n++){ w2f[n][0]=tr2(p0+n*2048,p0+n*2048+512); w2f[n][1]=tr2(p1+n*2048,p1+n*2048+512); } }
  WAITV(4);
  { lptr p0=a0+L_R2+w*2048,p1=a1+L_R2+w*2048; PINW(p0); PINW(p1);
#pragma unroll
    for(int n=0;n<4;n++){ w2f[2+n][0]=tr2(p0+n*16384,p0+n*16384+512); w2f[2+n][1]=tr2(p1+n*16384,p1+n*16384+512); }
    asm volatile("s_waitcnt lgkmcnt(0)":"+v"(w2f[4][0]),"+v"(w2f[4][1])::"memory"); }
  W2D(L_R4+w*2048,7,0);
  WAITV(4);
  BAR();
  STAMP(15);
  const u32 sH=ty*57344+tt*1024, lH0=ty*28672+tt*1024, lH1=(ty?57344:86016)+tt*1024;
  if(ty) __builtin_amdgcn_s_sleep(4);
  u32x4 hb[28],sa,sb; float ks[2][5]; const u32 vst=t*64+hf*32;
  auto dm1=[&](u32 l,int k,int u){ dmac(l+u*4096,l16,rh,sH+k*28672+wrap(u+r7,7)*4096); };
  auto dmas=[&](u32 l,int k){ sfor<0,7>([&](auto U){ dm1(l,k,U.v); }); };
  dmas(lH0,0); sa=ldc<0>(vst,rst,ty*4096); sb=ldc<16>(vst,rst,ty*4096);
  float cb=0;
  if(ty){
#pragma unroll
    for(int n=14;n<28;n++) cb=sum8(w1f[n],cb);
    asm volatile("; b":"+v"(cb));
  } else {
#pragma unroll
    for(int n=0;n<14;n++) cb=sum8(w1f[n],cb);
    asm volatile("; a":"+v"(cb)); }
  LDS(float,L+(ty?86016:57344)+tt*1024+lane*4)=cb; SB();
  STAMP(36);
  sfor<0,3>([&](auto U){ W2D(L_S+tt*12288+ty*6144+U.v*2048,8+3*ty+U.v,-ty*4*224*896); });
  WAITV(15);
  { lptr p0=a0+L_R4+w*2048,p1=a1+L_R4+w*2048; PINW(p0); PINW(p1); w2f[7][0]=tr2(p0,p0+512); w2f[7][1]=tr2(p1,p1+512);
    w2f[6][0]=tr2(p0+(L_R3-L_R4),p0+(L_R3-L_R4)+512); w2f[6][1]=tr2(p1+(L_R3-L_R4),p1+(L_R3-L_R4)+512); }
  auto vrd=[&](u32 l){ lptr p=L+l+l16; PINW(p);
#pragma unroll
    for(int n=0;n<7;n++) hb[4*n]=LDS(u32x4,p+n*4096);
    if(hf){ sb[2]=0; sb[3]=0; } };
  auto vbad=[&]{ u32 m=mx4(mx4(0,sa),sb);
#pragma unroll
    for(int n=0;n<7;n++) m=mx4(m,hb[4*n]);
    return bad(m); };
  auto vfix=[&](u32 l,int k){
    NOUNR for(int it=0;it<SPIN;it++){
      sfor<0,7>([&](auto U){ if(chk(hb[4*U.v])) dm1(l,k,U.v); });
      if(bad(mx4(mx4(0,sa),sb))){ rld<0>(sa,vst,rst,ty*4096+k*2048); rld<16>(sb,vst,rst,ty*4096+k*2048); }
      asm volatile("s_waitcnt vmcnt(0)":"+v"(sa),"+v"(sb)::"memory");
      vrd(l); if(!vbad()) break; } };
  auto vst_=[&](float* o){
    const float s1=swp_add((u2f(sa[0])+u2f(sa[2]))+(u2f(sb[0])+u2f(sb[2])))*(-1.f/448), s2=swp_add((u2f(sa[1])+u2f(sa[3]))+(u2f(sb[1])+u2f(sb[3])));
    const float g_=__builtin_fmaf(__builtin_fmaxf(__builtin_fmaf(s2,1.f/448,-s1*s1),0.f),1.00001f,1e-10f);
    const float rs_=__builtin_amdgcn_rsqf(g_); o[3]=0.5f*__builtin_amdgcn_logf(g_);
    o[0]=g_*rs_; o[1]=-2.302208198f*rs_; o[2]=-0.1029432397f*rs_*rs_*rs_; o[4]=hf?o[0]:s1; };
  auto vpin=[&](float* o){ PINW(o[0]); PINW(o[1]); PINW(o[2]); PINW(o[3]); PINW(o[4]); };
  if(ty) BAR();
  asm volatile("s_waitcnt vmcnt(6)":"+v"(sa),"+v"(sb)::"memory"); vrd(lH0); vst_(ks[0]); if(vbad()){ vfix(lH0,0); vst_(ks[0]); } vpin(ks[0]);
  if(!ty) BAR();
  STAMP(16);
  cb=swp_add(cb+LDS(float,L+lH1+lane*4)); cb=hf?u2f(b1v<<16):cb; PINW(cb);
  dmas(lH1,1); sa=ldc<0>(vst,rst,ty*4096+2048); sb=ldc<16>(vst,rst,ty*4096+2048);
  if(ty) BAR();
  sfor<0,2>([&](auto KK){ constexpr int k=KK.v;
    lptr q[4];
#pragma unroll
    for(int u=1;u<4;u++){ q[u]=L+(k?lH1:lH0)+(((u+tt)&3)-tt)*1024+l16; PINW(q[u]); }
    f32x16 c={};
    auto ord=[](int s){ return s<7?4*s:((s-7)/3)*4+1+(s-7)%3; };
    PRIO(1);
    sfor<0,28>([&](auto S){ constexpr int s=S.v;
      c=mfma(w1f[ord(s)],hb[ord(s)],c); PINW(c); SB();
      if constexpr(s+LA>=7&&s+LA<28) hb[ord(s+LA)]=LDS(u32x4,q[ord(s+LA)&3]+(ord(s+LA)>>2)*4096);
      if constexpr(k==0&&s%8==2&&s<24){ if(ty) W2D(bX+(s/8)*2048,8+s/8,0); }
      SB(); });
    c=__builtin_amdgcn_mfma_f32_32x32x2f32(cb,ks[k][4],c,0,0,0); PINW(c);
    PRIO(0);
    SB(); if(k==0||!ty) BAR(); PINW(c); SB();
    STAMP(17+2*k);
    if constexpr(k==0){ if(ty){ W2D(bY,11,0); W2D(bY+2048,12,0); W2D(bY+4096,13,0); if(tid==256) LDS(u32,L+L_SIG)=0; WAITV(12); } else WAITV(0);
      asm volatile("":"+v"(sa),"+v"(sb)::"memory"); vrd(lH1); vst_(ks[1]); }
    u32 g[8]; const float sg_=ks[k][0],K1=ks[k][1],K2=ks[k][2],Lg=ks[k][3];
#pragma unroll
    for(int p=0;p<8;p++){ float a=c[2*p],d=c[2*p+1];
      float u=__builtin_fmaf(a,__builtin_fmaf(a*a,K2,K1),Lg), v=__builtin_fmaf(d,__builtin_fmaf(d*d,K2,K1),Lg);
      u=__builtin_amdgcn_rcpf(__builtin_amdgcn_exp2f(u)+sg_); v=__builtin_amdgcn_rcpf(__builtin_amdgcn_exp2f(v)+sg_);
      g[p]=pk(a*u,d*v);
      if(p==3) st16(u32x4{g[0],g[1],g[2],g[3]},rhh,l16,(2*ty+k)*114688+(8*j+2*tt)*1024); }
    st16<1024>(u32x4{g[4],g[5],g[6],g[7]},rhh,l16,(2*ty+k)*114688+(8*j+2*tt)*1024);
    if constexpr(k==0){ if(vbad()){ vfix(lH1,1); vst_(ks[1]); } vpin(ks[1]);
      BAR(); STAMP(18); }
    SB();
  });
  { const u32 s5=th*229376+w*14336, dY=th?-114688u:344064u; const int inl=ty|th, tl=tt2?3-2*th:2*th;
    auto su=[&](int n){ return s5+wrap(n+r14,14)*1024; };
    u32x4 hh[14],hp,q2;
    auto rdw2=[&]{ lptr p0=a0+bX,p1=a1+bX; PINW(p0); PINW(p1);
#pragma unroll
      for(int n=0;n<3;n++){ w2f[8+n][0]=tr2(p0+n*2048,p0+n*2048+512); w2f[8+n][1]=tr2(p1+n*2048,p1+n*2048+512); }
      p0=a0+bY; p1=a1+bY; PINW(p0); PINW(p1);
#pragma unroll
      for(int n=0;n<3;n++){ w2f[11+n][0]=tr2(p0+n*2048,p0+n*2048+512); w2f[11+n][1]=tr2(p1+n*2048,p1+n*2048+512); } };
    STAMP(20);
    sfor<0,14>([&](auto N){ hh[N.v]=ldc<0>(l16,rhh,su(N.v)); });
    WAITV(18); rdw2();
#pragma unroll
    for(int n=8;n<14;n++){ PINW(w2f[n][0]); PINW(w2f[n][1]); }
    if(ty!=th) WAITV(14);
    if(ty) lsig(L+L_SIG);
    STAMP(21);
    f32x16 c[2]; auto zc=[&]{ c[0]=c[1]=f32x16{}; };
    auto mm=[&](auto N){ c[0]=mfma(w2f[N.v][0],hh[N.v],c[0]); PINW(c[0]); c[1]=mfma(w2f[N.v][1],hh[N.v],c[1]); PINW(c[1]); };
    const int rr2=chain<14>(hh,zc,[&](auto N){ waitb<13,13-N.v>(hh[N.v],inl); },mm,[&](auto N){ if constexpr(XS==1&&N.v%2==0&&N.v<12) STAMP(26+N.v/2); rldb(hh[N.v],l16,rhh,su(N.v)+dY,inl); },
      [&](auto N){ rld<0>(hh[N.v],l16,rhh,su(N.v)); });
    STAMP(22); CNT(39,rr2);
    lptr e=L+bX+l16; PINW(e);
    LDS(u32x4,e)=pk8(c[0],0); LDS(u32x4,e+1024)=pk8(c[0],8); LDS(u32x4,e+2048)=pk8(c[1],0); LDS(u32x4,e+3072)=pk8(c[1],8);
    if(!inl){ lwait(L+L_SIG,4); sfor<0,14>([&](auto N){ rld<0>(hh[N.v],l16,rhh,su(N.v)+dY); }); }
    hp=ldc<0>(vf,rh,tl*28672+cs*4096); q2=ldp(v8*16,mk(b2,896),cs*128);
    STAMP(23);
    const int rr3=chain<14>(hh,zc,[&](auto N){ waitr<15-N.v>(hh[N.v]); },mm,[](auto){},
      [&](auto N){ rld<0>(hh[N.v],l16,rhh,su(N.v)+dY); });
    STAMP(24); CNT(47,rr3);
    e=L+bY+l16; PINW(e);
    LDS(u32x4,e)=pk8(c[0],0); LDS(u32x4,e+1024)=pk8(c[0],8); LDS(u32x4,e+2048)=pk8(c[1],0); LDS(u32x4,e+3072)=pk8(c[1],8);
    asm volatile("s_waitcnt vmcnt(0)":"+v"(q2),"+v"(hp)::"memory");
    BAR();
    lptr e0=L+tt2*28672+vf,e1=L+L_S+tt2*6144+vf; PINW(e0); PINW(e1);
    const u32x2 id4=mkid(); f32x4 s[4]={}; acc8(id4,hp,s[0],s[1]); acc8(id4,q2,s[2],s[3]);
#pragma unroll
    for(int q=0;q<4;q++){ acc8(id4,LDS(u32x4,e0+q*6144),s[0],s[1]); acc8(id4,LDS(u32x4,e1+q*12288),s[2],s[3]); }
    __builtin_amdgcn_raw_buffer_store_b128(pk44(s[0]+s[2],s[1]+s[3]),mk(out+(size_t)i*128*448,128*896),vo,(tl*32+kq*8)*896+cs*128,16); }
  }
  STAMP(25);
}
}
#ifdef __HIP_DEVICE_COMPILE__
#pragma clang attribute pop
#endif
extern "C" void kernel_launch(void* const* d_in,const int* in_sizes,int n_in,void* d_out,int out_size,void* d_ws,size_t ws_size,hipStream_t stream){
  typedef const unsigned short* P;
  (void)hipFuncSetAttribute((const void*)fk::fused,hipFuncAttributeMaxDynamicSharedMemorySize,163840);
  fk::fused<<<256,512,163840,stream>>>((P)d_in[0],(P)d_in[1],(P)d_in[2],(P)d_in[3],(P)d_in[5],(P)d_in[6],(P)d_in[7],(P)d_in[8],(unsigned short*)d_out,(char*)d_ws);
}
```

```cpp
#define PROBE 1
#define PA 0
#define PB 25
#define PW 0
#define PBLK 67
#define PMUL 1
#include <hip/hip_runtime.h>
#include <stdint.h>
#ifdef __HIP_DEVICE_COMPILE__
#pragma clang attribute push(__attribute__((target("no-packed-fp32-ops"))),apply_to=function)
#endif
namespace fk {
typedef unsigned short u16; typedef unsigned u32;
typedef __attribute__((ext_vector_type(4))) u32 u32x4;
typedef __attribute__((ext_vector_type(2))) u32 u32x2;
typedef __attribute__((ext_vector_type(8))) __bf16 bf16x8;
typedef __attribute__((ext_vector_type(2))) __bf16 bf16x2;
typedef __attribute__((ext_vector_type(2))) float f32x2;
typedef __attribute__((ext_vector_type(4))) float f32x4;
typedef __attribute__((ext_vector_type(16))) float f32x16;
typedef short v4i16_t __attribute__((ext_vector_type(4)));
typedef __attribute__((address_space(3))) char* lptr;
typedef __amdgpu_buffer_rsrc_t rsrc_t;
#define DI __device__ __forceinline__
#define LDS(T,a) (*(__attribute__((address_space(3))) T*)(a))
#define STAMP(n) do{ if((n)==PA||(n)==PB){ uint64_t t_; asm volatile("s_memrealtime %0\n\ts_waitcnt lgkmcnt(0)":"=s"(t_)::"memory"); if((n)==PA) pa_=(u32)t_; if((n)==PB) pb_=(u32)t_; } }while(0)
#define CNT(n,v)
#define CST(n) do{ if((n)==46||(n)==47){ uint64_t t_; asm volatile("s_memtime %0\n\ts_waitcnt lgkmcnt(0)":"=s"(t_)::"memory"); if((n)==46) ca_=(u32)t_; else cb_=(u32)t_; } }while(0)
constexpr u32 WS_FLAG=4096, WS_ST=512u<<10, WS_KV=1u<<20, WS_O=2u<<20, WS_H=4u<<20, WS_HH=8u<<20, OOB=0x80000000u;
constexpr int L_WK=98304, L_MISC=155648, L_CNT=L_MISC+2048, L_ONE=L_CNT+64, L_KVX=159744;
constexpr int L_PHI=32768, L_VI=49152, L_WO=98304, L_R2=32768, L_R3=98304, L_R4=65536, L_S=114688, L_SIG=53248, L_PX=131072;
constexpr int w1l(int ks){ return ks<12?98304+ks*4096:ks<20?65536+(ks-12)*4096:32768+(ks-20)*4096; }
constexpr int xs(int c){ return (c%6==0?4:c%6==5?5:c%6-1)*16384; }
constexpr float LOG2E=1.4426950408889634f;
#define DEP 3
#define PZS 3
#define NQE 4
#define W1E 12
#define LA 6
#define R14J 2
#define XR 0
#define XS 0
#define ROT 1
constexpr int yng(int c){ return ((c-1+DEP<6?c-1+DEP:6)-c)*3; }
constexpr int nq(int s){ return s+DEP<=6||s<4?0:s==4?(NQE<4?NQE:4):s==5?(NQE>4?NQE-4:0):0; }
constexpr int nqc(int s){ return s<4?0:nqc(s-1)+nq(s); }
#define NOUNR _Pragma("clang loop unroll(disable)")
DI float u2f(u32 v){ return __builtin_bit_cast(float,v); }
DI u32 f2u(float v){ return __builtin_bit_cast(u32,v); }
template<int V> struct IC{ static constexpr int v=V; };
template<int I,int N,class F> DI void sfor(F&& f){ if constexpr(I<N){ f(IC<I>{}); sfor<I+1,N>(f); } }
DI rsrc_t mk(const void* p,u32 bytes){ return __builtin_amdgcn_make_buffer_rsrc((void*)p,0,bytes,0x00020000); }
DI void dma(u32 l,u32 voff,rsrc_t r,u32 soff){ asm volatile("s_mov_b32 m0, %0\n\ts_nop 0\n\tbuffer_load_dwordx4 %1, %2, %3 offen lds"::"s"(l),"v"(voff),"s"(r),"s"(soff):"memory"); }
#define WAITV(n) asm volatile("s_waitcnt vmcnt(" #n ")":::"memory")
#define BAR() asm volatile("s_waitcnt lgkmcnt(0)\n\ts_barrier":::"memory")
#define PIN(x) asm volatile(""::"v"(x))
#define PINW(x) asm volatile("":"+v"(x))
#define SB() __builtin_amdgcn_sched_barrier(0)
DI f32x16 mfma(u32x4 a,u32x4 b,f32x16 c){ return __builtin_amdgcn_mfma_f32_32x32x16_bf16(__builtin_bit_cast(bf16x8,a),__builtin_bit_cast(bf16x8,b),c,0,0,0); }
DI f32x4 m44(u32x2 a,u32x2 b,f32x4 c){ return __builtin_amdgcn_mfma_f32_4x4x4bf16_1k(__builtin_bit_cast(v4i16_t,a),__builtin_bit_cast(v4i16_t,b),c,0,0,0); }
DI u32x2 tr(lptr p){ return __builtin_bit_cast(u32x2,__builtin_amdgcn_ds_read_tr16_b64_v4i16((__attribute__((address_space(3))) v4i16_t*)p)); }
DI u32x4 tr2(lptr p0,lptr p1){ u32x2 a=tr(p0),b=tr(p1); return u32x4{a[0],a[1],b[0],b[1]}; }
DI u32 pk(float lo,float hi){ return __builtin_bit_cast(u32,__builtin_convertvector(f32x2{lo,hi},bf16x2)); }
DI float dot2(u32 a,u32 b,float c){ return __builtin_amdgcn_fdot2_f32_bf16(__builtin_bit_cast(bf16x2,a),__builtin_bit_cast(bf16x2,b),c,false); }
DI float swp_lo(float v){ auto r=__builtin_amdgcn_permlane32_swap(f2u(v),f2u(v),false,false); return u2f(r[0]); }
DI float swp_hi(float v){ auto r=__builtin_amdgcn_permlane32_swap(f2u(v),f2u(v),false,false); return u2f(r[1]); }
DI float swp_add(float v){ auto r=__builtin_amdgcn_permlane32_swap(f2u(v),f2u(v),false,false); return u2f(r[0])+u2f(r[1]); }
DI float swp_max(float v){ auto r=__builtin_amdgcn_permlane32_swap(f2u(v),f2u(v),false,false); return __builtin_fmaxf(u2f(r[0]),u2f(r[1])); }
DI float swp_min(float v){ auto r=__builtin_amdgcn_permlane32_swap(f2u(v),f2u(v),false,false); return __builtin_fminf(u2f(r[0]),u2f(r[1])); }
DI float red8(float v){ v+=u2f(__builtin_amdgcn_update_dpp(0u,f2u(v),0x128,0xf,0xf,false)); auto r=__builtin_amdgcn_permlane16_swap(f2u(v),f2u(v),false,false); return swp_add(u2f(r[0])+u2f(r[1])); }
DI u32x4 pk8(const f32x16& c,int b){ return u32x4{pk(c[b],c[b+1]),pk(c[b+2],c[b+3]),pk(c[b+4],c[b+5]),pk(c[b+6],c[b+7])}; }
DI void dsoftmax(const f32x16& p,float c,u32x4* ph){
  float mx=p[0],mn=p[0];
#pragma unroll
  for(int r=1;r<14;r++){ mx=__builtin_fmaxf(mx,p[r]); mn=__builtin_fminf(mn,p[r]); }
  mx=swp_max(mx); mn=swp_min(mn);
  const float a=-mx*c,b=mn*c; f32x16 e,g; float s=0,u=0;
#pragma unroll
  for(int r=0;r<14;r++){ e[r]=__builtin_amdgcn_exp2f(__builtin_fmaf(p[r],c,a)); g[r]=__builtin_amdgcn_exp2f(__builtin_fmaf(p[r],-c,b)); s+=e[r]; u+=g[r]; }
  s=__builtin_amdgcn_rcpf(swp_add(s)); u=__builtin_amdgcn_rcpf(swp_add(u));
#pragma unroll
  for(int r=0;r<14;r++){ e[r]*=s; g[r]*=u; }
  ph[0]=pk8(e,0); ph[1]=u32x4{pk(e[8],e[9]),pk(e[10],e[11]),pk(e[12],e[13]),0u};
  ph[2]=pk8(g,0); ph[3]=u32x4{pk(g[8],g[9]),pk(g[10],g[11]),pk(g[12],g[13]),0u};
}
DI void dmac(u32 l,u32 voff,rsrc_t r,u32 soff){ asm volatile("s_mov_b32 m0, %0\n\ts_nop 0\n\tbuffer_load_dwordx4 %1, %2, %3 offen sc1 lds"::"s"(l),"v"(voff),"s"(r),"s"(soff):"memory"); }
template<int O> DI void rld(u32x4& v,u32 voff,rsrc_t r,u32 soff){ asm volatile("buffer_load_dwordx4 %0, %1, %2, %3 offen offset:%4 sc1":"+v"(v):"v"(voff),"s"(r),"s"(soff),"n"(O):"memory"); }
template<int O> DI u32x4 ldc(u32 voff,rsrc_t r,u32 soff){ u32x4 v; asm volatile("buffer_load_dwordx4 %0, %1, %2, %3 offen offset:%4 sc1":"=v"(v):"v"(voff),"s"(r),"s"(soff),"n"(O):"memory"); return v; }
DI u32x4 ldp(u32 voff,rsrc_t r,u32 soff){ u32x4 v; asm volatile("buffer_load_dwordx4 %0, %1, %2, %3 offen":"=v"(v):"v"(voff),"s"(r),"s"(soff):"memory"); return v; }
template<int N> DI void waitv(){ asm volatile("s_waitcnt vmcnt(%0)"::"n"(N):"memory"); }
template<int O=0> DI void st16(u32x4 v,rsrc_t r,u32 voff,u32 soff){ asm volatile("buffer_store_dwordx4 %0, %1, %2, %3 offen offset:%4 sc1\n\ts_nop 1"::"v"(v),"v"(voff),"s"(r),"s"(soff),"n"(O):"memory"); }
DI u32 mx3(u32 a,u32 b,u32 c){ u32 r; asm("v_max3_u32 %0, %1, %2, %3":"=v"(r):"v"(a),"v"(b),"v"(c)); return r; }
DI u32 mx4(u32 m,u32x4 g){ return mx3(mx3(m,g[0],g[1]),g[2],g[3]); }
DI bool bad(u32 m){ return __builtin_amdgcn_ballot_w64(m==0xFFFFFFFFu)!=0; }
constexpr int SPIN=30000;
DI void lsig(lptr p){ asm volatile("s_mov_b64 exec, 1\n\tds_add_u32 %0, %1\n\ts_mov_b64 exec, -1"::"v"(p),"v"(1u):"memory"); }
DI void lwait(lptr p,u32 n){ NOUNR for(int it=0;it<SPIN*8;it++){ u32 v; asm volatile("ds_read_b32 %0, %1\n\ts_waitcnt lgkmcnt(0)":"=v"(v):"v"(p):"memory"); if(__builtin_amdgcn_readfirstlane(v)>=n) break; } }
DI float sum8(u32x4 f,float c){ c=dot2(f[0],0x3F803F80u,c); c=dot2(f[1],0x3F803F80u,c); c=dot2(f[2],0x3F803F80u,c); return dot2(f[3],0x3F803F80u,c); }
DI float sq8(u32x4 f,float c){ c=dot2(f[0],f[0],c); c=dot2(f[1],f[1],c); c=dot2(f[2],f[2],c); return dot2(f[3],f[3],c); }
DI void acc8(u32x2 id4,u32x4 g,f32x4& a,f32x4& b){ a=m44(id4,u32x2{g[0],g[1]},a); b=m44(id4,u32x2{g[2],g[3]},b); }
DI u32x4 pk44(f32x4 a,f32x4 b){ return u32x4{pk(a[0],a[1]),pk(a[2],a[3]),pk(b[0],b[1]),pk(b[2],b[3])}; }
DI bool chk(u32x4 g){ return bad(mx3(mx3(g[0],g[1],g[2]),g[3],g[3])); }
template<int N> DI void waitr(u32x4& v){ asm volatile("s_waitcnt vmcnt(%1)":"+v"(v):"n"(N):"memory"); }
template<int A,int B> DI void waitb(u32x4& v,int c){ asm volatile("s_cmp_lg_u32 %1, 0\n\ts_waitcnt vmcnt(%2)\n\ts_cbranch_scc1 1f\n\ts_waitcnt vmcnt(%3)\n1:":"+v"(v):"s"(c),"n"(A),"n"(B):"memory","scc"); }
DI void rldb(u32x4& v,u32 voff,rsrc_t r,u32 soff,int c){ asm volatile("s_cmp_eq_u32 %4, 0\n\ts_cbranch_scc1 1f\n\tbuffer_load_dwordx4 %0, %1, %2, %3 offen sc1\n1:":"+v"(v):"v"(voff),"s"(r),"s"(soff),"s"(c):"memory","scc"); }
DI u32x4 mkpz(){ u32x4 p={~0u,~0u,~0u,~0u}; PINW(p); return p; }
DI u32 wrap(u32 v,u32 n){ return v>=n?v-n:v; }
template<int N,class I,class W,class U,class X,class R> DI int chain(u32x4* g,I&& init,W&& wt,U&& use,X&& ex,R&& re){
  int it=0;
  NOUNR for(;it<SPIN;it++){
    init(); u32 m=0;
    sfor<0,N>([&](auto K){ wt(K); use(K); m=mx4(m,g[K.v]); PINW(m); ex(K); SB(); });
    if(!bad(m)) break;
    WAITV(0); sfor<0,N>(re); WAITV(0); }
  return it; }
#define PRIO(n) __builtin_amdgcn_s_setprio(n)

__global__ __launch_bounds__(512,2) void fused(const u16* __restrict__ x,const u16* __restrict__ wqkv,const u16* __restrict__ wphi,const u16* __restrict__ wo,
    const u16* __restrict__ w1,const u16* __restrict__ b1,const u16* __restrict__ w2,const u16* __restrict__ b2,u16* __restrict__ out,char* __restrict__ ws
    ){
  extern __shared__ __attribute__((aligned(16))) char smem[];
  const lptr L=(lptr)smem;
  const int tid=__builtin_amdgcn_workitem_id_x(),lane=tid&63,w=__builtin_amdgcn_readfirstlane(tid>>6),tt=w&3,ty=w>>2,t=lane&31,hf=lane>>5;
  const int b=__builtin_amdgcn_workgroup_id_x(),i3=(b>>3)&3,jq=b>>5,th=b&1,i=((b&7)>>1)*4+i3;
  const int j1=th*8+jq,odd=j1&1;
  const bool act=jq<7; const int j=th*7+jq,cs=jq;
  u32 pa_=0,pb_=0;
  STAMP(0);
  if(tid==0) __hip_atomic_store((u32*)(ws+(16u<<20)+b*4),pa_,__ATOMIC_RELAXED,__HIP_MEMORY_SCOPE_AGENT);
  asm volatile(""::"s"(x),"s"(wqkv),"s"(wphi),"s"(wo),"s"(w1),"s"(b1),"s"(w2),"s"(b2),"s"(out),"s"(ws));
  const u32 epoch=*(const __attribute__((address_space(4))) u32*)ws;
  const rsrc_t rw=mk(wqkv,448*2688), rx=mk(x+(size_t)i*128*448,128*896);
  const u32 vw=(lane>>2)*2688+(lane&3)*16, hoff=56*j1-8*odd+tt*16*2688, sW=hoff+(ty+1)*896, lW=L_WK+ty*28672+tt*1024;
  const u32 vx0=(lane>>3)*896+(((lane&7)^(lane>>4))<<4), vx1=vx0^64, sX=(tt*32+ty*16)*896, lX=tt*4096+ty*2048;
  const u32 rot=ROT?(u32)(j1+2*i3+XR*(i>>2))%7u:0u;
  auto cc=[&](int c){ return wrap(c+rot,7); };
  auto ch=[&](int c,int u){ const u32 q_=cc(c); if(u==0) dma(lW+c*4096,vw,rw,sW+q_*(64*2688)); else dma(xs(c)+lX+(u-1)*1024,u==1?vx0:vx1,rx,sX+(u-1)*7168+q_*128); };
#define SBAR() asm volatile("s_barrier":::"memory")
  if(w<2) dma(L_MISC+w*1024,tid*16,mk(wphi+j1*784,1568),0);
  const u32 l16=lane*16;
  const rsrc_t ro=mk(ws+WS_O+i*114688,114688), rkv=mk(ws+WS_KV+j1*65536,65536), rh=mk(ws+WS_H+i*114688,114688), rst=mk(ws+WS_ST+i*8192,8192);
  auto ost=[&](u32x4 o0,u32x4 o1){ const u32 ub=t*16+((7*j1)>>1)*512, so=tt*28672;
    st16(o0,ro,ub+(odd+2*hf)*512,so); st16(o1,ro,hf?OOB:ub+(odd+1)*512,so);
    __builtin_amdgcn_raw_buffer_store_b64(u32x2{o1[0],o1[1]},ro,hf?ub+(odd?8:1536):OOB,so,16); };
  STAMP(32);
  sfor<0,DEP>([&](auto C){ if constexpr(C.v>0) SBAR(); ch(C.v,0); ch(C.v,1); ch(C.v,2); STAMP(33+C.v); });
  auto poison=[&]{ const u32x4 PZ=mkpz();
    if(ty){ const int t1=j1>=7; const u32 so=(2*t1+(tt&1))*28672+(4*(j1-7*t1)+(tt>>1))*1024, v=j1<14?l16:OOB; st16(PZ,rh,v,so); st16<2048>(PZ,rh,v,so);
      st16(PZ,rst,lane<8?l16:OOB,(j1*4+tt)*128); }
    else { st16(PZ,rkv,l16,i*4096+tt*1024); ost(PZ,PZ); } };
  if(tid<3) LDS(u32x4,L+L_CNT+tid*16)=u32x4{0,0,0,0};
  if(tid<2) LDS(u32x2,L+L_ONE+tid*256)=u32x2{0x3F80u,0u};
  const int tt2=w&1, kq=w>>1;
  const int q4=(lane&15)>>2,mu=((lane>>4)&1)*4+(lane&3),sg=4*(mu&1)+(mu>>1);
  const int rowo=(8*hf+q4)*64, kslot=(mu+odd)&7;
  lptr aW[4],aQ[4],xb[4];
  { const int sw=hf^((t>>1)&7), sl=ty?(odd?(sg==7?0:((sg+1)%7)+1):sg):kslot;
#pragma unroll
    for(int u=0;u<4;u++){ const int r=(u+tt+2*ty)&3; aW[u]=mu==7?L+L_ONE:L+L_WK+ty*28672+rowo+sl*8+r*1024; aQ[u]=L+rowo+kslot*8+r*1024; xb[u]=L+(tt*32+t)*128+((sw^(r*2))<<4); PINW(aW[u]); PINW(xb[u]); } }
  u32 tag=epoch*0x9E3779B1u+0x7F4A7C15u; asm volatile("":"+s"(tag));
  const rsrc_t r1=mk(w1,act?448*3584:0), r2=mk(w2,act?1792*896:0), r0=mk(wo,act?448*896:0);
  const lptr cnt=L+L_CNT;
  const rsrc_t rf=mk(ws,1u<<20); const u32 vfl=lane<16?WS_FLAG+(j1*16+lane)*128:lane<32?WS_FLAG+((lane-16)*16+i)*128:OOB; u32 flv;
  STAMP(43);
  u32 stp=mu==7?0:4096; PINW(stp);
  const u32 tq2=(tt+2)&3, hq=56*j1-8*odd+tq2*16*2688;
  auto wq=[&](int u){ dma(tq2*1024+u*4096,vw,rw,hq+cc(u)*(64*2688)); };
  u32x4 xf[28],wp[2],fw[2][4]; f32x16 c1={}; float ss=0,s2=0,cz=0;
  auto rd=[&](auto C,int u){ constexpr int c=C.v; fw[c&1][u]=tr2(aW[u],aW[u]+256); xf[4*c+u]=LDS(u32x4,xb[u]+xs(c)); if(c<6) aW[u]+=stp; };
  auto md=[&](auto C,int u){ constexpr int c=C.v; c1=mfma(fw[c&1][u],xf[4*c+u],c1); PINW(c1); SB(); };
  auto dd=[&](auto C,int u){ constexpr int c=C.v;
    if(u==0){ cz=sum8(fw[c&1][0],cz); PINW(cz); } else if(u==1){ ss=sq8(xf[4*c],ss); PINW(ss); } else if(u==2){ s2=sq8(xf[4*c+1],s2); PINW(s2); } };
  sfor<0,7>([&](auto C){ constexpr int c=C.v;
    constexpr int pz=c>PZS&&c<PZS+DEP, fl=c>PZS+DEP;
    if(ty) waitv<yng(c)+3*pz+nqc(c-1)>(); else if(fl&&w==2) waitv<yng(c)+4*pz+1>(); else waitv<yng(c)+4*pz>();
    if constexpr(c<3) STAMP(44+c);
    if constexpr(c==0) BAR(); else SBAR();
    if constexpr(c==0) STAMP(1); else if constexpr(!XS) STAMP(25+c);
    if constexpr(c==PZS+DEP){ if(tid==128) __hip_atomic_store((u32*)(ws+WS_FLAG+(j1*16+i)*128),tag,__ATOMIC_RELAXED,__HIP_MEMORY_SCOPE_AGENT); }
    if constexpr(c==6){ if(!ty) PRIO(2); }
    if constexpr(c==PZS) poison();
    sfor<0,4>([&](auto U){ constexpr int u=U.v;
      if constexpr(c>0&&c<6) md(IC<c-1>{},u);
      rd(C,u);
      if constexpr(c+DEP<=6&&u<3) ch(c+DEP,u);
      if constexpr(u<nq(c)){ if(ty) wq(nqc(c-1)+u); }
      if constexpr(c>0&&c<6) dd(IC<c-1>{},u);
      SB(); });
    if constexpr(c==0){
      const bool mv=(t&26)!=26; const lptr p=L+L_MISC+(t<26?t:t-2)*2+hf*224; u32 e[16];
#pragma unroll
      for(int q=0;q<16;q++){ const int d=16*(q>>3)+8*((q>>2)&1)+(q&3); e[q]=LDS(u16,p+d*56); }
#pragma unroll
      for(int q=0;q<8;q++){ u32 v=e[2*q]|(e[2*q+1]<<16); v=(mv&&(q<6||hf==0))?v:0u; asm volatile("":"+v"(v)); wp[q>>2][q&3]=v; } SB(); }
    if constexpr(c==5) sfor<0,4>([&](auto U){ md(C,U.v); dd(C,U.v); SB(); });
  });
  md(IC<6>{},0); dd(IC<6>{},0); dd(IC<6>{},1); SB(); md(IC<6>{},1); dd(IC<6>{},2);
  LDS(float,L+L_KVX+w*256+lane*4)=ss+s2; LDS(float,L+L_KVX+2048+ty*1024+l16+tt*4)=cz; SB();
  md(IC<6>{},2); md(IC<6>{},3);
  BAR();
  { const f32x4 z=LDS(f32x4,L+L_KVX+2048+ty*1024+l16); const float so_=LDS(float,L+L_KVX+(w^4)*256+lane*4);
    ss=(ss+s2)+so_; cz=(z[0]+z[1])+(z[2]+z[3]); }
  const float nmean=swp_hi(c1[12])*(-1.f/448);
  c1=__builtin_amdgcn_mfma_f32_32x32x2f32(cz,nmean,c1,0,0,0);
  const float rstd=__builtin_amdgcn_rsqf(__builtin_fmaxf(__builtin_fmaf(swp_add(ss),1.f/448,-nmean*nmean),0.f)+1e-5f);
  STAMP(2);
  const int wro=(tt*4096+t*64),c0=((2*hf)^((t>>2)&3))<<4;
  const rsrc_t rhh=mk(ws+WS_HH+i*458752,act?458752:0);
  const u32 v8=(lane>>3), p8=((lane&7)^(lane>>2&4))<<4, vw2=v8*896+p8, sW2=w*224*896+cs*128;
  const u32 r7=ROT?(u32)(((7*i3)>>2)+jq+XR*(i>>3))%7u:0u, r14=ROT?(u32)(((7*i3)>>1)+R14J*jq+XR*(i>>2))%14u:0u;
#define W2D(l,n,o) do{ const u32 s_=sW2+(o)+wrap((n)+r14,14)*14336; dma((l),vw2,r2,s_); dma((l)+1024,vw2,r2,s_+7168); }while(0)
  const int nq=(mu&4)|((mu&1)<<1)|((mu>>1)&1);
  const int ao=(8*hf+q4)*128+(q4>>1)*64+nq*8; const lptr a0=L+ao,a1=L+(ao^64);
  auto mkid=[&]{ u32 l=l16; PINW(l); const u32 v=(l&16)?0x3F800000u:0x3F80u; return u32x2{(l&32)?0u:v,(l&32)?v:0u}; };
  const u32 vo=(lane&7)*896+v8*16, so_=((2*th+tt2)*32+kq*8)*896+cs*128, vf=v8*512+(lane&7)*16+kq*128;
  const u32 vw1=v8*3584+p8;
  auto w1d=[&](int n,int q){ dma(w1l(n)+q*1024,vw1,r1,(q&1)*8*3584+j*256+(q>>1)*128+(wrap((n>>2)+r7,7)*4+(n&3))*16*3584); };
  u32x4 phq[4],w1f[28],w2f[14][2],xr; u32 b1v;
  auto pzh=[&]{ const u32x4 PZ=mkpz(); const u32 sh=(8*j+2*tt)*1024;
    sfor<0,4>([&](auto U){ st16(PZ,rhh,l16,sh+U.v*114688); st16<1024>(PZ,rhh,l16,sh+U.v*114688); }); };
  auto misc=[&]{ xr=ldp(act?vo:OOB,rx,so_);
    const int tq=t>>2; asm volatile("buffer_load_ushort %0, %1, %2, %3 offen":"=v"(b1v):"v"((((tq&4)|((tq&1)<<1)|((tq>>1)&1))*4+(t&3))*2),"s"(mk(b1,act?3584:0)),"s"(j*256+tt*64):"memory"); };
  auto rdw1=[&](auto P){ lptr q[4]; constexpr int n0=P.v?12:0, n1=P.v?20:8, n2=P.v?28:12;
#pragma unroll
    for(int u=0;u<4;u++){ q[u]=L+w1l(n0)+((tt&1)?ao^64:ao)+(tt>>1)*2048+((u+tt)&3)*4096; PINW(q[u]); }
#pragma unroll
    for(int n=n0;n<n1;n++) w1f[n]=tr2(q[n&3]+(w1l(n&~3)-w1l(n0)),q[n&3]+(w1l(n&~3)-w1l(n0))+512);
#pragma unroll
    for(int u=0;u<4;u++){ q[u]+=P.v?-32768:32768; PINW(q[u]); }
#pragma unroll
    for(int n=n1;n<n2;n++) w1f[n]=tr2(q[n&3]+(w1l(n&~3)-w1l(n1)),q[n&3]+(w1l(n&~3)-w1l(n1))+512); };
  auto rdw1a=[&]{ lwait(cnt+20,4); rdw1(IC<0>{}); asm volatile("s_waitcnt lgkmcnt(0)":::"memory"); lsig(cnt+24); };
  if(ty==0){
    f32x16 p=mfma(wp[0],pk8(c1,0),f32x16{}); p=mfma(wp[1],pk8(c1,8),p);
    u32x4 ph[4]; dsoftmax(p,rstd*LOG2E,ph);
    LDS(u32x4,L+L_PHI+wro+c0)=ph[0]; LDS(u32x4,L+L_PHI+wro+(c0^16))=ph[1];
    LDS(u32x4,L+L_PHI+2048+wro+c0)=ph[2]; LDS(u32x4,L+L_PHI+2048+wro+(c0^16))=ph[3];
    lsig(cnt);
    STAMP(3);
    if(tt<2){
      lwait(cnt,8);
      const int ko=(8*hf+q4)*64+(sg&1)*8; const lptr k0=L+ko+(((sg>>1)^(2*hf))<<4),k1=L+ko+256+(((sg>>1)^(2*hf+1))<<4);
      f32x16 c={}; u32x4 fa[8],fb[8];
#pragma unroll
      for(int u=0;u<8;u++){ const int o=(u>>1)*4096+(u&1)*1024; fa[u]=tr2(k0+L_PHI+tt*2048+o,k1+L_PHI+tt*2048+o); fb[u]=tr2(k0+L_VI+o,k1+L_VI+o); }
      SB();
#pragma unroll
      for(int u=0;u<8;u++) c=mfma(fa[u],fb[u],c);
      st16(pk8(c,0),rkv,l16,i*4096+tt*2048); st16<1024>(pk8(c,8),rkv,l16,i*4096+tt*2048);
      lsig(cnt+8);
      STAMP(4);
    } else {
      if(tt==2) asm volatile("buffer_load_dword %0, %1, %2, 0 offen sc1":"=v"(flv):"v"(vfl),"s"(rf):"memory");
      sfor<W1E,20>([&](auto K){ w1d(K.v,2*(tt&1)); w1d(K.v,2*(tt&1)+1); });
      if(tt==2){
        asm volatile("s_waitcnt vmcnt(%1)":"+v"(flv):"n"(2*(20-W1E)):"memory");
        NOUNR for(int it=0;it<SPIN;it++){
          if(__builtin_amdgcn_ballot_w64(lane<32&&flv!=tag)==0) break;
          asm volatile("buffer_load_dword %0, %1, %2, 0 offen sc1\n\ts_waitcnt vmcnt(0)":"+v"(flv):"v"(vfl),"s"(rf):"memory"); }
        lsig(cnt+12); } }
    lwait(cnt+8,2);
    STAMP(5);
    sfor<20,28>([&](auto K){ w1d(K.v,tt); });
    if(tt<2){ WAITV(8); lsig(cnt+12); }
    STAMP(6);
    lwait(cnt+12,3);
    STAMP(8);
    u32x4 g[16]; const u32 rk=4*i3;
    sfor<0,16>([&](auto N){ g[N.v]=ldc<0>(l16,rkv,((N.v+rk)&15)*4096+tt*1024); });
    STAMP(42);
    rdw1a();
    WAITV(16); lsig(cnt+28);
    misc();
    STAMP(7);
    f32x4 s[4]; const u32x2 id4=mkid();
    const int rr0=chain<16>(g,[&]{ s[0]=s[1]=s[2]=s[3]=f32x4{}; },[&](auto N){ waitr<17-N.v>(g[N.v]); },[&](auto N){ acc8(id4,g[N.v],s[2*(N.v&1)],s[2*(N.v&1)+1]); PINW(s[2*(N.v&1)]); PINW(s[2*(N.v&1)+1]); },[](auto){},
      [&](auto N){ rld<0>(g[N.v],l16,rkv,((N.v+rk)&15)*4096+tt*1024); });
    CNT(37,rr0);
    LDS(u32x4,L+L_KVX+tt*1024+l16)=pk44(s[0]+s[2],s[1]+s[3]);
    PRIO(0);
  } else {
#pragma unroll
    for(int r=0;r<16;r++) c1[r]*=rstd;
    u32x4 v0=pk8(c1,0),v1=pk8(c1,8); if(hf){ v1[2]=0x3F80u; v1[3]=0u; }
    LDS(u32x4,L+L_VI+wro+c0)=v0; LDS(u32x4,L+L_VI+wro+(c0^16))=v1;
    lsig(cnt);
    STAMP(3);
    sfor<nqc(5),7>([&](auto U){ wq(U.v); });
    sfor<0,W1E>([&](auto K){ w1d(K.v,tt); });
    STAMP(40);
    waitv<W1E>(); lsig(cnt+4);
    u32x4 fq[28]; float cq=0;
#pragma unroll
    for(int u=0;u<7;u++) fq[4*u]=tr2(aQ[0]+u*4096,aQ[0]+u*4096+256);
    lwait(cnt+4,4);
    STAMP(4);
    auto ord=[](int s){ return s<7?4*s:((s-7)/3)*4+1+(s-7)%3; };
    f32x16 cQ={};
    sfor<0,28>([&](auto S){ constexpr int s=S.v;
      cQ=mfma(fq[ord(s)],xf[ord(s)],cQ); PINW(cQ); SB();
      if constexpr(s<21){ constexpr int n=ord(s+7); fq[n]=tr2(aQ[n&3]+(n>>2)*4096,aQ[n&3]+(n>>2)*4096+256); }
      if constexpr(s<7){ cq=sum8(fq[4*s],cq); PINW(cq); }
      if constexpr(s==7) LDS(float,L+L_MISC+l16+tt*4)=cq;
      if constexpr(s==22) lsig(cnt+16);
      SB(); });
    lwait(cnt+16,4);
    const f32x4 z=LDS(f32x4,L+L_MISC+l16);
    cQ=__builtin_amdgcn_mfma_f32_32x32x2f32((z[0]+z[1])+(z[2]+z[3]),nmean,cQ,0,0,0);
    f32x16 p=mfma(wp[0],pk8(cQ,0),f32x16{}); p=mfma(wp[1],pk8(cQ,8),p); PINW(p);
    STAMP(5);
    dsoftmax(p,rstd*LOG2E,phq);
    PINW(phq[0]); PINW(phq[1]); PINW(phq[2]); PINW(phq[3]);
    WAITV(0); lsig(cnt+20);
    STAMP(6);
    rdw1a();
    lwait(cnt+28,4); rdw1(IC<1>{});
    lwait(cnt+24,8);
    STAMP(42);
    misc();
    { const u32 rw_=ROT?((7*i3)>>1)+XR*(i>>2):0; sfor<0,14>([&](auto U){ const u32 u=tt*14+wrap(U.v+rw_,14); dma(L_WO+u*1024,vw2,r0,u*7168+cs*128); }); }
    STAMP(7);
    PRIO(2);
  }
  BAR();
  STAMP(9);
  if(ty){ f32x16 c={};
#pragma unroll
    for(int n=0;n<4;n++) c=mfma(LDS(u32x4,L+L_KVX+n*1024+l16),phq[n],c);
    const float rd=__builtin_amdgcn_rcpf(swp_hi(c[12])+1e-6f);
#pragma unroll
    for(int r=0;r<16;r++) c[r]*=rd;
    ost(pk8(c,0),pk8(c,8));
    PRIO(0);
    WAITV(3); lsig(cnt+32);
  }
  else rdw1(IC<1>{});
  STAMP(10);
  if(act){
  { const u32 so=(2*th+tt2)*28672+kq*7168;
    lwait(cnt+32,4);
    if(!ty){ pzh(); W2D(w*4096,0,0); W2D(w*4096+2048,1,0); W2D(w*4096+16384,0,4*224*896); W2D(w*4096+18432,1,4*224*896); }
    lptr p0=a0+L_WO+kq*14336,p1=a1+L_WO+kq*14336; PINW(p0); PINW(p1);
    u32x4 fa[7][2]; f32x16 c[2];
#pragma unroll
    for(int n=0;n<7;n++){ fa[n][0]=tr2(p0+n*2048,p0+n*2048+512); fa[n][1]=tr2(p1+n*2048,p1+n*2048+512); PINW(fa[n][0]); PINW(fa[n][1]); }
    SB();
    STAMP(11);
    if(ty) WAITV(0); else WAITV(16);
    asm volatile("":"+v"(xr),"+v"(b1v)::"memory");
    BAR();
    STAMP(12);
    u32x4 of[7];
    sfor<0,7>([&](auto N){ of[N.v]=ldc<(N.v&3)*1024>(l16,ro,so+(N.v>>2)*4096); });
    auto r2d=[&](int n){ dma(L_R2+(n>>1)*16384+w*2048+(n&1)*1024,vw2,r2,sW2+wrap(2+(n>>1)+r14,14)*14336+(n&1)*7168); };
    STAMP(41);
    const int rr1=chain<7>(of,[&]{ c[0]=c[1]=f32x16{}; },[&](auto N){ waitr<6>(of[N.v]); },[&](auto N){ c[0]=mfma(fa[N.v][0],of[N.v],c[0]); PINW(c[0]); c[1]=mfma(fa[N.v][1],of[N.v],c[1]); PINW(c[1]); },[&](auto N){ r2d(N.v); if(N.v==6) r2d(7); },
      [&](auto N){ rld<(N.v&3)*1024>(of[N.v],l16,ro,so+(N.v>>2)*4096); });
    SB();
    CNT(38,rr1);
    lptr e=L+L_PX+w*4096+l16;
    LDS(u32x4,e)=pk8(c[0],0); LDS(u32x4,e+1024)=pk8(c[0],8); LDS(u32x4,e+2048)=pk8(c[1],0); LDS(u32x4,e+3072)=pk8(c[1],8);
    STAMP(13);
    BAR();
    e=L+L_PX+tt2*4096+vf;
    const u32x2 id4=mkid(); f32x4 s[4]={}; acc8(id4,xr,s[0],s[1]);
#pragma unroll
    for(int q=0;q<4;q++) acc8(id4,LDS(u32x4,e+q*8192),s[2*(q&1)],s[2*(q&1)+1]);
    s[0]+=s[2]; s[1]+=s[3];
    st16(pk44(s[0],s[1]),rh,vf,(2*th+tt2)*28672+cs*4096);
    float s1=((s[0][0]+s[0][1])+(s[0][2]+s[0][3]))+((s[1][0]+s[1][1])+(s[1][2]+s[1][3])), s2=s[0][0]*s[0][0], s3=s[1][0]*s[1][0];
#pragma unroll
    for(int q=1;q<4;q++){ s2=__builtin_fmaf(s[0][q],s[0][q],s2); s3=__builtin_fmaf(s[1][q],s[1][q],s3); }
    s1=red8(s1); s2=red8(s2+s3);
    asm volatile("buffer_store_dwordx2 %0, %1, %2, %3 offen sc1"::"v"(f32x2{s1,s2}),"v"(lane<8?lane*64:OOB),"s"(rst),"s"(((2*th+tt2)*32+kq*8)*64+cs*8):"memory");
    SB(); }
  STAMP(14);
  if(b==0&&tid==0) __hip_atomic_store((u32*)ws,tag,__ATOMIC_RELAXED,__HIP_MEMORY_SCOPE_AGENT);
  const u32 bX=ty?tt*6144:L_S+tt*12288, bY=ty?28672+tt*6144:bX+6144;
  W2D(L_R3+w*2048,6,0);
  { lptr p0=a0+w*4096,p1=a1+w*4096;
#pragma unroll
    for(int n=0;n<2;n++){ w2f[n][0]=tr2(p0+n*2048,p0+n*2048+512); w2f[n][1]=tr2(p1+n*2048,p1+n*2048+512); } }
  WAITV(4);
  { lptr p0=a0+L_R2+w*2048,p1=a1+L_R2+w*2048; PINW(p0); PINW(p1);
#pragma unroll
    for(int n=0;n<4;n++){ w2f[2+n][0]=tr2(p0+n*16384,p0+n*16384+512); w2f[2+n][1]=tr2(p1+n*16384,p1+n*16384+512); }
    asm volatile("s_waitcnt lgkmcnt(0)":"+v"(w2f[4][0]),"+v"(w2f[4][1])::"memory"); }
  W2D(L_R4+w*2048,7,0);
  WAITV(4);
  BAR();
  STAMP(15);
  const u32 sH=ty*57344+tt*1024, lH0=ty*28672+tt*1024, lH1=(ty?57344:86016)+tt*1024;
  if(ty) __builtin_amdgcn_s_sleep(4);
  u32x4 hb[28],sa,sb; float ks[2][5]; const u32 vst=t*64+hf*32;
  auto dm1=[&](u32 l,int k,int u){ dmac(l+u*4096,l16,rh,sH+k*28672+wrap(u+r7,7)*4096); };
  auto dmas=[&](u32 l,int k){ sfor<0,7>([&](auto U){ dm1(l,k,U.v); }); };
  dmas(lH0,0); sa=ldc<0>(vst,rst,ty*4096); sb=ldc<16>(vst,rst,ty*4096);
  float cb=0;
  if(ty){
#pragma unroll
    for(int n=14;n<28;n++) cb=sum8(w1f[n],cb);
    asm volatile("; b":"+v"(cb));
  } else {
#pragma unroll
    for(int n=0;n<14;n++) cb=sum8(w1f[n],cb);
    asm volatile("; a":"+v"(cb)); }
  LDS(float,L+(ty?86016:57344)+tt*1024+lane*4)=cb; SB();
  STAMP(36);
  sfor<0,3>([&](auto U){ W2D(L_S+tt*12288+ty*6144+U.v*2048,8+3*ty+U.v,-ty*4*224*896); });
  WAITV(15);
  { lptr p0=a0+L_R4+w*2048,p1=a1+L_R4+w*2048; PINW(p0); PINW(p1); w2f[7][0]=tr2(p0,p0+512); w2f[7][1]=tr2(p1,p1+512);
    w2f[6][0]=tr2(p0+(L_R3-L_R4),p0+(L_R3-L_R4)+512); w2f[6][1]=tr2(p1+(L_R3-L_R4),p1+(L_R3-L_R4)+512); }
  auto vrd=[&](u32 l){ lptr p=L+l+l16; PINW(p);
#pragma unroll
    for(int n=0;n<7;n++) hb[4*n]=LDS(u32x4,p+n*4096);
    if(hf){ sb[2]=0; sb[3]=0; } };
  auto vbad=[&]{ u32 m=mx4(mx4(0,sa),sb);
#pragma unroll
    for(int n=0;n<7;n++) m=mx4(m,hb[4*n]);
    return bad(m); };
  auto vfix=[&](u32 l,int k){
    NOUNR for(int it=0;it<SPIN;it++){
      sfor<0,7>([&](auto U){ if(chk(hb[4*U.v])) dm1(l,k,U.v); });
      if(bad(mx4(mx4(0,sa),sb))){ rld<0>(sa,vst,rst,ty*4096+k*2048); rld<16>(sb,vst,rst,ty*4096+k*2048); }
      asm volatile("s_waitcnt vmcnt(0)":"+v"(sa),"+v"(sb)::"memory");
      vrd(l); if(!vbad()) break; } };
  auto vst_=[&](float* o){
    const float s1=swp_add((u2f(sa[0])+u2f(sa[2]))+(u2f(sb[0])+u2f(sb[2])))*(-1.f/448), s2=swp_add((u2f(sa[1])+u2f(sa[3]))+(u2f(sb[1])+u2f(sb[3])));
    const float g_=__builtin_fmaf(__builtin_fmaxf(__builtin_fmaf(s2,1.f/448,-s1*s1),0.f),1.00001f,1e-10f);
    const float rs_=__builtin_amdgcn_rsqf(g_); o[3]=0.5f*__builtin_amdgcn_logf(g_);
    o[0]=g_*rs_; o[1]=-2.302208198f*rs_; o[2]=-0.1029432397f*rs_*rs_*rs_; o[4]=hf?o[0]:s1; };
  auto vpin=[&](float* o){ PINW(o[0]); PINW(o[1]); PINW(o[2]); PINW(o[3]); PINW(o[4]); };
  if(ty) BAR();
  asm volatile("s_waitcnt vmcnt(6)":"+v"(sa),"+v"(sb)::"memory"); vrd(lH0); vst_(ks[0]); if(vbad()){ vfix(lH0,0); vst_(ks[0]); } vpin(ks[0]);
  if(!ty) BAR();
  STAMP(16);
  cb=swp_add(cb+LDS(float,L+lH1+lane*4)); cb=hf?u2f(b1v<<16):cb; PINW(cb);
  dmas(lH1,1); sa=ldc<0>(vst,rst,ty*4096+2048); sb=ldc<16>(vst,rst,ty*4096+2048);
  if(ty) BAR();
  sfor<0,2>([&](auto KK){ constexpr int k=KK.v;
    lptr q[4];
#pragma unroll
    for(int u=1;u<4;u++){ q[u]=L+(k?lH1:lH0)+(((u+tt)&3)-tt)*1024+l16; PINW(q[u]); }
    f32x16 c={};
    auto ord=[](int s){ return s<7?4*s:((s-7)/3)*4+1+(s-7)%3; };
    PRIO(1);
    sfor<0,28>([&](auto S){ constexpr int s=S.v;
      c=mfma(w1f[ord(s)],hb[ord(s)],c); PINW(c); SB();
      if constexpr(s+LA>=7&&s+LA<28) hb[ord(s+LA)]=LDS(u32x4,q[ord(s+LA)&3]+(ord(s+LA)>>2)*4096);
      if constexpr(k==0&&s%8==2&&s<24){ if(ty) W2D(bX+(s/8)*2048,8+s/8,0); }
      SB(); });
    c=__builtin_amdgcn_mfma_f32_32x32x2f32(cb,ks[k][4],c,0,0,0); PINW(c);
    PRIO(0);
    SB(); if(k==0||!ty) BAR(); PINW(c); SB();
    STAMP(17+2*k);
    if constexpr(k==0){ if(ty){ W2D(bY,11,0); W2D(bY+2048,12,0); W2D(bY+4096,13,0); if(tid==256) LDS(u32,L+L_SIG)=0; WAITV(12); } else WAITV(0);
      asm volatile("":"+v"(sa),"+v"(sb)::"memory"); vrd(lH1); vst_(ks[1]); }
    u32 g[8]; const float sg_=ks[k][0],K1=ks[k][1],K2=ks[k][2],Lg=ks[k][3];
#pragma unroll
    for(int p=0;p<8;p++){ float a=c[2*p],d=c[2*p+1];
      float u=__builtin_fmaf(a,__builtin_fmaf(a*a,K2,K1),Lg), v=__builtin_fmaf(d,__builtin_fmaf(d*d,K2,K1),Lg);
      u=__builtin_amdgcn_rcpf(__builtin_amdgcn_exp2f(u)+sg_); v=__builtin_amdgcn_rcpf(__builtin_amdgcn_exp2f(v)+sg_);
      g[p]=pk(a*u,d*v);
      if(p==3) st16(u32x4{g[0],g[1],g[2],g[3]},rhh,l16,(2*ty+k)*114688+(8*j+2*tt)*1024); }
    st16<1024>(u32x4{g[4],g[5],g[6],g[7]},rhh,l16,(2*ty+k)*114688+(8*j+2*tt)*1024);
    if constexpr(k==0){ if(vbad()){ vfix(lH1,1); vst_(ks[1]); } vpin(ks[1]);
      BAR(); STAMP(18); }
    SB();
  });
  { const u32 s5=th*229376+w*14336, dY=th?-114688u:344064u; const int inl=ty|th, tl=tt2?3-2*th:2*th;
    auto su=[&](int n){ return s5+wrap(n+r14,14)*1024; };
    u32x4 hh[14],hp,q2;
    auto rdw2=[&]{ lptr p0=a0+bX,p1=a1+bX; PINW(p0); PINW(p1);
#pragma unroll
      for(int n=0;n<3;n++){ w2f[8+n][0]=tr2(p0+n*2048,p0+n*2048+512); w2f[8+n][1]=tr2(p1+n*2048,p1+n*2048+512); }
      p0=a0+bY; p1=a1+bY; PINW(p0); PINW(p1);
#pragma unroll
      for(int n=0;n<3;n++){ w2f[11+n][0]=tr2(p0+n*2048,p0+n*2048+512); w2f[11+n][1]=tr2(p1+n*2048,p1+n*2048+512); } };
    STAMP(20);
    sfor<0,14>([&](auto N){ hh[N.v]=ldc<0>(l16,rhh,su(N.v)); });
    WAITV(18); rdw2();
#pragma unroll
    for(int n=8;n<14;n++){ PINW(w2f[n][0]); PINW(w2f[n][1]); }
    if(ty!=th) WAITV(14);
    if(ty) lsig(L+L_SIG);
    STAMP(21);
    f32x16 c[2]; auto zc=[&]{ c[0]=c[1]=f32x16{}; };
    auto mm=[&](auto N){ c[0]=mfma(w2f[N.v][0],hh[N.v],c[0]); PINW(c[0]); c[1]=mfma(w2f[N.v][1],hh[N.v],c[1]); PINW(c[1]); };
    const int rr2=chain<14>(hh,zc,[&](auto N){ waitb<13,13-N.v>(hh[N.v],inl); },mm,[&](auto N){ if constexpr(XS==1&&N.v%2==0&&N.v<12) STAMP(26+N.v/2); rldb(hh[N.v],l16,rhh,su(N.v)+dY,inl); },
      [&](auto N){ rld<0>(hh[N.v],l16,rhh,su(N.v)); });
    STAMP(22); CNT(39,rr2);
    lptr e=L+bX+l16; PINW(e);
    LDS(u32x4,e)=pk8(c[0],0); LDS(u32x4,e+1024)=pk8(c[0],8); LDS(u32x4,e+2048)=pk8(c[1],0); LDS(u32x4,e+3072)=pk8(c[1],8);
    if(!inl){ lwait(L+L_SIG,4); sfor<0,14>([&](auto N){ rld<0>(hh[N.v],l16,rhh,su(N.v)+dY); }); }
    hp=ldc<0>(vf,rh,tl*28672+cs*4096); q2=ldp(v8*16,mk(b2,896),cs*128);
    STAMP(23);
    const int rr3=chain<14>(hh,zc,[&](auto N){ waitr<15-N.v>(hh[N.v]); },mm,[](auto){},
      [&](auto N){ rld<0>(hh[N.v],l16,rhh,su(N.v)+dY); });
    STAMP(24); CNT(47,rr3);
    e=L+bY+l16; PINW(e);
    LDS(u32x4,e)=pk8(c[0],0); LDS(u32x4,e+1024)=pk8(c[0],8); LDS(u32x4,e+2048)=pk8(c[1],0); LDS(u32x4,e+3072)=pk8(c[1],8);
    asm volatile("s_waitcnt vmcnt(0)":"+v"(q2),"+v"(hp)::"memory");
    BAR();
    lptr e0=L+tt2*28672+vf,e1=L+L_S+tt2*6144+vf; PINW(e0); PINW(e1);
    const u32x2 id4=mkid(); f32x4 s[4]={}; acc8(id4,hp,s[0],s[1]); acc8(id4,q2,s[2],s[3]);
#pragma unroll
    for(int q=0;q<4;q++){ acc8(id4,LDS(u32x4,e0+q*6144),s[0],s[1]); acc8(id4,LDS(u32x4,e1+q*12288),s[2],s[3]); }
    __builtin_amdgcn_raw_buffer_store_b128(pk44(s[0]+s[2],s[1]+s[3]),mk(out+(size_t)i*128*448,128*896),vo,(tl*32+kq*8)*896+cs*128,16); }
  }
  STAMP(25);
  if(b==PBLK&&w==PW){ int mn=0x7fffffff,mxx=-0x7fffffff; for(int q=0;q<4;q++){ int v=(int)(__hip_atomic_load((u32*)(ws+(16u<<20)+(q*64+lane)*4),__ATOMIC_RELAXED,__HIP_MEMORY_SCOPE_AGENT)-pa_); mn=min(mn,v); mxx=max(mxx,v);}
    for(int o=32;o>0;o>>=1){ mn=min(mn,__shfl_xor(mn,o)); mxx=max(mxx,__shfl_xor(mxx,o)); }
    mn=__builtin_amdgcn_readfirstlane(mn); mxx=__builtin_amdgcn_readfirstlane(mxx);
    u32 d=(0)*PMUL; if(d>40000) d=40000; uint64_t t0_,t1_; asm volatile("s_memrealtime %0\n\ts_waitcnt lgkmcnt(0)":"=s"(t0_)::"memory");
    do{ asm volatile("s_memrealtime %0\n\ts_waitcnt lgkmcnt(0)":"=s"(t1_)::"memory"); }while((u32)t1_-(u32)t0_<d); }
}
}
#ifdef __HIP_DEVICE_COMPILE__
#pragma clang attribute pop
#endif
extern "C" void kernel_launch(void* const* d_in,const int* in_sizes,int n_in,void* d_out,int out_size,void* d_ws,size_t ws_size,hipStream_t stream){
  typedef const unsigned short* P;
  (void)hipFuncSetAttribute((const void*)fk::fused,hipFuncAttributeMaxDynamicSharedMemorySize,163840);
  fk::fused<<<256,512,163840,stream>>>((P)d_in[0],(P)d_in[1],(P)d_in[2],(P)d_in[3],(P)d_in[5],(P)d_in[6],(P)d_in[7],(P)d_in[8],(unsigned short*)d_out,(char*)d_ws);
}
```

```cpp
#include <hip/hip_runtime.h>
#include <stdint.h>
#ifdef __HIP_DEVICE_COMPILE__
#pragma clang attribute push(__attribute__((target("no-packed-fp32-ops"))),apply_to=function)
#endif
namespace fk {
typedef unsigned short u16; typedef unsigned u32;
typedef __attribute__((ext_vector_type(4))) u32 u32x4;
typedef __attribute__((ext_vector_type(2))) u32 u32x2;
typedef __attribute__((ext_vector_type(8))) __bf16 bf16x8;
typedef __attribute__((ext_vector_type(2))) __bf16 bf16x2;
typedef __attribute__((ext_vector_type(2))) float f32x2;
typedef __attribute__((ext_vector_type(4))) float f32x4;
typedef __attribute__((ext_vector_type(16))) float f32x16;
typedef short v4i16_t __attribute__((ext_vector_type(4)));
typedef __attribute__((address_space(3))) char* lptr;
typedef __amdgpu_buffer_rsrc_t rsrc_t;
#define DI __device__ __forceinline__
#define LDS(T,a) (*(__attribute__((address_space(3))) T*)(a))
#define STAMP(n)
#define CNT(n,v)
#define CST(n)
constexpr u32 WS_FLAG=4096, WS_ST=512u<<10, WS_KV=1u<<20, WS_O=2u<<20, WS_H=4u<<20, WS_HH=8u<<20, OOB=0x80000000u;
constexpr int L_WK=98304, L_MISC=155648, L_CNT=L_MISC+2048, L_ONE=L_CNT+64, L_KVX=159744;
constexpr int L_PHI=32768, L_VI=49152, L_WO=98304, L_R2=32768, L_R3=98304, L_R4=65536, L_S=114688, L_SIG=53248, L_PX=131072;
constexpr int w1l(int ks){ return ks<12?98304+ks*4096:ks<20?65536+(ks-12)*4096:32768+(ks-20)*4096; }
constexpr int xs(int c){ return (c%6==0?4:c%6==5?5:c%6-1)*16384; }
constexpr float LOG2E=1.4426950408889634f;
#define DEP 3
#define PZS 3
#define NQE 4
#define W1E 12
#define LA 6
#define R14J 2
#define XR 0
#define XS 0
#define ROT 1
constexpr int yng(int c){ return ((c-1+DEP<6?c-1+DEP:6)-c)*3; }
constexpr int nq(int s){ return s+DEP<=6||s<4?0:s==4?(NQE<4?NQE:4):s==5?(NQE>4?NQE-4:0):0; }
constexpr int nqc(int s){ return s<4?0:nqc(s-1)+nq(s); }
#define NOUNR _Pragma("clang loop unroll(disable)")
DI float u2f(u32 v){ return __builtin_bit_cast(float,v); }
DI u32 f2u(float v){ return __builtin_bit_cast(u32,v); }
template<int V> struct IC{ static constexpr int v=V; };
template<int I,int N,class F> DI void sfor(F&& f){ if constexpr(I<N){ f(IC<I>{}); sfor<I+1,N>(f); } }
DI rsrc_t mk(const void* p,u32 bytes){ return __builtin_amdgcn_make_buffer_rsrc((void*)p,0,bytes,0x00020000); }
DI void dma(u32 l,u32 voff,rsrc_t r,u32 soff){ asm volatile("s_mov_b32 m0, %0\n\ts_nop 0\n\tbuffer_load_dwordx4 %1, %2, %3 offen lds"::"s"(l),"v"(voff),"s"(r),"s"(soff):"memory"); }
#define WAITV(n) asm volatile("s_waitcnt vmcnt(" #n ")":::"memory")
#define BAR() asm volatile("s_waitcnt lgkmcnt(0)\n\ts_barrier":::"memory")
#define PIN(x) asm volatile(""::"v"(x))
#define PINW(x) asm volatile("":"+v"(x))
#define SB() __builtin_amdgcn_sched_barrier(0)
DI f32x16 mfma(u32x4 a,u32x4 b,f32x16 c){ return __builtin_amdgcn_mfma_f32_32x32x16_bf16(__builtin_bit_cast(bf16x8,a),__builtin_bit_cast(bf16x8,b),c,0,0,0); }
DI f32x4 m44(u32x2 a,u32x2 b,f32x4 c){ return __builtin_amdgcn_mfma_f32_4x4x4bf16_1k(__builtin_bit_cast(v4i16_t,a),__builtin_bit_cast(v4i16_t,b),c,0,0,0); }
DI u32x2 tr(lptr p){ return __builtin_bit_cast(u32x2,__builtin_amdgcn_ds_read_tr16_b64_v4i16((__attribute__((address_space(3))) v4i16_t*)p)); }
DI u32x4 tr2(lptr p0,lptr p1){ u32x2 a=tr(p0),b=tr(p1); return u32x4{a[0],a[1],b[0],b[1]}; }
DI u32 pk(float lo,float hi){ return __builtin_bit_cast(u32,__builtin_convertvector(f32x2{lo,hi},bf16x2)); }
DI float dot2(u32 a,u32 b,float c){ return __builtin_amdgcn_fdot2_f32_bf16(__builtin_bit_cast(bf16x2,a),__builtin_bit_cast(bf16x2,b),c,false); }
DI float swp_lo(float v){ auto r=__builtin_amdgcn_permlane32_swap(f2u(v),f2u(v),false,false); return u2f(r[0]); }
DI float swp_hi(float v){ auto r=__builtin_amdgcn_permlane32_swap(f2u(v),f2u(v),false,false); return u2f(r[1]); }
DI float swp_add(float v){ auto r=__builtin_amdgcn_permlane32_swap(f2u(v),f2u(v),false,false); return u2f(r[0])+u2f(r[1]); }
DI float swp_max(float v){ auto r=__builtin_amdgcn_permlane32_swap(f2u(v),f2u(v),false,false); return __builtin_fmaxf(u2f(r[0]),u2f(r[1])); }
DI float swp_min(float v){ auto r=__builtin_amdgcn_permlane32_swap(f2u(v),f2u(v),false,false); return __builtin_fminf(u2f(r[0]),u2f(r[1])); }
DI float red8(float v){ v+=u2f(__builtin_amdgcn_update_dpp(0u,f2u(v),0x128,0xf,0xf,false)); auto r=__builtin_amdgcn_permlane16_swap(f2u(v),f2u(v),false,false); return swp_add(u2f(r[0])+u2f(r[1])); }
DI u32x4 pk8(const f32x16& c,int b){ return u32x4{pk(c[b],c[b+1]),pk(c[b+2],c[b+3]),pk(c[b+4],c[b+5]),pk(c[b+6],c[b+7])}; }
DI void dsoftmax(const f32x16& p,float c,u32x4* ph){
  float mx=p[0],mn=p[0];
#pragma unroll
  for(int r=1;r<14;r++){ mx=__builtin_fmaxf(mx,p[r]); mn=__builtin_fminf(mn,p[r]); }
  mx=swp_max(mx); mn=swp_min(mn);
  const float a=-mx*c,b=mn*c; f32x16 e,g; float s=0,u=0;
#pragma unroll
  for(int r=0;r<14;r++){ e[r]=__builtin_amdgcn_exp2f(__builtin_fmaf(p[r],c,a)); g[r]=__builtin_amdgcn_exp2f(__builtin_fmaf(p[r],-c,b)); s+=e[r]; u+=g[r]; }
  s=__builtin_amdgcn_rcpf(swp_add(s)); u=__builtin_amdgcn_rcpf(swp_add(u));
#pragma unroll
  for(int r=0;r<14;r++){ e[r]*=s; g[r]*=u; }
  ph[0]=pk8(e,0); ph[1]=u32x4{pk(e[8],e[9]),pk(e[10],e[11]),pk(e[12],e[13]),0u};
  ph[2]=pk8(g,0); ph[3]=u32x4{pk(g[8],g[9]),pk(g[10],g[11]),pk(g[12],g[13]),0u};
}
DI void dmac(u32 l,u32 voff,rsrc_t r,u32 soff){ asm volatile("s_mov_b32 m0, %0\n\ts_nop 0\n\tbuffer_load_dwordx4 %1, %2, %3 offen sc1 lds"::"s"(l),"v"(voff),"s"(r),"s"(soff):"memory"); }
template<int O> DI void rld(u32x4& v,u32 voff,rsrc_t r,u32 soff){ asm volatile("buffer_load_dwordx4 %0, %1, %2, %3 offen offset:%4 sc1":"+v"(v):"v"(voff),"s"(r),"s"(soff),"n"(O):"memory"); }
template<int O> DI u32x4 ldc(u32 voff,rsrc_t r,u32 soff){ u32x4 v; asm volatile("buffer_load_dwordx4 %0, %1, %2, %3 offen offset:%4 sc1":"=v"(v):"v"(voff),"s"(r),"s"(soff),"n"(O):"memory"); return v; }
DI u32x4 ldp(u32 voff,rsrc_t r,u32 soff){ u32x4 v; asm volatile("buffer_load_dwordx4 %0, %1, %2, %3 offen":"=v"(v):"v"(voff),"s"(r),"s"(soff):"memory"); return v; }
template<int N> DI void waitv(){ asm volatile("s_waitcnt vmcnt(%0)"::"n"(N):"memory"); }
template<int O=0> DI void st16(u32x4 v,rsrc_t r,u32 voff,u32 soff){ asm volatile("buffer_store_dwordx4 %0, %1, %2, %3 offen offset:%4 sc1\n\ts_nop 1"::"v"(v),"v"(voff),"s"(r),"s"(soff),"n"(O):"memory"); }
DI u32 mx3(u32 a,u32 b,u32 c){ u32 r; asm("v_max3_u32 %0, %1, %2, %3":"=v"(r):"v"(a),"v"(b),"v"(c)); return r; }
DI u32 mx4(u32 m,u32x4 g){ return mx3(mx3(m,g[0],g[1]),g[2],g[3]); }
DI bool bad(u32 m){ return __builtin_amdgcn_ballot_w64(m==0xFFFFFFFFu)!=0; }
constexpr int SPIN=30000;
DI void lsig(lptr p){ asm volatile("s_mov_b64 exec, 1\n\tds_add_u32 %0, %1\n\ts_mov_b64 exec, -1"::"v"(p),"v"(1u):"memory"); }
DI void lwait(lptr p,u32 n){ NOUNR for(int it=0;it<SPIN*8;it++){ u32 v; asm volatile("ds_read_b32 %0, %1\n\ts_waitcnt lgkmcnt(0)":"=v"(v):"v"(p):"memory"); if(__builtin_amdgcn_readfirstlane(v)>=n) break; } }
DI float sum8(u32x4 f,float c){ c=dot2(f[0],0x3F803F80u,c); c=dot2(f[1],0x3F803F80u,c); c=dot2(f[2],0x3F803F80u,c); return dot2(f[3],0x3F803F80u,c); }
DI float sq8(u32x4 f,float c){ c=dot2(f[0],f[0],c); c=dot2(f[1],f[1],c); c=dot2(f[2],f[2],c); return dot2(f[3],f[3],c); }
DI void acc8(u32x2 id4,u32x4 g,f32x4& a,f32x4& b){ a=m44(id4,u32x2{g[0],g[1]},a); b=m44(id4,u32x2{g[2],g[3]},b); }
DI u32x4 pk44(f32x4 a,f32x4 b){ return u32x4{pk(a[0],a[1]),pk(a[2],a[3]),pk(b[0],b[1]),pk(b[2],b[3])}; }
DI bool chk(u32x4 g){ return bad(mx3(mx3(g[0],g[1],g[2]),g[3],g[3])); }
template<int N> DI void waitr(u32x4& v){ asm volatile("s_waitcnt vmcnt(%1)":"+v"(v):"n"(N):"memory"); }
template<int A,int B> DI void waitb(u32x4& v,int c){ asm volatile("s_cmp_lg_u32 %1, 0\n\ts_waitcnt vmcnt(%2)\n\ts_cbranch_scc1 1f\n\ts_waitcnt vmcnt(%3)\n1:":"+v"(v):"s"(c),"n"(A),"n"(B):"memory","scc"); }
DI void rldb(u32x4& v,u32 voff,rsrc_t r,u32 soff,int c){ asm volatile("s_cmp_eq_u32 %4, 0\n\ts_cbranch_scc1 1f\n\tbuffer_load_dwordx4 %0, %1, %2, %3 offen sc1\n1:":"+v"(v):"v"(voff),"s"(r),"s"(soff),"s"(c):"memory","scc"); }
DI u32x4 mkpz(){ u32x4 p={~0u,~0u,~0u,~0u}; PINW(p); return p; }
DI u32 wrap(u32 v,u32 n){ return v>=n?v-n:v; }
template<int N,class I,class W,class U,class X,class R> DI int chain(u32x4* g,I&& init,W&& wt,U&& use,X&& ex,R&& re){
  int it=0;
  NOUNR for(;it<SPIN;it++){
    init(); u32 m=0;
    sfor<0,N>([&](auto K){ wt(K); use(K); m=mx4(m,g[K.v]); PINW(m); ex(K); SB(); });
    if(!bad(m)) break;
    WAITV(0); sfor<0,N>(re); WAITV(0); }
  return it; }
#define PRIO(n) __builtin_amdgcn_s_setprio(n)

__global__ __launch_bounds__(512,2) void fused(const u16* __restrict__ x,const u16* __restrict__ wqkv,const u16* __restrict__ wphi,const u16* __restrict__ wo,
    const u16* __restrict__ w1,const u16* __restrict__ b1,const u16* __restrict__ w2,const u16* __restrict__ b2,u16* __restrict__ out,char* __restrict__ ws
    ){
  extern __shared__ __attribute__((aligned(16))) char smem[];
  const lptr L=(lptr)smem;
  const int tid=__builtin_amdgcn_workitem_id_x(),lane=tid&63,w=__builtin_amdgcn_readfirstlane(tid>>6),tt=w&3,ty=w>>2,t=lane&31,hf=lane>>5;
  const int b=__builtin_amdgcn_workgroup_id_x(),i3=(b>>3)&3,jq=b>>5,th=b&1,i=((b&7)>>1)*4+i3;
  const int j1=th*8+jq,odd=j1&1;
  const bool act=jq<7; const int j=th*7+jq,cs=jq;
  STAMP(0);
  asm volatile(""::"s"(x),"s"(wqkv),"s"(wphi),"s"(wo),"s"(w1),"s"(b1),"s"(w2),"s"(b2),"s"(out),"s"(ws));
  const u32 epoch=*(const __attribute__((address_space(4))) u32*)ws;
  const rsrc_t rw=mk(wqkv,448*2688), rx=mk(x+(size_t)i*128*448,128*896);
  const u32 vw=(lane>>2)*2688+(lane&3)*16, hoff=56*j1-8*odd+tt*16*2688, sW=hoff+(ty+1)*896, lW=L_WK+ty*28672+tt*1024;
  const u32 vx0=(lane>>3)*896+(((lane&7)^(lane>>4))<<4), vx1=vx0^64, sX=(tt*32+ty*16)*896, lX=tt*4096+ty*2048;
  const u32 rot=ROT?(u32)(j1+2*i3+XR*(i>>2))%7u:0u;
  auto cc=[&](int c){ return wrap(c+rot,7); };
  auto ch=[&](int c,int u){ const u32 q_=cc(c); if(u==0) dma(lW+c*4096,vw,rw,sW+q_*(64*2688)); else dma(xs(c)+lX+(u-1)*1024,u==1?vx0:vx1,rx,sX+(u-1)*7168+q_*128); };
#define SBAR() asm volatile("s_barrier":::"memory")
  if(w<2) dma(L_MISC+w*1024,tid*16,mk(wphi+j1*784,1568),0);
  const u32 l16=lane*16;
  const rsrc_t ro=mk(ws+WS_O+i*114688,114688), rkv=mk(ws+WS_KV+j1*65536,65536), rh=mk(ws+WS_H+i*114688,114688), rst=mk(ws+WS_ST+i*8192,8192);
  auto ost=[&](u32x4 o0,u32x4 o1){ const u32 ub=t*16+((7*j1)>>1)*512, so=tt*28672;
    st16(o0,ro,ub+(odd+2*hf)*512,so); st16(o1,ro,hf?OOB:ub+(odd+1)*512,so);
    __builtin_amdgcn_raw_buffer_store_b64(u32x2{o1[0],o1[1]},ro,hf?ub+(odd?8:1536):OOB,so,16); };
  STAMP(32);
  sfor<0,DEP>([&](auto C){ if constexpr(C.v>0) SBAR(); ch(C.v,0); ch(C.v,1); ch(C.v,2); STAMP(33+C.v); });
  auto poison=[&]{ const u32x4 PZ=mkpz();
    if(ty){ const int t1=j1>=7; const u32 so=(2*t1+(tt&1))*28672+(4*(j1-7*t1)+(tt>>1))*1024, v=j1<14?l16:OOB; st16(PZ,rh,v,so); st16<2048>(PZ,rh,v,so);
      st16(PZ,rst,lane<8?l16:OOB,(j1*4+tt)*128); }
    else { st16(PZ,rkv,l16,i*4096+tt*1024); ost(PZ,PZ); } };
  if(tid<3) LDS(u32x4,L+L_CNT+tid*16)=u32x4{0,0,0,0};
  if(tid<2) LDS(u32x2,L+L_ONE+tid*256)=u32x2{0x3F80u,0u};
  const int tt2=w&1, kq=w>>1;
  const int q4=(lane&15)>>2,mu=((lane>>4)&1)*4+(lane&3),sg=4*(mu&1)+(mu>>1);
  const int rowo=(8*hf+q4)*64, kslot=(mu+odd)&7;
  lptr aW[4],aQ[4],xb[4];
  { const int sw=hf^((t>>1)&7), sl=ty?(odd?(sg==7?0:((sg+1)%7)+1):sg):kslot;
#pragma unroll
    for(int u=0;u<4;u++){ const int r=(u+tt+2*ty)&3; aW[u]=mu==7?L+L_ONE:L+L_WK+ty*28672+rowo+sl*8+r*1024; aQ[u]=L+rowo+kslot*8+r*1024; xb[u]=L+(tt*32+t)*128+((sw^(r*2))<<4); PINW(aW[u]); PINW(xb[u]); } }
  u32 tag=epoch*0x9E3779B1u+0x7F4A7C15u; asm volatile("":"+s"(tag));
  const rsrc_t r1=mk(w1,act?448*3584:0), r2=mk(w2,act?1792*896:0), r0=mk(wo,act?448*896:0);
  const lptr cnt=L+L_CNT;
  const rsrc_t rf=mk(ws,1u<<20); const u32 vfl=lane<16?WS_FLAG+(j1*16+lane)*128:lane<32?WS_FLAG+((lane-16)*16+i)*128:OOB; u32 flv;
  STAMP(43);
  u32 stp=mu==7?0:4096; PINW(stp);
  const u32 tq2=(tt+2)&3, hq=56*j1-8*odd+tq2*16*2688;
  auto wq=[&](int u){ dma(tq2*1024+u*4096,vw,rw,hq+cc(u)*(64*2688)); };
  u32x4 xf[28],wp[2],fw[2][4]; f32x16 c1={}; float ss=0,s2=0,cz=0;
  auto rd=[&](auto C,int u){ constexpr int c=C.v; fw[c&1][u]=tr2(aW[u],aW[u]+256); xf[4*c+u]=LDS(u32x4,xb[u]+xs(c)); if(c<6) aW[u]+=stp; };
  auto md=[&](auto C,int u){ constexpr int c=C.v; c1=mfma(fw[c&1][u],xf[4*c+u],c1); PINW(c1); SB(); };
  auto dd=[&](auto C,int u){ constexpr int c=C.v;
    if(u==0){ cz=sum8(fw[c&1][0],cz); PINW(cz); } else if(u==1){ ss=sq8(xf[4*c],ss); PINW(ss); } else if(u==2){ s2=sq8(xf[4*c+1],s2); PINW(s2); } };
  sfor<0,7>([&](auto C){ constexpr int c=C.v;
    constexpr int pz=c>PZS&&c<PZS+DEP, fl=c>PZS+DEP;
    if(ty) waitv<yng(c)+3*pz+nqc(c-1)>(); else if(fl&&w==2) waitv<yng(c)+4*pz+1>(); else waitv<yng(c)+4*pz>();
    if constexpr(c<3) STAMP(44+c);
    if constexpr(c==0) BAR(); else SBAR();
    if constexpr(c==0) STAMP(1); else if constexpr(!XS) STAMP(25+c);
    if constexpr(c==PZS+DEP){ if(tid==128) __hip_atomic_store((u32*)(ws+WS_FLAG+(j1*16+i)*128),tag,__ATOMIC_RELAXED,__HIP_MEMORY_SCOPE_AGENT); }
    if constexpr(c==6){ if(!ty) PRIO(2); }
    if constexpr(c==PZS) poison();
    sfor<0,4>([&](auto U){ constexpr int u=U.v;
      if constexpr(c>0&&c<6) md(IC<c-1>{},u);
      rd(C,u);
      if constexpr(c+DEP<=6&&u<3) ch(c+DEP,u);
      if constexpr(u<nq(c)){ if(ty) wq(nqc(c-1)+u); }
      if constexpr(c>0&&c<6) dd(IC<c-1>{},u);
      SB(); });
    if constexpr(c==0){
      const bool mv=(t&26)!=26; const lptr p=L+L_MISC+(t<26?t:t-2)*2+hf*224; u32 e[16];
#pragma unroll
      for(int q=0;q<16;q++){ const int d=16*(q>>3)+8*((q>>2)&1)+(q&3); e[q]=LDS(u16,p+d*56); }
#pragma unroll
      for(int q=0;q<8;q++){ u32 v=e[2*q]|(e[2*q+1]<<16); v=(mv&&(q<6||hf==0))?v:0u; asm volatile("":"+v"(v)); wp[q>>2][q&3]=v; } SB(); }
    if constexpr(c==5) sfor<0,4>([&](auto U){ md(C,U.v); dd(C,U.v); SB(); });
  });
  md(IC<6>{},0); dd(IC<6>{},0); dd(IC<6>{},1); SB(); md(IC<6>{},1); dd(IC<6>{},2);
  LDS(float,L+L_KVX+w*256+lane*4)=ss+s2; LDS(float,L+L_KVX+2048+ty*1024+l16+tt*4)=cz; SB();
  md(IC<6>{},2); md(IC<6>{},3);
  BAR();
  { const f32x4 z=LDS(f32x4,L+L_KVX+2048+ty*1024+l16); const float so_=LDS(float,L+L_KVX+(w^4)*256+lane*4);
    ss=(ss+s2)+so_; cz=(z[0]+z[1])+(z[2]+z[3]); }
  const float nmean=swp_hi(c1[12])*(-1.f/448);
  c1=__builtin_amdgcn_mfma_f32_32x32x2f32(cz,nmean,c1,0,0,0);
  const float rstd=__builtin_amdgcn_rsqf(__builtin_fmaxf(__builtin_fmaf(swp_add(ss),1.f/448,-nmean*nmean),0.f)+1e-5f);
  STAMP(2);
  const int wro=(tt*4096+t*64),c0=((2*hf)^((t>>2)&3))<<4;
  const rsrc_t rhh=mk(ws+WS_HH+i*458752,act?458752:0);
  const u32 v8=(lane>>3), p8=((lane&7)^(lane>>2&4))<<4, vw2=v8*896+p8, sW2=w*224*896+cs*128;
  const u32 r7=ROT?(u32)(((7*i3)>>2)+jq+XR*(i>>3))%7u:0u, r14=ROT?(u32)(((7*i3)>>1)+R14J*jq+XR*(i>>2))%14u:0u;
#define W2D(l,n,o) do{ const u32 s_=sW2+(o)+wrap((n)+r14,14)*14336; dma((l),vw2,r2,s_); dma((l)+1024,vw2,r2,s_+7168); }while(0)
  const int nq=(mu&4)|((mu&1)<<1)|((mu>>1)&1);
  const int ao=(8*hf+q4)*128+(q4>>1)*64+nq*8; const lptr a0=L+ao,a1=L+(ao^64);
  auto mkid=[&]{ u32 l=l16; PINW(l); const u32 v=(l&16)?0x3F800000u:0x3F80u; return u32x2{(l&32)?0u:v,(l&32)?v:0u}; };
  const u32 vo=(lane&7)*896+v8*16, so_=((2*th+tt2)*32+kq*8)*896+cs*128, vf=v8*512+(lane&7)*16+kq*128;
  const u32 vw1=v8*3584+p8;
  auto w1d=[&](int n,int q){ dma(w1l(n)+q*1024,vw1,r1,(q&1)*8*3584+j*256+(q>>1)*128+(wrap((n>>2)+r7,7)*4+(n&3))*16*3584); };
  u32x4 phq[4],w1f[28],w2f[14][2],xr; u32 b1v;
  auto pzh=[&]{ const u32x4 PZ=mkpz(); const u32 sh=(8*j+2*tt)*1024;
    sfor<0,4>([&](auto U){ st16(PZ,rhh,l16,sh+U.v*114688); st16<1024>(PZ,rhh,l16,sh+U.v*114688); }); };
  auto misc=[&]{ xr=ldp(act?vo:OOB,rx,so_);
    const int tq=t>>2; asm volatile("buffer_load_ushort %0, %1, %2, %3 offen":"=v"(b1v):"v"((((tq&4)|((tq&1)<<1)|((tq>>1)&1))*4+(t&3))*2),"s"(mk(b1,act?3584:0)),"s"(j*256+tt*64):"memory"); };
  auto rdw1=[&](auto P){ lptr q[4]; constexpr int n0=P.v?12:0, n1=P.v?20:8, n2=P.v?28:12;
#pragma unroll
    for(int u=0;u<4;u++){ q[u]=L+w1l(n0)+((tt&1)?ao^64:ao)+(tt>>1)*2048+((u+tt)&3)*4096; PINW(q[u]); }
#pragma unroll
    for(int n=n0;n<n1;n++) w1f[n]=tr2(q[n&3]+(w1l(n&~3)-w1l(n0)),q[n&3]+(w1l(n&~3)-w1l(n0))+512);
#pragma unroll
    for(int u=0;u<4;u++){ q[u]+=P.v?-32768:32768; PINW(q[u]); }
#pragma unroll
    for(int n=n1;n<n2;n++) w1f[n]=tr2(q[n&3]+(w1l(n&~3)-w1l(n1)),q[n&3]+(w1l(n&~3)-w1l(n1))+512); };
  auto rdw1a=[&]{ lwait(cnt+20,4); rdw1(IC<0>{}); asm volatile("s_waitcnt lgkmcnt(0)":::"memory"); lsig(cnt+24); };
  if(ty==0){
    f32x16 p=mfma(wp[0],pk8(c1,0),f32x16{}); p=mfma(wp[1],pk8(c1,8),p);
    u32x4 ph[4]; dsoftmax(p,rstd*LOG2E,ph);
    LDS(u32x4,L+L_PHI+wro+c0)=ph[0]; LDS(u32x4,L+L_PHI+wro+(c0^16))=ph[1];
    LDS(u32x4,L+L_PHI+2048+wro+c0)=ph[2]; LDS(u32x4,L+L_PHI+2048+wro+(c0^16))=ph[3];
    lsig(cnt);
    STAMP(3);
    if(tt<2){
      lwait(cnt,8);
      const int ko=(8*hf+q4)*64+(sg&1)*8; const lptr k0=L+ko+(((sg>>1)^(2*hf))<<4),k1=L+ko+256+(((sg>>1)^(2*hf+1))<<4);
      f32x16 c={}; u32x4 fa[8],fb[8];
#pragma unroll
      for(int u=0;u<8;u++){ const int o=(u>>1)*4096+(u&1)*1024; fa[u]=tr2(k0+L_PHI+tt*2048+o,k1+L_PHI+tt*2048+o); fb[u]=tr2(k0+L_VI+o,k1+L_VI+o); }
      SB();
#pragma unroll
      for(int u=0;u<8;u++) c=mfma(fa[u],fb[u],c);
      st16(pk8(c,0),rkv,l16,i*4096+tt*2048); st16<1024>(pk8(c,8),rkv,l16,i*4096+tt*2048);
      lsig(cnt+8);
      STAMP(4);
    } else {
      if(tt==2) asm volatile("buffer_load_dword %0, %1, %2, 0 offen sc1":"=v"(flv):"v"(vfl),"s"(rf):"memory");
      sfor<W1E,20>([&](auto K){ w1d(K.v,2*(tt&1)); w1d(K.v,2*(tt&1)+1); });
      if(tt==2){
        asm volatile("s_waitcnt vmcnt(%1)":"+v"(flv):"n"(2*(20-W1E)):"memory");
        NOUNR for(int it=0;it<SPIN;it++){
          if(__builtin_amdgcn_ballot_w64(lane<32&&flv!=tag)==0) break;
          asm volatile("buffer_load_dword %0, %1, %2, 0 offen sc1\n\ts_waitcnt vmcnt(0)":"+v"(flv):"v"(vfl),"s"(rf):"memory"); }
        lsig(cnt+12); } }
    lwait(cnt+8,2);
    STAMP(5);
    sfor<20,28>([&](auto K){ w1d(K.v,tt); });
    if(tt<2){ WAITV(8); lsig(cnt+12); }
    STAMP(6);
    lwait(cnt+12,3);
    STAMP(8);
    u32x4 g[16]; const u32 rk=4*i3;
    sfor<0,16>([&](auto N){ g[N.v]=ldc<0>(l16,rkv,((N.v+rk)&15)*4096+tt*1024); });
    STAMP(42);
    rdw1a();
    WAITV(16); lsig(cnt+28);
    misc();
    STAMP(7);
    f32x4 s[4]; const u32x2 id4=mkid();
    const int rr0=chain<16>(g,[&]{ s[0]=s[1]=s[2]=s[3]=f32x4{}; },[&](auto N){ waitr<17-N.v>(g[N.v]); },[&](auto N){ acc8(id4,g[N.v],s[2*(N.v&1)],s[2*(N.v&1)+1]); PINW(s[2*(N.v&1)]); PINW(s[2*(N.v&1)+1]); },[](auto){},
      [&](auto N){ rld<0>(g[N.v],l16,rkv,((N.v+rk)&15)*4096+tt*1024); });
    CNT(37,rr0);
    LDS(u32x4,L+L_KVX+tt*1024+l16)=pk44(s[0]+s[2],s[1]+s[3]);
    PRIO(0);
  } else {
#pragma unroll
    for(int r=0;r<16;r++) c1[r]*=rstd;
    u32x4 v0=pk8(c1,0),v1=pk8(c1,8); if(hf){ v1[2]=0x3F80u; v1[3]=0u; }
    LDS(u32x4,L+L_VI+wro+c0)=v0; LDS(u32x4,L+L_VI+wro+(c0^16))=v1;
    lsig(cnt);
    STAMP(3);
    sfor<nqc(5),7>([&](auto U){ wq(U.v); });
    sfor<0,W1E>([&](auto K){ w1d(K.v,tt); });
    STAMP(40);
    waitv<W1E>(); lsig(cnt+4);
    u32x4 fq[28]; float cq=0;
#pragma unroll
    for(int u=0;u<7;u++) fq[4*u]=tr2(aQ[0]+u*4096,aQ[0]+u*4096+256);
    lwait(cnt+4,4);
    STAMP(4);
    auto ord=[](int s){ return s<7?4*s:((s-7)/3)*4+1+(s-7)%3; };
    f32x16 cQ={};
    sfor<0,28>([&](auto S){ constexpr int s=S.v;
      cQ=mfma(fq[ord(s)],xf[ord(s)],cQ); PINW(cQ); SB();
      if constexpr(s<21){ constexpr int n=ord(s+7); fq[n]=tr2(aQ[n&3]+(n>>2)*4096,aQ[n&3]+(n>>2)*4096+256); }
      if constexpr(s<7){ cq=sum8(fq[4*s],cq); PINW(cq); }
      if constexpr(s==7) LDS(float,L+L_MISC+l16+tt*4)=cq;
      if constexpr(s==22) lsig(cnt+16);
      SB(); });
    lwait(cnt+16,4);
    const f32x4 z=LDS(f32x4,L+L_MISC+l16);
    cQ=__builtin_amdgcn_mfma_f32_32x32x2f32((z[0]+z[1])+(z[2]+z[3]),nmean,cQ,0,0,0);
    f32x16 p=mfma(wp[0],pk8(cQ,0),f32x16{}); p=mfma(wp[1],pk8(cQ,8),p); PINW(p);
    STAMP(5);
    dsoftmax(p,rstd*LOG2E,phq);
    PINW(phq[0]); PINW(phq[1]); PINW(phq[2]); PINW(phq[3]);
    WAITV(0); lsig(cnt+20);
    STAMP(6);
    rdw1a();
    lwait(cnt+28,4); rdw1(IC<1>{});
    lwait(cnt+24,8);
    STAMP(42);
    misc();
    { const u32 rw_=ROT?((7*i3)>>1)+XR*(i>>2):0; sfor<0,14>([&](auto U){ const u32 u=tt*14+wrap(U.v+rw_,14); dma(L_WO+u*1024,vw2,r0,u*7168+cs*128); }); }
    STAMP(7);
    PRIO(2);
  }
  BAR();
  STAMP(9);
  if(ty){ f32x16 c={};
#pragma unroll
    for(int n=0;n<4;n++) c=mfma(LDS(u32x4,L+L_KVX+n*1024+l16),phq[n],c);
    const float rd=__builtin_amdgcn_rcpf(swp_hi(c[12])+1e-6f);
#pragma unroll
    for(int r=0;r<16;r++) c[r]*=rd;
    ost(pk8(c,0),pk8(c,8));
    PRIO(0);
    WAITV(3); lsig(cnt+32);
  }
  else rdw1(IC<1>{});
  STAMP(10);
  if(act){
  { const u32 so=(2*th+tt2)*28672+kq*7168;
    lwait(cnt+32,4);
    if(!ty){ pzh(); W2D(w*4096,0,0); W2D(w*4096+2048,1,0); W2D(w*4096+16384,0,4*224*896); W2D(w*4096+18432,1,4*224*896); }
    lptr p0=a0+L_WO+kq*14336,p1=a1+L_WO+kq*14336; PINW(p0); PINW(p1);
    u32x4 fa[7][2]; f32x16 c[2];
#pragma unroll
    for(int n=0;n<7;n++){ fa[n][0]=tr2(p0+n*2048,p0+n*2048+512); fa[n][1]=tr2(p1+n*2048,p1+n*2048+512); PINW(fa[n][0]); PINW(fa[n][1]); }
    SB();
    STAMP(11);
    if(ty) WAITV(0); else WAITV(16);
    asm volatile("":"+v"(xr),"+v"(b1v)::"memory");
    BAR();
    STAMP(12);
    u32x4 of[7];
    sfor<0,7>([&](auto N){ of[N.v]=ldc<(N.v&3)*1024>(l16,ro,so+(N.v>>2)*4096); });
    auto r2d=[&](int n){ dma(L_R2+(n>>1)*16384+w*2048+(n&1)*1024,vw2,r2,sW2+wrap(2+(n>>1)+r14,14)*14336+(n&1)*7168); };
    STAMP(41);
    const int rr1=chain<7>(of,[&]{ c[0]=c[1]=f32x16{}; },[&](auto N){ waitr<6>(of[N.v]); },[&](auto N){ c[0]=mfma(fa[N.v][0],of[N.v],c[0]); PINW(c[0]); c[1]=mfma(fa[N.v][1],of[N.v],c[1]); PINW(c[1]); },[&](auto N){ r2d(N.v); if(N.v==6) r2d(7); },
      [&](auto N){ rld<(N.v&3)*1024>(of[N.v],l16,ro,so+(N.v>>2)*4096); });
    SB();
    CNT(38,rr1);
    lptr e=L+L_PX+w*4096+l16;
    LDS(u32x4,e)=pk8(c[0],0); LDS(u32x4,e+1024)=pk8(c[0],8); LDS(u32x4,e+2048)=pk8(c[1],0); LDS(u32x4,e+3072)=pk8(c[1],8);
    STAMP(13);
    BAR();
    e=L+L_PX+tt2*4096+vf;
    const u32x2 id4=mkid(); f32x4 s[4]={}; acc8(id4,xr,s[0],s[1]);
#pragma unroll
    for(int q=0;q<4;q++) acc8(id4,LDS(u32x4,e+q*8192),s[2*(q&1)],s[2*(q&1)+1]);
    s[0]+=s[2]; s[1]+=s[3];
    st16(pk44(s[0],s[1]),rh,vf,(2*th+tt2)*28672+cs*4096);
    float s1=((s[0][0]+s[0][1])+(s[0][2]+s[0][3]))+((s[1][0]+s[1][1])+(s[1][2]+s[1][3])), s2=s[0][0]*s[0][0], s3=s[1][0]*s[1][0];
#pragma unroll
    for(int q=1;q<4;q++){ s2=__builtin_fmaf(s[0][q],s[0][q],s2); s3=__builtin_fmaf(s[1][q],s[1][q],s3); }
    s1=red8(s1); s2=red8(s2+s3);
    asm volatile("buffer_store_dwordx2 %0, %1, %2, %3 offen sc1"::"v"(f32x2{s1,s2}),"v"(lane<8?lane*64:OOB),"s"(rst),"s"(((2*th+tt2)*32+kq*8)*64+cs*8):"memory");
    SB(); }
  STAMP(14);
  if(b==0&&tid==0) __hip_atomic_store((u32*)ws,tag,__ATOMIC_RELAXED,__HIP_MEMORY_SCOPE_AGENT);
  const u32 bX=ty?tt*6144:L_S+tt*12288, bY=ty?28672+tt*6144:bX+6144;
  W2D(L_R3+w*2048,6,0);
  { lptr p0=a0+w*4096,p1=a1+w*4096;
#pragma unroll
    for(int n=0;n<2;n++){ w2f[n][0]=tr2(p0+n*2048,p0+n*2048+512); w2f[n][1]=tr2(p1+n*2048,p1+n*2048+512); } }
  WAITV(4);
  { lptr p0=a0+L_R2+w*2048,p1=a1+L_R2+w*2048; PINW(p0); PINW(p1);
#pragma unroll
    for(int n=0;n<4;n++){ w2f[2+n][0]=tr2(p0+n*16384,p0+n*16384+512); w2f[2+n][1]=tr2(p1+n*16384,p1+n*16384+512); }
    asm volatile("s_waitcnt lgkmcnt(0)":"+v"(w2f[4][0]),"+v"(w2f[4][1])::"memory"); }
  W2D(L_R4+w*2048,7,0);
  WAITV(4);
  BAR();
  STAMP(15);
  const u32 sH=ty*57344+tt*1024, lH0=ty*28672+tt*1024, lH1=(ty?57344:86016)+tt*1024;
  if(ty) __builtin_amdgcn_s_sleep(4);
  u32x4 hb[28],sa,sb; float ks[2][5]; const u32 vst=t*64+hf*32;
  auto dm1=[&](u32 l,int k,int u){ dmac(l+u*4096,l16,rh,sH+k*28672+wrap(u+r7,7)*4096); };
  auto dmas=[&](u32 l,int k){ sfor<0,7>([&](auto U){ dm1(l,k,U.v); }); };
  dmas(lH0,0); sa=ldc<0>(vst,rst,ty*4096); sb=ldc<16>(vst,rst,ty*4096);
  float cb=0;
  if(ty){
#pragma unroll
    for(int n=14;n<28;n++) cb=sum8(w1f[n],cb);
    asm volatile("; b":"+v"(cb));
  } else {
#pragma unroll
    for(int n=0;n<14;n++) cb=sum8(w1f[n],cb);
    asm volatile("; a":"+v"(cb)); }
  LDS(float,L+(ty?86016:57344)+tt*1024+lane*4)=cb; SB();
  STAMP(36);
  sfor<0,3>([&](auto U){ W2D(L_S+tt*12288+ty*6144+U.v*2048,8+3*ty+U.v,-ty*4*224*896); });
  WAITV(15);
  { lptr p0=a0+L_R4+w*2048,p1=a1+L_R4+w*2048; PINW(p0); PINW(p1); w2f[7][0]=tr2(p0,p0+512); w2f[7][1]=tr2(p1,p1+512);
    w2f[6][0]=tr2(p0+(L_R3-L_R4),p0+(L_R3-L_R4)+512); w2f[6][1]=tr2(p1+(L_R3-L_R4),p1+(L_R3-L_R4)+512); }
  auto vrd=[&](u32 l){ lptr p=L+l+l16; PINW(p);
#pragma unroll
    for(int n=0;n<7;n++) hb[4*n]=LDS(u32x4,p+n*4096);
    if(hf){ sb[2]=0; sb[3]=0; } };
  auto vbad=[&]{ u32 m=mx4(mx4(0,sa),sb);
#pragma unroll
    for(int n=0;n<7;n++) m=mx4(m,hb[4*n]);
    return bad(m); };
  auto vfix=[&](u32 l,int k){
    NOUNR for(int it=0;it<SPIN;it++){
      sfor<0,7>([&](auto U){ if(chk(hb[4*U.v])) dm1(l,k,U.v); });
      if(bad(mx4(mx4(0,sa),sb))){ rld<0>(sa,vst,rst,ty*4096+k*2048); rld<16>(sb,vst,rst,ty*4096+k*2048); }
      asm volatile("s_waitcnt vmcnt(0)":"+v"(sa),"+v"(sb)::"memory");
      vrd(l); if(!vbad()) break; } };
  auto vst_=[&](float* o){
    const float s1=swp_add((u2f(sa[0])+u2f(sa[2]))+(u2f(sb[0])+u2f(sb[2])))*(-1.f/448), s2=swp_add((u2f(sa[1])+u2f(sa[3]))+(u2f(sb[1])+u2f(sb[3])));
    const float g_=__builtin_fmaf(__builtin_fmaxf(__builtin_fmaf(s2,1.f/448,-s1*s1),0.f),1.00001f,1e-10f);
    const float rs_=__builtin_amdgcn_rsqf(g_); o[3]=0.5f*__builtin_amdgcn_logf(g_);
    o[0]=g_*rs_; o[1]=-2.302208198f*rs_; o[2]=-0.1029432397f*rs_*rs_*rs_; o[4]=hf?o[0]:s1; };
  auto vpin=[&](float* o){ PINW(o[0]); PINW(o[1]); PINW(o[2]); PINW(o[3]); PINW(o[4]); };
  if(ty) BAR();
  asm volatile("s_waitcnt vmcnt(6)":"+v"(sa),"+v"(sb)::"memory"); vrd(lH0); vst_(ks[0]); if(vbad()){ vfix(lH0,0); vst_(ks[0]); } vpin(ks[0]);
  if(!ty) BAR();
  STAMP(16);
  cb=swp_add(cb+LDS(float,L+lH1+lane*4)); cb=hf?u2f(b1v<<16):cb; PINW(cb);
  dmas(lH1,1); sa=ldc<0>(vst,rst,ty*4096+2048); sb=ldc<16>(vst,rst,ty*4096+2048);
  if(ty) BAR();
  sfor<0,2>([&](auto KK){ constexpr int k=KK.v;
    lptr q[4];
#pragma unroll
    for(int u=1;u<4;u++){ q[u]=L+(k?lH1:lH0)+(((u+tt)&3)-tt)*1024+l16; PINW(q[u]); }
    f32x16 c={};
    auto ord=[](int s){ return s<7?4*s:((s-7)/3)*4+1+(s-7)%3; };
    PRIO(1);
    sfor<0,28>([&](auto S){ constexpr int s=S.v;
      c=mfma(w1f[ord(s)],hb[ord(s)],c); PINW(c); SB();
      if constexpr(s+LA>=7&&s+LA<28) hb[ord(s+LA)]=LDS(u32x4,q[ord(s+LA)&3]+(ord(s+LA)>>2)*4096);
      if constexpr(k==0&&s%8==2&&s<24){ if(ty) W2D(bX+(s/8)*2048,8+s/8,0); }
      SB(); });
    c=__builtin_amdgcn_mfma_f32_32x32x2f32(cb,ks[k][4],c,0,0,0); PINW(c);
    PRIO(0);
    SB(); if(k==0||!ty) BAR(); PINW(c); SB();
    STAMP(17+2*k);
    if constexpr(k==0){ if(ty){ W2D(bY,11,0); W2D(bY+2048,12,0); W2D(bY+4096,13,0); if(tid==256) LDS(u32,L+L_SIG)=0; WAITV(12); } else WAITV(0);
      asm volatile("":"+v"(sa),"+v"(sb)::"memory"); vrd(lH1); vst_(ks[1]); }
    u32 g[8]; const float sg_=ks[k][0],K1=ks[k][1],K2=ks[k][2],Lg=ks[k][3];
#pragma unroll
    for(int p=0;p<8;p++){ float a=c[2*p],d=c[2*p+1];
      float u=__builtin_fmaf(a,__builtin_fmaf(a*a,K2,K1),Lg), v=__builtin_fmaf(d,__builtin_fmaf(d*d,K2,K1),Lg);
      u=__builtin_amdgcn_rcpf(__builtin_amdgcn_exp2f(u)+sg_); v=__builtin_amdgcn_rcpf(__builtin_amdgcn_exp2f(v)+sg_);
      g[p]=pk(a*u,d*v);
      if(p==3) st16(u32x4{g[0],g[1],g[2],g[3]},rhh,l16,(2*ty+k)*114688+(8*j+2*tt)*1024); }
    st16<1024>(u32x4{g[4],g[5],g[6],g[7]},rhh,l16,(2*ty+k)*114688+(8*j+2*tt)*1024);
    if constexpr(k==0){ if(vbad()){ vfix(lH1,1); vst_(ks[1]); } vpin(ks[1]);
      BAR(); STAMP(18); }
    SB();
  });
  { const u32 s5=th*229376+w*14336, dY=th?-114688u:344064u; const int inl=ty|th, tl=tt2?3-2*th:2*th;
    auto su=[&](int n){ return s5+wrap(n+r14,14)*1024; };
    u32x4 hh[14],hp,q2;
    auto rdw2=[&]{ lptr p0=a0+bX,p1=a1+bX; PINW(p0); PINW(p1);
#pragma unroll
      for(int n=0;n<3;n++){ w2f[8+n][0]=tr2(p0+n*2048,p0+n*2048+512); w2f[8+n][1]=tr2(p1+n*2048,p1+n*2048+512); }
      p0=a0+bY; p1=a1+bY; PINW(p0); PINW(p1);
#pragma unroll
      for(int n=0;n<3;n++){ w2f[11+n][0]=tr2(p0+n*2048,p0+n*2048+512); w2f[11+n][1]=tr2(p1+n*2048,p1+n*2048+512); } };
    STAMP(20);
    sfor<0,14>([&](auto N){ hh[N.v]=ldc<0>(l16,rhh,su(N.v)); });
    WAITV(18); rdw2();
#pragma unroll
    for(int n=8;n<14;n++){ PINW(w2f[n][0]); PINW(w2f[n][1]); }
    if(ty!=th) WAITV(14);
    if(ty) lsig(L+L_SIG);
    STAMP(21);
    f32x16 c[2]; auto zc=[&]{ c[0]=c[1]=f32x16{}; };
    auto mm=[&](auto N){ c[0]=mfma(w2f[N.v][0],hh[N.v],c[0]); PINW(c[0]); c[1]=mfma(w2f[N.v][1],hh[N.v],c[1]); PINW(c[1]); };
    const int rr2=chain<14>(hh,zc,[&](auto N){ waitb<13,13-N.v>(hh[N.v],inl); },mm,[&](auto N){ if constexpr(XS==1&&N.v%2==0&&N.v<12) STAMP(26+N.v/2); rldb(hh[N.v],l16,rhh,su(N.v)+dY,inl); },
      [&](auto N){ rld<0>(hh[N.v],l16,rhh,su(N.v)); });
    STAMP(22); CNT(39,rr2);
    lptr e=L+bX+l16; PINW(e);
    LDS(u32x4,e)=pk8(c[0],0); LDS(u32x4,e+1024)=pk8(c[0],8); LDS(u32x4,e+2048)=pk8(c[1],0); LDS(u32x4,e+3072)=pk8(c[1],8);
    if(!inl){ lwait(L+L_SIG,4); sfor<0,14>([&](auto N){ rld<0>(hh[N.v],l16,rhh,su(N.v)+dY); }); }
    hp=ldc<0>(vf,rh,tl*28672+cs*4096); q2=ldp(v8*16,mk(b2,896),cs*128);
    STAMP(23);
    const int rr3=chain<14>(hh,zc,[&](auto N){ waitr<15-N.v>(hh[N.v]); },mm,[](auto){},
      [&](auto N){ rld<0>(hh[N.v],l16,rhh,su(N.v)+dY); });
    STAMP(24); CNT(47,rr3);
    e=L+bY+l16; PINW(e);
    LDS(u32x4,e)=pk8(c[0],0); LDS(u32x4,e+1024)=pk8(c[0],8); LDS(u32x4,e+2048)=pk8(c[1],0); LDS(u32x4,e+3072)=pk8(c[1],8);
    asm volatile("s_waitcnt vmcnt(0)":"+v"(q2),"+v"(hp)::"memory");
    BAR();
    lptr e0=L+tt2*28672+vf,e1=L+L_S+tt2*6144+vf; PINW(e0); PINW(e1);
    const u32x2 id4=mkid(); f32x4 s[4]={}; acc8(id4,hp,s[0],s[1]); acc8(id4,q2,s[2],s[3]);
#pragma unroll
    for(int q=0;q<4;q++){ acc8(id4,LDS(u32x4,e0+q*6144),s[0],s[1]); acc8(id4,LDS(u32x4,e1+q*12288),s[2],s[3]); }
    __builtin_amdgcn_raw_buffer_store_b128(pk44(s[0]+s[2],s[1]+s[3]),mk(out+(size_t)i*128*448,128*896),vo,(tl*32+kq*8)*896+cs*128,16); }
  }
  STAMP(25);
}
}
#ifdef __HIP_DEVICE_COMPILE__
#pragma clang attribute pop
#endif

__global__ void nul_small(float* p){ if(p==nullptr) __builtin_trap(); }
__global__ __launch_bounds__(512,2) void nul_big(float* p){ extern __shared__ char sm2[]; asm volatile("v_mov_b32 v255, 0":::"v255"); if(p==nullptr) sm2[0]=1; }
__global__ __launch_bounds__(512,2) void nul_mid(float* p){ if(p==nullptr) __builtin_trap(); }
extern "C" void kernel_launch(void* const* d_in,const int* in_sizes,int n_in,void* d_out,int out_size,void* d_ws,size_t ws_size,hipStream_t stream){
  typedef const unsigned short* P;
  (void)hipFuncSetAttribute((const void*)fk::fused,hipFuncAttributeMaxDynamicSharedMemorySize,163840);
  fk::fused<<<256,512,163840,stream>>>((P)d_in[0],(P)d_in[1],(P)d_in[2],(P)d_in[3],(P)d_in[5],(P)d_in[6],(P)d_in[7],(P)d_in[8],(unsigned short*)d_out,(char*)d_ws);
}
```

```cpp
#define D7K 24
#define PP 2
#define PK 0
#define NW6 11
#define NW5 0
#define PFO 1
#define PFB 1
#include <hip/hip_runtime.h>
#include <stdint.h>
#if defined(__HIP_DEVICE_COMPILE__)&&!PK
#pragma clang attribute push(__attribute__((target("no-packed-fp32-ops"))),apply_to=function)
#endif
namespace fk {
typedef unsigned short u16; typedef unsigned u32;
typedef __attribute__((ext_vector_type(4))) u32 u32x4;
typedef __attribute__((ext_vector_type(2))) u32 u32x2;
typedef __attribute__((ext_vector_type(8))) __bf16 bf16x8;
typedef __attribute__((ext_vector_type(2))) __bf16 bf16x2;
typedef __attribute__((ext_vector_type(2))) float f32x2;
typedef __attribute__((ext_vector_type(4))) float f32x4;
typedef __attribute__((ext_vector_type(16))) float f32x16;
typedef short v4i16_t __attribute__((ext_vector_type(4)));
typedef __attribute__((address_space(3))) char* lptr;
typedef __amdgpu_buffer_rsrc_t rsrc_t;
#define DI __device__ __forceinline__
#define LDS(T,a) (*(__attribute__((address_space(3))) T*)(a))
#define STAMP(n)
#define CNT(n,v)
#define MINST 0
#define CST(n)
DI void dly(u32 d){ uint64_t t0_,t1_; asm volatile("s_memrealtime %0\n\ts_waitcnt lgkmcnt(0)":"=s"(t0_)::"memory"); do{ asm volatile("s_memrealtime %0\n\ts_waitcnt lgkmcnt(0)":"=s"(t1_)::"memory"); }while((u32)t1_-(u32)t0_<d); }
#define D5K 0
#define D5V 0
#define D7B D7K
#define D7V 0
#define SL7 4
#define FLIP 0
#define KC 0
#define KI 0
#define S0 1
#define D8K 0
#define D8V 0
constexpr u32 WS_FLAG=4096, WS_ST=512u<<10, WS_KV=1u<<20, WS_O=2u<<20, WS_H=4u<<20, WS_HH=8u<<20, OOB=0x80000000u;
constexpr int L_WK=98304, L_MISC=155648, L_CNT=L_MISC+2048, L_ONE=L_CNT+64, L_KVX=159744;
constexpr int L_PHI=32768, L_VI=49152, L_WO=98304, L_R2=32768, L_R3=98304, L_R4=65536, L_S=114688, L_SIG=53248, L_PX=131072;
constexpr int w1l(int ks){ return ks<12?98304+ks*4096:ks<20?65536+(ks-12)*4096:32768+(ks-20)*4096; }
constexpr int w1o(int k){ return k<4?k:k<8?k+3:k==8?4:k==9?5:k==10?11:6; }
constexpr int xs(int c){ return (c%6==0?4:c%6==5?5:c%6-1)*16384; }
constexpr float LOG2E=1.4426950408889634f;
#define DEP 3
#define PZS 3
#define NQE 7
#define NX 0
#define WB7 0
#define W1E 12
#define LA 6
#define R14J 2
#define XR 0
#define XS 0
#define PFC 11
#define ROT 1
constexpr int yng(int c){ return ((c-1+DEP<6?c-1+DEP:6)-c)*3; }
constexpr int nq(int s){ return s+DEP<=6||s<4?0:s==4?(NQE<4?NQE:4):s==5?(NQE>4?NQE-4:0):0; }
constexpr int nqc(int s){ return s<4?0:nqc(s-1)+nq(s); }
#define NOUNR _Pragma("clang loop unroll(disable)")
DI float u2f(u32 v){ return __builtin_bit_cast(float,v); }
DI u32 f2u(float v){ return __builtin_bit_cast(u32,v); }
template<int V> struct IC{ static constexpr int v=V; };
template<int I,int N,class F> DI void sfor(F&& f){ if constexpr(I<N){ f(IC<I>{}); sfor<I+1,N>(f); } }
DI rsrc_t mk(const void* p,u32 bytes){ return __builtin_amdgcn_make_buffer_rsrc((void*)p,0,bytes,0x00020000); }
DI void dma(u32 l,u32 voff,rsrc_t r,u32 soff){ asm volatile("s_mov_b32 m0, %0\n\ts_nop 0\n\tbuffer_load_dwordx4 %1, %2, %3 offen lds"::"s"(l),"v"(voff),"s"(r),"s"(soff):"memory"); }
#define WAITV(n) asm volatile("s_waitcnt vmcnt(" #n ")":::"memory")
#define BAR() asm volatile("s_waitcnt lgkmcnt(0)\n\ts_barrier":::"memory")
#define PIN(x) asm volatile(""::"v"(x))
#define PINW(x) asm volatile("":"+v"(x))
#define SB() __builtin_amdgcn_sched_barrier(0)
DI f32x16 mfma(u32x4 a,u32x4 b,f32x16 c){ return __builtin_amdgcn_mfma_f32_32x32x16_bf16(__builtin_bit_cast(bf16x8,a),__builtin_bit_cast(bf16x8,b),c,0,0,0); }
DI f32x4 m44(u32x2 a,u32x2 b,f32x4 c){ return __builtin_amdgcn_mfma_f32_4x4x4bf16_1k(__builtin_bit_cast(v4i16_t,a),__builtin_bit_cast(v4i16_t,b),c,0,0,0); }
DI u32x2 tr(lptr p){ return __builtin_bit_cast(u32x2,__builtin_amdgcn_ds_read_tr16_b64_v4i16((__attribute__((address_space(3))) v4i16_t*)p)); }
DI u32x4 tr2(lptr p0,lptr p1){ u32x2 a=tr(p0),b=tr(p1); return u32x4{a[0],a[1],b[0],b[1]}; }
DI u32 pk(float lo,float hi){ return __builtin_bit_cast(u32,__builtin_convertvector(f32x2{lo,hi},bf16x2)); }
DI float dot2(u32 a,u32 b,float c){ return __builtin_amdgcn_fdot2_f32_bf16(__builtin_bit_cast(bf16x2,a),__builtin_bit_cast(bf16x2,b),c,false); }
DI float swp_lo(float v){ auto r=__builtin_amdgcn_permlane32_swap(f2u(v),f2u(v),false,false); return u2f(r[0]); }
DI float swp_hi(float v){ auto r=__builtin_amdgcn_permlane32_swap(f2u(v),f2u(v),false,false); return u2f(r[1]); }
DI float swp_add(float v){ auto r=__builtin_amdgcn_permlane32_swap(f2u(v),f2u(v),false,false); return u2f(r[0])+u2f(r[1]); }
DI float swp_max(float v){ auto r=__builtin_amdgcn_permlane32_swap(f2u(v),f2u(v),false,false); return __builtin_fmaxf(u2f(r[0]),u2f(r[1])); }
DI float swp_min(float v){ auto r=__builtin_amdgcn_permlane32_swap(f2u(v),f2u(v),false,false); return __builtin_fminf(u2f(r[0]),u2f(r[1])); }
DI float red8(float v){ v+=u2f(__builtin_amdgcn_update_dpp(0u,f2u(v),0x128,0xf,0xf,false)); auto r=__builtin_amdgcn_permlane16_swap(f2u(v),f2u(v),false,false); return swp_add(u2f(r[0])+u2f(r[1])); }
DI u32x4 pk8(const f32x16& c,int b){ return u32x4{pk(c[b],c[b+1]),pk(c[b+2],c[b+3]),pk(c[b+4],c[b+5]),pk(c[b+6],c[b+7])}; }
DI void dsoftmax(const f32x16& p,float c,u32x4* ph){
  float mx=p[0],mn=p[0];
#pragma unroll
  for(int r=1;r<14;r++){ mx=__builtin_fmaxf(mx,p[r]); mn=__builtin_fminf(mn,p[r]); }
  mx=swp_max(mx); mn=swp_min(mn);
  const float a=-mx*c,b=mn*c; f32x16 e,g; float s=0,u=0;
#pragma unroll
  for(int r=0;r<14;r++){ e[r]=__builtin_amdgcn_exp2f(__builtin_fmaf(p[r],c,a)); g[r]=__builtin_amdgcn_exp2f(__builtin_fmaf(p[r],-c,b)); s+=e[r]; u+=g[r]; }
  s=__builtin_amdgcn_rcpf(swp_add(s)); u=__builtin_amdgcn_rcpf(swp_add(u));
#pragma unroll
  for(int r=0;r<14;r++){ e[r]*=s; g[r]*=u; }
  ph[0]=pk8(e,0); ph[1]=u32x4{pk(e[8],e[9]),pk(e[10],e[11]),pk(e[12],e[13]),0u};
  ph[2]=pk8(g,0); ph[3]=u32x4{pk(g[8],g[9]),pk(g[10],g[11]),pk(g[12],g[13]),0u};
}
DI void dmac(u32 l,u32 voff,rsrc_t r,u32 soff){ asm volatile("s_mov_b32 m0, %0\n\ts_nop 0\n\tbuffer_load_dwordx4 %1, %2, %3 offen sc1 lds"::"s"(l),"v"(voff),"s"(r),"s"(soff):"memory"); }
template<int O> DI void rld(u32x4& v,u32 voff,rsrc_t r,u32 soff){ asm volatile("buffer_load_dwordx4 %0, %1, %2, %3 offen offset:%4 sc1":"+v"(v):"v"(voff),"s"(r),"s"(soff),"n"(O):"memory"); }
template<int O> DI u32x4 ldc(u32 voff,rsrc_t r,u32 soff){ u32x4 v; asm volatile("buffer_load_dwordx4 %0, %1, %2, %3 offen offset:%4 sc1":"=v"(v):"v"(voff),"s"(r),"s"(soff),"n"(O):"memory"); return v; }
DI u32x4 ldp(u32 voff,rsrc_t r,u32 soff){ u32x4 v; asm volatile("buffer_load_dwordx4 %0, %1, %2, %3 offen":"=v"(v):"v"(voff),"s"(r),"s"(soff):"memory"); return v; }
template<int N> DI void waitv(){ asm volatile("s_waitcnt vmcnt(%0)"::"n"(N):"memory"); }
template<int O=0> DI void st16(u32x4 v,rsrc_t r,u32 voff,u32 soff){ asm volatile("buffer_store_dwordx4 %0, %1, %2, %3 offen offset:%4 sc1\n\ts_nop 1"::"v"(v),"v"(voff),"s"(r),"s"(soff),"n"(O):"memory"); }
DI u32 mx3(u32 a,u32 b,u32 c){ u32 r; asm("v_max3_u32 %0, %1, %2, %3":"=v"(r):"v"(a),"v"(b),"v"(c)); return r; }
DI u32 mx4(u32 m,u32x4 g){ return mx3(mx3(m,g[0],g[1]),g[2],g[3]); }
DI bool bad(u32 m){ return __builtin_amdgcn_ballot_w64(m==0xFFFFFFFFu)!=0; }
constexpr int SPIN=30000;
DI void lsig(lptr p){ asm volatile("s_mov_b64 exec, 1\n\tds_add_u32 %0, %1\n\ts_mov_b64 exec, -1"::"v"(p),"v"(1u):"memory"); }
DI void lwait(lptr p,u32 n){ NOUNR for(int it=0;it<SPIN*8;it++){ u32 v; asm volatile("ds_read_b32 %0, %1\n\ts_waitcnt lgkmcnt(0)":"=v"(v):"v"(p):"memory"); if(__builtin_amdgcn_readfirstlane(v)>=n) break; } }
DI float sum8(u32x4 f,float c){ c=dot2(f[0],0x3F803F80u,c); c=dot2(f[1],0x3F803F80u,c); c=dot2(f[2],0x3F803F80u,c); return dot2(f[3],0x3F803F80u,c); }
DI float sq8(u32x4 f,float c){ c=dot2(f[0],f[0],c); c=dot2(f[1],f[1],c); c=dot2(f[2],f[2],c); return dot2(f[3],f[3],c); }
DI void acc8(u32x2 id4,u32x4 g,f32x4& a,f32x4& b){ a=m44(id4,u32x2{g[0],g[1]},a); b=m44(id4,u32x2{g[2],g[3]},b); }
DI u32x4 pk44(f32x4 a,f32x4 b){ return u32x4{pk(a[0],a[1]),pk(a[2],a[3]),pk(b[0],b[1]),pk(b[2],b[3])}; }
DI bool chk(u32x4 g){ return bad(mx3(mx3(g[0],g[1],g[2]),g[3],g[3])); }
template<int N> DI void waitr(u32x4& v){ asm volatile("s_waitcnt vmcnt(%1)":"+v"(v):"n"(N):"memory"); }
template<int A,int B> DI void waitb(u32x4& v,int c){ asm volatile("s_cmp_lg_u32 %1, 0\n\ts_waitcnt vmcnt(%2)\n\ts_cbranch_scc1 1f\n\ts_waitcnt vmcnt(%3)\n1:":"+v"(v):"s"(c),"n"(A),"n"(B):"memory","scc"); }
DI void rldb(u32x4& v,u32 voff,rsrc_t r,u32 soff,int c){ asm volatile("s_cmp_eq_u32 %4, 0\n\ts_cbranch_scc1 1f\n\tbuffer_load_dwordx4 %0, %1, %2, %3 offen sc1\n1:":"+v"(v):"v"(voff),"s"(r),"s"(soff),"s"(c):"memory","scc"); }
DI u32x4 mkpz(){ u32x4 p={~0u,~0u,~0u,~0u}; PINW(p); return p; }
DI u32 wrap(u32 v,u32 n){ return v>=n?v-n:v; }
DI f32x2 gl1(f32x2 a,f32x2 k,f32x2 l){ f32x2 t; asm("v_pk_mul_f32 %0, %1, %1\n\tv_pk_fma_f32 %0, %0, %2, %2 op_sel:[0,0,1] op_sel_hi:[1,0,1]\n\tv_pk_fma_f32 %0, %1, %0, %3 op_sel_hi:[1,1,0]":"=&v"(t):"v"(a),"v"(k),"v"(l)); return t; }
template<int N,class I,class W,class U,class X,class R> DI int chain(u32x4* g,I&& init,W&& wt,U&& use,X&& ex,R&& re){
  int it=0;
  NOUNR for(;it<SPIN;it++){
    init(); u32 m=0;
    sfor<0,N>([&](auto K){ wt(K); use(K); m=mx4(m,g[K.v]); PINW(m); ex(K); SB(); });
    if(!bad(m)) break;
    WAITV(0); sfor<0,N>(re); WAITV(0); }
  return it; }
#define PRIO(n) __builtin_amdgcn_s_setprio(n)

__global__ __launch_bounds__(512,2) void fused(const u16* __restrict__ x,const u16* __restrict__ wqkv,const u16* __restrict__ wphi,const u16* __restrict__ wo,
    const u16* __restrict__ w1,const u16* __restrict__ b1,const u16* __restrict__ w2,const u16* __restrict__ b2,u16* __restrict__ out,char* __restrict__ ws
    ){
  extern __shared__ __attribute__((aligned(16))) char smem[];
  const lptr L=(lptr)smem;
  const int tid=__builtin_amdgcn_workitem_id_x(),lane=tid&63,w=__builtin_amdgcn_readfirstlane(tid>>6),tt=w&3,ty=w>>2,t=lane&31,hf=lane>>5;
  const int b=__builtin_amdgcn_workgroup_id_x(),i3=(b>>3)&3,jq=b>>5,th=(b&1)^FLIP,i=((b&7)>>1)*4+i3;
  const int j1=th*8+jq,odd=j1&1;
  const bool act=jq<7; const int j=th*7+jq,cs=jq;
  STAMP(0);
  asm volatile(""::"s"(x),"s"(wqkv),"s"(wphi),"s"(wo),"s"(w1),"s"(b1),"s"(w2),"s"(b2),"s"(out),"s"(ws));
  const u32 epoch=*(const __attribute__((address_space(4))) u32*)ws;
  const rsrc_t rw=mk(wqkv,448*2688), rx=mk(x+(size_t)i*128*448,128*896);
  const u32 vw=(lane>>2)*2688+(lane&3)*16, hoff=56*j1-8*odd+tt*16*2688, sW=hoff+(ty+1)*896, lW=L_WK+ty*28672+tt*1024;
  const u32 vx0=(lane>>3)*896+(((lane&7)^(lane>>4))<<4), vx1=vx0^64, sX=(tt*32+ty*16)*896, lX=tt*4096+ty*2048;
  const u32 rot=ROT?(u32)(j1+2*i3+XR*(i>>2))%7u:0u;
  auto cc=[&](int c){ return wrap(c+rot,7); };
  auto ch=[&](int c,int u){ const u32 q_=cc(c); if(u==0) dma(lW+c*4096,vw,rw,sW+q_*(64*2688)); else dma(xs(c)+lX+(u-1)*1024,u==1?vx0:vx1,rx,sX+(u-1)*7168+q_*128); };
#define SBAR() asm volatile("s_barrier":::"memory")
  if(w<2) dma(L_MISC+w*1024,tid*16,mk(wphi+j1*784,1568),0);
  u32 pfc;
  { uint64_t pc; u32 len; asm volatile("s_getpc_b64 %0\n.Lfk_c0:\n\ts_mov_b32 %1, .Lfk_c1-.Lfk_c0":"=s"(pc),"=s"(len));
    asm volatile("buffer_load_dword %0, %1, %2, 0 offen":"=v"(pfc):"v"(lane<PFC?((b>>3)*PFC+lane)*128:OOB),"s"(mk((const void*)pc,w==7?len:0u)):"memory"); }
  const u32 l16=lane*16;
  const rsrc_t ro=mk(ws+WS_O+i*114688,114688), rkv=mk(ws+WS_KV+j1*65536,65536), rh=mk(ws+WS_H+i*114688,114688), rst=mk(ws+WS_ST+i*8192,8192);
  auto ost=[&](u32x4 o0,u32x4 o1){ const u32 ub=t*16+((7*j1)>>1)*512, so=tt*28672;
    st16(o0,ro,ub+(odd+2*hf)*512,so); st16(o1,ro,hf?OOB:ub+(odd+1)*512,so);
    __builtin_amdgcn_raw_buffer_store_b64(u32x2{o1[0],o1[1]},ro,hf?ub+(odd?8:1536):OOB,so,16); };
  STAMP(32);
  sfor<0,DEP>([&](auto C){ if constexpr(C.v>0) SBAR(); ch(C.v,0); ch(C.v,1); ch(C.v,2); STAMP(33+C.v); });
  auto poison=[&]{ const u32x4 PZ=mkpz();
    if(ty){ const int t1=j1>=7; const u32 so=(2*t1+(tt&1))*28672+(4*(j1-7*t1)+(tt>>1))*1024, v=j1<14?l16:OOB; st16(PZ,rh,v,so); st16<2048>(PZ,rh,v,so);
      st16(PZ,rst,lane<8?l16:OOB,(j1*4+tt)*128); }
    else { st16(PZ,rkv,l16,i*4096+tt*1024); ost(PZ,PZ); } };
  if(tid<3) LDS(u32x4,L+L_CNT+tid*16)=u32x4{0,0,0,0};
  if(tid<2) LDS(u32x2,L+L_ONE+tid*256)=u32x2{0x3F80u,0u};
  const int tt2=w&1, kq=w>>1;
  const int q4=(lane&15)>>2,mu=((lane>>4)&1)*4+(lane&3),sg=4*(mu&1)+(mu>>1);
  const int rowo=(8*hf+q4)*64, kslot=(mu+odd)&7;
  lptr aW[4],aQ[4],xb[4];
  { const int sw=hf^((t>>1)&7), sl=ty?(odd?(sg==7?0:((sg+1)%7)+1):sg):kslot;
#pragma unroll
    for(int u=0;u<4;u++){ const int r=(u+tt+2*ty)&3; aW[u]=mu==7?L+L_ONE:L+L_WK+ty*28672+rowo+sl*8+r*1024; aQ[u]=L+rowo+kslot*8+r*1024; xb[u]=L+(tt*32+t)*128+((sw^(r*2))<<4); PINW(aW[u]); PINW(xb[u]); } }
  u32 tag=epoch*0x9E3779B1u+0x7F4A7C15u; asm volatile("":"+s"(tag));
  const rsrc_t r1=mk(w1,act?448*3584:0), r2=mk(w2,act?1792*896:0), r0=mk(wo,act?448*896:0);
  const lptr cnt=L+L_CNT;
  const rsrc_t rf=mk(ws,1u<<20); const u32 vfl=lane<16?WS_FLAG+(j1*16+lane)*128:lane<32?WS_FLAG+((lane-16)*16+i)*128:OOB; u32 flv;
  STAMP(43);
  u32 stp=mu==7?0:4096; PINW(stp);
  const u32 tq2=(tt+2)&3, hq=56*j1-8*odd+tq2*16*2688;
  auto wq=[&](int u){ dma(tq2*1024+u*4096,vw,rw,hq+cc(u)*(64*2688)); };
  const u32 v8=(lane>>3), p8=((lane&7)^(lane>>2&4))<<4, r7=ROT?(u32)(((7*i3)>>2)+jq+XR*(i>>3))%7u:0u;
  const u32 vw1=v8*3584+p8;
  auto w1d=[&](int n,int q){ dma(w1l(n)+q*1024,vw1,r1,(q&1)*8*3584+j*256+(q>>1)*128+(wrap((n>>2)+r7,7)*4+(n&3))*16*3584); };
  u32 pfo; auto pfw=[&]{ asm volatile("buffer_load_dword %0, %1, %2, %3 offen":"=v"(pfo):"v"(lane<28?lane*896:OOB),"s"(r0),"s"((i3*112+tt*28)*896+cs*128):"memory"); };
  const u32 r14=ROT?(u32)(((7*i3)>>1)+R14J*jq+XR*(i>>2))%14u:0u;
  u32 pfb; auto pfx=[&](int p){ const u32 s0=wrap(2*p+r14,14)*14336, s1=wrap(2*p+1+r14,14)*14336;
    asm volatile("buffer_load_dword %0, %1, %2, %3 offen":"=v"(pfb):"v"(((lane&16)?s1:s0)+(lane>>5)*(224*896)+(lane&15)*896),"s"(r2),"s"(tt*(448*896)+cs*128):"memory"); };
  u32x4 xf[28],wp[2],fw[2][4]; f32x16 c1={}; float ss=0,s2=0,cz=0;
  auto rd=[&](auto C,int u){ constexpr int c=C.v; fw[c&1][u]=tr2(aW[u],aW[u]+256); xf[4*c+u]=LDS(u32x4,xb[u]+xs(c)); if(c<6) aW[u]+=stp; };
  auto md=[&](auto C,int u){ constexpr int c=C.v; c1=mfma(fw[c&1][u],xf[4*c+u],c1); PINW(c1); SB(); };
  auto dd=[&](auto C,int u){ constexpr int c=C.v;
    if(u==0){ cz=sum8(fw[c&1][0],cz); PINW(cz); } else if(u==1){ ss=sq8(xf[4*c],ss); PINW(ss); } else if(u==2){ s2=sq8(xf[4*c+1],s2); PINW(s2); } };
  sfor<0,7>([&](auto C){ constexpr int c=C.v;
    constexpr int pz=c>PZS&&c<PZS+DEP, fl=c>PZS+DEP;
    if(ty) waitv<yng(c)+3*pz+nqc(c-1)+(c==6?NW5+(PFO==3):0)>(); else if(fl&&w==2) waitv<yng(c)+4*pz+1>(); else waitv<yng(c)+4*pz>();
    if constexpr(c<3) STAMP(44+c);
    if constexpr(c==0) PIN(pfc);
    if constexpr(c==0) BAR(); else SBAR();
    if constexpr(c==0) STAMP(1); else if constexpr(!XS) STAMP(25+c);
    if constexpr(c==PZS+DEP){ if(tid==128) __hip_atomic_store((u32*)(ws+WS_FLAG+(j1*16+i)*128),tag,__ATOMIC_RELAXED,__HIP_MEMORY_SCOPE_AGENT); }
    if constexpr(c==6){ if(!ty) PRIO(2); }
    if constexpr(c==PZS) poison();
    sfor<0,4>([&](auto U){ constexpr int u=U.v;
      if constexpr(c>0&&c<6) md(IC<c-1>{},u);
      rd(C,u);
      if constexpr(c+DEP<=6&&u<3) ch(c+DEP,u);
      if constexpr(u<nq(c)){ if(ty) wq(nqc(c-1)+u); }
      if constexpr(c==5&&u==3){ if(ty) sfor<0,NW5>([&](auto K){ w1d(w1o(K.v),tt); }); }
      if constexpr(c==6){ if(ty) sfor<NW5+(NW6-NW5)*u/4,NW5+(NW6-NW5)*(u+1)/4>([&](auto K){ w1d(w1o(K.v),tt); }); }
      if constexpr(PFO==1&&c==6&&u==3||PFO==3&&c==5&&u==3){ if(ty){ pfw(); sfor<0,7>([&](auto P){ if constexpr(PFB>>P.v&1) pfx(P.v); }); } }
      if constexpr(c==6&&u>=4-NX){ if(ty) w1d(16+u,tt); }
      if constexpr(c>0&&c<6) dd(IC<c-1>{},u);
      SB(); });
    if constexpr(c==0){
      const bool mv=(t&26)!=26; const lptr p=L+L_MISC+(t<26?t:t-2)*2+hf*224; u32 e[16];
#pragma unroll
      for(int q=0;q<16;q++){ const int d=16*(q>>3)+8*((q>>2)&1)+(q&3); e[q]=LDS(u16,p+d*56); }
#pragma unroll
      for(int q=0;q<8;q++){ u32 v=e[2*q]|(e[2*q+1]<<16); v=(mv&&(q<6||hf==0))?v:0u; asm volatile("":"+v"(v)); wp[q>>2][q&3]=v; } SB(); }
    if constexpr(c==5) sfor<0,4>([&](auto U){ md(C,U.v); dd(C,U.v); SB(); });
  });
  md(IC<6>{},0); dd(IC<6>{},0); dd(IC<6>{},1); SB(); md(IC<6>{},1); dd(IC<6>{},2);
  LDS(float,L+L_KVX+w*256+lane*4)=ss+s2; LDS(float,L+L_KVX+2048+ty*1024+l16+tt*4)=cz; SB();
  md(IC<6>{},2); md(IC<6>{},3);
  if(WB7){ if(ty) waitv<NW6+NX+(PFO==1||PFO==3)>(); }
  BAR();
  { const f32x4 z=LDS(f32x4,L+L_KVX+2048+ty*1024+l16); const float so_=LDS(float,L+L_KVX+(w^4)*256+lane*4);
    ss=(ss+s2)+so_; cz=(z[0]+z[1])+(z[2]+z[3]); }
  const float nmean=swp_hi(c1[12])*(-1.f/448);
  c1=__builtin_amdgcn_mfma_f32_32x32x2f32(cz,nmean,c1,0,0,0);
  const float rstd=__builtin_amdgcn_rsqf(__builtin_fmaxf(__builtin_fmaf(swp_add(ss),1.f/448,-nmean*nmean),0.f)+1e-5f);
  STAMP(2);
  const int wro=(tt*4096+t*64),c0=((2*hf)^((t>>2)&3))<<4;
  const rsrc_t rhh=mk(ws+WS_HH+i*458752,act?458752:0);
  const u32 vw2=v8*896+p8, wk=w^(4*th), d4=th?0u-4*224*896:4*224*896, sW2=wk*224*896+cs*128;
#define W2D(l,n,o) do{ const u32 s_=sW2+(o)+wrap((n)+r14,14)*14336; dma((l),vw2,r2,s_); dma((l)+1024,vw2,r2,s_+7168); }while(0)
  const int nq=(mu&4)|((mu&1)<<1)|((mu>>1)&1);
  const int ao=(8*hf+q4)*128+(q4>>1)*64+nq*8; const lptr a0=L+ao,a1=L+(ao^64);
  auto mkid=[&]{ u32 l=l16; PINW(l); const u32 v=(l&16)?0x3F800000u:0x3F80u; return u32x2{(l&32)?0u:v,(l&32)?v:0u}; };
  const u32 vo=(lane&7)*896+v8*16, so_=((2*th+tt2)*32+kq*8)*896+cs*128, vf=v8*512+(lane&7)*16+kq*128;
  u32x4 phq[4],w1f[28],w2f[14][2],xr; u32 b1v;
  auto pzh=[&]{ const u32x4 PZ=mkpz(); const u32 sh=(8*j+2*tt)*1024;
    sfor<0,4>([&](auto U){ st16(PZ,rhh,l16,sh+U.v*114688); st16<1024>(PZ,rhh,l16,sh+U.v*114688); }); };
  auto misc=[&]{ xr=ldp(act?vo:OOB,rx,so_);
    const int tq=t>>2; asm volatile("buffer_load_ushort %0, %1, %2, %3 offen":"=v"(b1v):"v"((((tq&4)|((tq&1)<<1)|((tq>>1)&1))*4+(t&3))*2),"s"(mk(b1,act?3584:0)),"s"(j*256+tt*64):"memory"); };
  auto rdw1=[&](auto P){ lptr q[4]; constexpr int n0=P.v?12:0, n1=P.v?20:8, n2=P.v?28:12;
#pragma unroll
    for(int u=0;u<4;u++){ q[u]=L+w1l(n0)+((tt&1)?ao^64:ao)+(tt>>1)*2048+((u+tt)&3)*4096; PINW(q[u]); }
#pragma unroll
    for(int n=n0;n<n1;n++) w1f[n]=tr2(q[n&3]+(w1l(n&~3)-w1l(n0)),q[n&3]+(w1l(n&~3)-w1l(n0))+512);
#pragma unroll
    for(int u=0;u<4;u++){ q[u]+=P.v?-32768:32768; PINW(q[u]); }
#pragma unroll
    for(int n=n1;n<n2;n++) w1f[n]=tr2(q[n&3]+(w1l(n&~3)-w1l(n1)),q[n&3]+(w1l(n&~3)-w1l(n1))+512); };
  auto rdw1a=[&]{ lwait(cnt+20,4); rdw1(IC<0>{}); asm volatile("s_waitcnt lgkmcnt(0)":::"memory"); lsig(cnt+24); };
  if(ty==0){
    f32x16 p=mfma(wp[0],pk8(c1,0),f32x16{}); p=mfma(wp[1],pk8(c1,8),p);
    u32x4 ph[4]; dsoftmax(p,rstd*LOG2E,ph);
    LDS(u32x4,L+L_PHI+wro+c0)=ph[0]; LDS(u32x4,L+L_PHI+wro+(c0^16))=ph[1];
    LDS(u32x4,L+L_PHI+2048+wro+c0)=ph[2]; LDS(u32x4,L+L_PHI+2048+wro+(c0^16))=ph[3];
    lsig(cnt);
    STAMP(3);
    if(tt<2){
      lwait(cnt,8);
      const int ko=(8*hf+q4)*64+(sg&1)*8; const lptr k0=L+ko+(((sg>>1)^(2*hf))<<4),k1=L+ko+256+(((sg>>1)^(2*hf+1))<<4);
      f32x16 c={}; u32x4 fa[8],fb[8];
#pragma unroll
      for(int u=0;u<8;u++){ const int o=(u>>1)*4096+(u&1)*1024; fa[u]=tr2(k0+L_PHI+tt*2048+o,k1+L_PHI+tt*2048+o); fb[u]=tr2(k0+L_VI+o,k1+L_VI+o); }
      SB();
#pragma unroll
      for(int u=0;u<8;u++) c=mfma(fa[u],fb[u],c);
      st16(pk8(c,0),rkv,l16,i*4096+tt*2048); st16<1024>(pk8(c,8),rkv,l16,i*4096+tt*2048);
      lsig(cnt+8);
      STAMP(4);
    } else {
      if(tt==2) asm volatile("buffer_load_dword %0, %1, %2, 0 offen sc1":"=v"(flv):"v"(vfl),"s"(rf):"memory");
      sfor<W1E,20-NX>([&](auto K){ w1d(K.v,2*(tt&1)); w1d(K.v,2*(tt&1)+1); });
      if(tt==2){
        asm volatile("s_waitcnt vmcnt(%1)":"+v"(flv):"n"(2*(20-NX-W1E)):"memory");
        NOUNR for(int it=0;it<SPIN;it++){
          if(__builtin_amdgcn_ballot_w64(lane<32&&flv!=tag)==0) break;
          asm volatile("buffer_load_dword %0, %1, %2, 0 offen sc1\n\ts_waitcnt vmcnt(0)":"+v"(flv):"v"(vfl),"s"(rf):"memory"); }
        lsig(cnt+12); } }
    lwait(cnt+8,2);
    STAMP(5);
    sfor<20,28>([&](auto K){ w1d(K.v,tt); });
    if(tt<2){ WAITV(8); lsig(cnt+12); }
    STAMP(6);
    lwait(cnt+12,3);
    STAMP(8);
    u32x4 g[16]; const u32 rk=4*i3;
    sfor<0,16>([&](auto N){ g[N.v]=ldc<0>(l16,rkv,((N.v+rk)&15)*4096+tt*1024); });
    STAMP(42);
    rdw1a();
    WAITV(16); lsig(cnt+28);
    misc();
    STAMP(7);
    f32x4 s[4]; const u32x2 id4=mkid();
    const int rr0=chain<16>(g,[&]{ s[0]=s[1]=s[2]=s[3]=f32x4{}; },[&](auto N){ waitr<17-N.v>(g[N.v]); },[&](auto N){ acc8(id4,g[N.v],s[2*(N.v&1)],s[2*(N.v&1)+1]); PINW(s[2*(N.v&1)]); PINW(s[2*(N.v&1)+1]); },[](auto){},
      [&](auto N){ rld<0>(g[N.v],l16,rkv,((N.v+rk)&15)*4096+tt*1024); });
    CNT(37,rr0);
    LDS(u32x4,L+L_KVX+tt*1024+l16)=pk44(s[0]+s[2],s[1]+s[3]);
    PRIO(0);
  } else {
#pragma unroll
    for(int r=0;r<16;r++) c1[r]*=rstd;
    u32x4 v0=pk8(c1,0),v1=pk8(c1,8); if(hf){ v1[2]=0x3F80u; v1[3]=0u; }
    LDS(u32x4,L+L_VI+wro+c0)=v0; LDS(u32x4,L+L_VI+wro+(c0^16))=v1;
    lsig(cnt);
    STAMP(3);
    sfor<nqc(5),7>([&](auto U){ wq(U.v); if constexpr(XS==3) STAMP(26+U.v-nqc(5)); });
    sfor<NW6,12>([&](auto K){ w1d(w1o(K.v),tt); });
    STAMP(40);
    if(PFO==2) pfw();
    if(!WB7){ waitv<W1E+NX+(PFO>0)+__builtin_popcount(PFB)>(); lsig(cnt+4); }
    u32x4 fq[28]; float cq=0;
#pragma unroll
    for(int u=0;u<7;u++) fq[4*u]=tr2(aQ[0]+u*4096,aQ[0]+u*4096+256);
    if(!WB7) lwait(cnt+4,4);
    STAMP(4);
    auto ord=[](int s){ return s<7?4*s:((s-7)/3)*4+1+(s-7)%3; };
    f32x16 cQ={};
    sfor<0,28>([&](auto S){ constexpr int s=S.v;
      cQ=mfma(fq[ord(s)],xf[ord(s)],cQ); PINW(cQ); SB();
      if constexpr(s<21){ constexpr int n=ord(s+7); fq[n]=tr2(aQ[n&3]+(n>>2)*4096,aQ[n&3]+(n>>2)*4096+256); }
      if constexpr(s<7){ cq=sum8(fq[4*s],cq); PINW(cq); }
      if constexpr(s==7) LDS(float,L+L_MISC+l16+tt*4)=cq;
      if constexpr(s==22) lsig(cnt+16);
      SB(); });
    lwait(cnt+16,4);
    const f32x4 z=LDS(f32x4,L+L_MISC+l16);
    cQ=__builtin_amdgcn_mfma_f32_32x32x2f32((z[0]+z[1])+(z[2]+z[3]),nmean,cQ,0,0,0);
    f32x16 p=mfma(wp[0],pk8(cQ,0),f32x16{}); p=mfma(wp[1],pk8(cQ,8),p); PINW(p);
    STAMP(5);
    dsoftmax(p,rstd*LOG2E,phq);
    PINW(phq[0]); PINW(phq[1]); PINW(phq[2]); PINW(phq[3]);
    WAITV(0); if(PFO) asm volatile(""::"v"(pfo)); if(PFB) asm volatile(""::"v"(pfb)); lsig(cnt+20);
    STAMP(6);
    rdw1a();
    lwait(cnt+28,4); rdw1(IC<1>{});
    lwait(cnt+24,8);
    STAMP(42);
    misc();
    { const u32 rw_=ROT?((7*i3)>>1)+XR*(i>>2):0; sfor<0,14>([&](auto U){ const u32 u=tt*14+wrap(U.v+rw_,14); dma(L_WO+u*1024,vw2,r0,u*7168+cs*128); }); }
    STAMP(7);
    PRIO(2);
  }
  BAR();
  STAMP(9);
  if(ty){ f32x16 c={};
#pragma unroll
    for(int n=0;n<4;n++) c=mfma(LDS(u32x4,L+L_KVX+n*1024+l16),phq[n],c);
    const float rd=__builtin_amdgcn_rcpf(swp_hi(c[12])+1e-6f);
#pragma unroll
    for(int r=0;r<16;r++) c[r]*=rd;
    ost(pk8(c,0),pk8(c,8));
    PRIO(0);
    WAITV(3); lsig(cnt+32);
  }
  else rdw1(IC<1>{});
  STAMP(10);
  if(act){
  { const u32 kc=KC?kq^(2*th):kq, so=(2*th+tt2)*28672+kc*7168;
    lwait(cnt+32,4);
    if(!ty){ pzh(); W2D(w*4096,0,0); W2D(w*4096+2048,1,0); W2D(w*4096+16384,0,d4); W2D(w*4096+18432,1,d4); }
    lptr p0=a0+L_WO+kc*14336,p1=a1+L_WO+kc*14336; PINW(p0); PINW(p1);
    u32x4 fa[7][2]; f32x16 c[2];
#pragma unroll
    for(int n=0;n<7;n++){ fa[n][0]=tr2(p0+n*2048,p0+n*2048+512); fa[n][1]=tr2(p1+n*2048,p1+n*2048+512); PINW(fa[n][0]); PINW(fa[n][1]); }
    SB();
    STAMP(11);
    if(ty) WAITV(0); else WAITV(16);
    asm volatile("":"+v"(xr),"+v"(b1v)::"memory");
    BAR();
    STAMP(12);
    if(D5K+D5V) dly(ty?D5V:D5K);
    u32x4 of[7];
    sfor<0,7>([&](auto N){ of[N.v]=ldc<(N.v&3)*1024>(l16,ro,so+(N.v>>2)*4096); });
    auto r2d=[&](int n){ dma(L_R2+(n>>1)*16384+w*2048+(n&1)*1024,vw2,r2,sW2+wrap(2+(n>>1)+r14,14)*14336+(n&1)*7168); };
    STAMP(41);
    const int rr1=chain<7>(of,[&]{ c[0]=c[1]=f32x16{}; },[&](auto N){ waitr<6>(of[N.v]); },[&](auto N){ c[0]=mfma(fa[N.v][0],of[N.v],c[0]); PINW(c[0]); c[1]=mfma(fa[N.v][1],of[N.v],c[1]); PINW(c[1]); },[&](auto N){ r2d(N.v); if(N.v==6) r2d(7); },
      [&](auto N){ rld<(N.v&3)*1024>(of[N.v],l16,ro,so+(N.v>>2)*4096); });
    SB();
    CNT(38,rr1);
    lptr e=L+L_PX+(KC?w^(4*th):w)*4096+l16;
    LDS(u32x4,e)=pk8(c[0],0); LDS(u32x4,e+1024)=pk8(c[0],8); LDS(u32x4,e+2048)=pk8(c[1],0); LDS(u32x4,e+3072)=pk8(c[1],8);
    STAMP(13);
    BAR();
    e=L+L_PX+tt2*4096+vf;
    const u32x2 id4=mkid(); f32x4 s[4]={}; acc8(id4,xr,s[0],s[1]);
#pragma unroll
    for(int q=0;q<4;q++) acc8(id4,LDS(u32x4,e+q*8192),s[2*(q&1)],s[2*(q&1)+1]);
    s[0]+=s[2]; s[1]+=s[3];
    st16(pk44(s[0],s[1]),rh,vf,(2*th+tt2)*28672+cs*4096);
    float s1=((s[0][0]+s[0][1])+(s[0][2]+s[0][3]))+((s[1][0]+s[1][1])+(s[1][2]+s[1][3])), s2=s[0][0]*s[0][0], s3=s[1][0]*s[1][0];
#pragma unroll
    for(int q=1;q<4;q++){ s2=__builtin_fmaf(s[0][q],s[0][q],s2); s3=__builtin_fmaf(s[1][q],s[1][q],s3); }
    s1=red8(s1); s2=red8(s2+s3);
    asm volatile("buffer_store_dwordx2 %0, %1, %2, %3 offen sc1"::"v"(f32x2{s1,s2}),"v"(lane<8?lane*64:OOB),"s"(rst),"s"(((2*th+tt2)*32+kq*8)*64+cs*8):"memory");
    SB(); }
  STAMP(14);
  if(b==0&&tid==0) __hip_atomic_store((u32*)ws,tag,__ATOMIC_RELAXED,__HIP_MEMORY_SCOPE_AGENT);
  const u32 bX=ty?tt*6144:L_S+tt*12288, bY=ty?28672+tt*6144:bX+6144;
  W2D(L_R3+w*2048,6,0);
  { lptr p0=a0+w*4096,p1=a1+w*4096;
#pragma unroll
    for(int n=0;n<2;n++){ w2f[n][0]=tr2(p0+n*2048,p0+n*2048+512); w2f[n][1]=tr2(p1+n*2048,p1+n*2048+512); } }
  WAITV(4);
  { lptr p0=a0+L_R2+w*2048,p1=a1+L_R2+w*2048; PINW(p0); PINW(p1);
#pragma unroll
    for(int n=0;n<4;n++){ w2f[2+n][0]=tr2(p0+n*16384,p0+n*16384+512); w2f[2+n][1]=tr2(p1+n*16384,p1+n*16384+512); }
    asm volatile("s_waitcnt lgkmcnt(0)":"+v"(w2f[4][0]),"+v"(w2f[4][1])::"memory"); }
  W2D(L_R4+w*2048,7,0);
  WAITV(4);
  BAR();
  STAMP(15);
  const u32 hx=ty^th, sH=hx*57344+tt*1024, lH0=ty*28672+tt*1024, lH1=(ty?57344:86016)+tt*1024;
  if(SL7) if(ty) __builtin_amdgcn_s_sleep(SL7);
  if(D7K+D7B) dly((th?D7B:D7K)+(ty?D7V:0));
  if constexpr(MINST){ CNT(29,0); }
  u32x4 hb[28],sa,sb; float ks[2][5]; const u32 vst=t*64+hf*32;
  auto dm1=[&](u32 l,int k,int u){ dmac(l+u*4096,l16,rh,sH+k*28672+wrap(u+r7,7)*4096); };
  auto dmas=[&](u32 l,int k){ sfor<0,7>([&](auto U){ dm1(l,k,U.v); }); };
  dmas(lH0,0); sa=ldc<0>(vst,rst,hx*4096); sb=ldc<16>(vst,rst,hx*4096);
  float cb=0;
  if(ty){
#pragma unroll
    for(int n=14;n<28;n++) cb=sum8(w1f[n],cb);
    asm volatile("; b":"+v"(cb));
  } else {
#pragma unroll
    for(int n=0;n<14;n++) cb=sum8(w1f[n],cb);
    asm volatile("; a":"+v"(cb)); }
  LDS(float,L+(ty?86016:57344)+tt*1024+lane*4)=cb; SB();
  STAMP(36);
  sfor<0,3>([&](auto U){ W2D(L_S+tt*12288+ty*6144+U.v*2048,8+3*ty+U.v,0u-ty*d4); });
  WAITV(15);
  { lptr p0=a0+L_R4+w*2048,p1=a1+L_R4+w*2048; PINW(p0); PINW(p1); w2f[7][0]=tr2(p0,p0+512); w2f[7][1]=tr2(p1,p1+512);
    w2f[6][0]=tr2(p0+(L_R3-L_R4),p0+(L_R3-L_R4)+512); w2f[6][1]=tr2(p1+(L_R3-L_R4),p1+(L_R3-L_R4)+512); }
  auto vrd=[&](u32 l){ lptr p=L+l+l16; PINW(p);
#pragma unroll
    for(int n=0;n<7;n++) hb[4*n]=LDS(u32x4,p+n*4096);
    if(hf){ sb[2]=0; sb[3]=0; } };
  auto vbad=[&]{ u32 m=mx4(mx4(0,sa),sb);
#pragma unroll
    for(int n=0;n<7;n++) m=mx4(m,hb[4*n]);
    return bad(m); };
  auto vfix=[&](u32 l,int k){
    NOUNR for(int it=0;it<SPIN;it++){
      sfor<0,7>([&](auto U){ if(chk(hb[4*U.v])) dm1(l,k,U.v); });
      if(bad(mx4(mx4(0,sa),sb))){ rld<0>(sa,vst,rst,hx*4096+k*2048); rld<16>(sb,vst,rst,hx*4096+k*2048); }
      asm volatile("s_waitcnt vmcnt(0)":"+v"(sa),"+v"(sb)::"memory");
      vrd(l);
      if(!vbad()) break; } };
  auto vst_=[&](float* o){
    const float s1=swp_add((u2f(sa[0])+u2f(sa[2]))+(u2f(sb[0])+u2f(sb[2])))*(-1.f/448), s2=swp_add((u2f(sa[1])+u2f(sa[3]))+(u2f(sb[1])+u2f(sb[3])));
    const float g_=__builtin_fmaf(__builtin_fmaxf(__builtin_fmaf(s2,1.f/448,-s1*s1),0.f),1.00001f,1e-10f);
    const float rs_=__builtin_amdgcn_rsqf(g_); o[3]=0.5f*__builtin_amdgcn_logf(g_);
    o[0]=g_*rs_; o[1]=-2.302208198f*rs_; o[2]=-0.1029432397f*rs_*rs_*rs_; o[4]=hf?o[0]:s1; };
  auto vpin=[&](float* o){ PINW(o[0]); PINW(o[1]); PINW(o[2]); PINW(o[3]); PINW(o[4]); };
  if(ty) BAR();
  asm volatile("s_waitcnt vmcnt(6)":"+v"(sa),"+v"(sb)::"memory"); vrd(lH0); vst_(ks[0]); if(vbad()){ vfix(lH0,0); vst_(ks[0]); } vpin(ks[0]);
  if(!ty) BAR();
  STAMP(16);
  cb=swp_add(cb+LDS(float,L+lH1+lane*4)); cb=hf?u2f(b1v<<16):cb; PINW(cb);
  const u32 sS=hx*4096+2048;
  if(KI==0||ty){ dmas(lH1,1); rld<0>(sa,vst,rst,sS); rld<16>(sb,vst,rst,sS); }
  STAMP(43);
  if(ty) BAR();
  sfor<0,2>([&](auto KK){ constexpr int k=KK.v;
    lptr q[4];
#pragma unroll
    for(int u=1;u<4;u++){ q[u]=L+(k?lH1:lH0)+(((u+tt)&3)-tt)*1024+l16; PINW(q[u]); }
    f32x16 c={};
    auto ord=[](int s){ return s<7?4*s:((s-7)/3)*4+1+(s-7)%3; };
    PRIO(1);
    sfor<0,28>([&](auto S){ constexpr int s=S.v;
      c=mfma(w1f[ord(s)],hb[ord(s)],c); PINW(c); SB();
      if constexpr(XS==4&&k==0&&s%7==0) STAMP(26+s/7);
      if constexpr(s+LA>=7&&s+LA<28) hb[ord(s+LA)]=LDS(u32x4,q[ord(s+LA)&3]+(ord(s+LA)>>2)*4096);
      if constexpr(s%8==2&&s<24){ if(ty) W2D((k?bY:bX)+(s/8)*2048,8+3*k+s/8,0); }
      if constexpr(k==1){ float* o=ks[1];
        if constexpr(s==S0){ o[0]=(u2f(sa[0])+u2f(sa[2]))+(u2f(sb[0])+u2f(sb[2])); o[1]=(u2f(sa[1])+u2f(sa[3]))+(u2f(sb[1])+u2f(sb[3])); PINW(o[0]); PINW(o[1]); }
        if constexpr(s==S0+2){ o[4]=swp_add(o[0])*(-1.f/448); o[1]=swp_add(o[1]); PINW(o[4]); PINW(o[1]); }
        if constexpr(s==S0+4){ o[0]=__builtin_fmaf(__builtin_fmaxf(__builtin_fmaf(o[1],1.f/448,-o[4]*o[4]),0.f),1.00001f,1e-10f); PINW(o[0]); }
        if constexpr(s==S0+5){ o[2]=__builtin_amdgcn_rsqf(o[0]); o[3]=__builtin_amdgcn_logf(o[0]); PINW(o[2]); PINW(o[3]); }
        if constexpr(s==S0+7){ const float rs_=o[2]; o[3]*=0.5f; o[0]*=rs_; o[1]=-2.302208198f*rs_; o[2]=-0.1029432397f*rs_*rs_*rs_; o[4]=hf?o[0]:o[4]; vpin(o); } }
      if constexpr(KI&&k==0&&s%KI==KI-1&&s/KI<9){ if(!ty){ if constexpr(s/KI<7) dm1(lH1,1,s/KI); else if constexpr(s/KI==7) rld<0>(sa,vst,rst,sS); else rld<16>(sb,vst,rst,sS); } }
      SB(); });
    c=__builtin_amdgcn_mfma_f32_32x32x2f32(cb,ks[k][4],c,0,0,0); PINW(c);
    PRIO(0); if constexpr(PP==3&&k==1){ if(ty) PRIO(2); }
    SB(); STAMP(44+k); if(k==0||!ty) BAR(); PINW(c); SB();
    STAMP(17+2*k);
    u32 m0=0,m1=0;
    u32 g[8]; const float sg_=ks[k][0],K1=ks[k][1],K2=ks[k][2],Lg=ks[k][3]; const f32x2 kk={K2,K1},ls={Lg,sg_};
    sfor<0,8>([&](auto PP_){ constexpr int p=PP_.v; float a=c[2*p],d=c[2*p+1];
      if constexpr(k==0){
        if constexpr(p==4){ if(ty) waitv<7>(); else waitv<1>(); asm volatile("":"+v"(sa),"+v"(sb)::"memory"); vrd(lH1); }
        if constexpr(p==5){ m0=mx4(m0,sa); m1=mx4(m1,sb); }
        if constexpr(p==6){ m0=mx4(m0,hb[0]); m1=mx4(m1,hb[4]); m0=mx4(m0,hb[8]); }
        if constexpr(p==7){ m1=mx4(m1,hb[12]); m0=mx4(m0,hb[16]); m1=mx4(m1,hb[20]); m0=mx4(m0,hb[24]); }
        if constexpr(p==4||p==5) SB(); }
      float u=__builtin_fmaf(a,__builtin_fmaf(a*a,K2,K1),Lg), v=__builtin_fmaf(d,__builtin_fmaf(d*d,K2,K1),Lg);
      u=__builtin_amdgcn_rcpf(__builtin_amdgcn_exp2f(u)+sg_); v=__builtin_amdgcn_rcpf(__builtin_amdgcn_exp2f(v)+sg_);
      g[p]=pk(a*u,d*v);
      if constexpr(p==3) st16(u32x4{g[0],g[1],g[2],g[3]},rhh,l16,(2*hx+k)*114688+(8*j+2*tt)*1024); });
    st16<1024>(u32x4{g[4],g[5],g[6],g[7]},rhh,l16,(2*hx+k)*114688+(8*j+2*tt)*1024);
    if constexpr(k==0){ if(bad(m0>m1?m0:m1)) vfix(lH1,1);
      STAMP(46); BAR(); STAMP(18); }
    SB();
  });
  { const u32 s5=th*229376+wk*14336, dY=114688; const int tl=2*th+tt2;
    auto su=[&](int n){ return s5+wrap(n+r14,14)*1024; };
    u32x4 hh[14],hp,q2;
    auto rdw2=[&]{ lptr p0=a0+bX,p1=a1+bX; PINW(p0); PINW(p1);
#pragma unroll
      for(int n=0;n<3;n++){ w2f[8+n][0]=tr2(p0+n*2048,p0+n*2048+512); w2f[8+n][1]=tr2(p1+n*2048,p1+n*2048+512); }
      p0=a0+bY; p1=a1+bY; PINW(p0); PINW(p1);
#pragma unroll
      for(int n=0;n<3;n++){ w2f[11+n][0]=tr2(p0+n*2048,p0+n*2048+512); w2f[11+n][1]=tr2(p1+n*2048,p1+n*2048+512); } };
    STAMP(20);
    if(D8K+D8V) dly(ty?D8V:D8K);
    if(PP) if(ty) PRIO(2);
    sfor<0,14>([&](auto N){ hh[N.v]=ldc<0>(l16,rhh,su(N.v)); });
    WAITV(16); rdw2();
#pragma unroll
    for(int n=8;n<14;n++){ PINW(w2f[n][0]); PINW(w2f[n][1]); }
    WAITV(14);
    STAMP(21);
    f32x16 c[2]; auto zc=[&]{ c[0]=c[1]=f32x16{}; };
    auto mm=[&](auto N){ c[0]=mfma(w2f[N.v][0],hh[N.v],c[0]); PINW(c[0]); c[1]=mfma(w2f[N.v][1],hh[N.v],c[1]); PINW(c[1]); };
    const int rr2=chain<14>(hh,zc,[&](auto N){ waitr<13>(hh[N.v]); },mm,[&](auto N){ if constexpr(XS==1&&N.v%2==0&&N.v<12) STAMP(26+N.v/2); rld<0>(hh[N.v],l16,rhh,su(N.v)+dY); },
      [&](auto N){ rld<0>(hh[N.v],l16,rhh,su(N.v)); });
    if(PP>=2) PRIO(0);
    STAMP(22); CNT(39,rr2);
    lptr e=L+bX+l16; PINW(e);
    LDS(u32x4,e)=pk8(c[0],0); LDS(u32x4,e+1024)=pk8(c[0],8); LDS(u32x4,e+2048)=pk8(c[1],0); LDS(u32x4,e+3072)=pk8(c[1],8);
    hp=ldc<0>(vf,rh,tl*28672+cs*4096); q2=ldp(v8*16,mk(b2,896),cs*128);
    STAMP(23);
    const int rr3=chain<14>(hh,zc,[&](auto N){ waitr<15-N.v>(hh[N.v]); },mm,[](auto){},
      [&](auto N){ rld<0>(hh[N.v],l16,rhh,su(N.v)+dY); });
    STAMP(24); CNT(47,rr3);
    e=L+bY+l16; PINW(e);
    LDS(u32x4,e)=pk8(c[0],0); LDS(u32x4,e+1024)=pk8(c[0],8); LDS(u32x4,e+2048)=pk8(c[1],0); LDS(u32x4,e+3072)=pk8(c[1],8);
    asm volatile("s_waitcnt vmcnt(0)":"+v"(q2),"+v"(hp)::"memory");
    BAR();
    lptr e0=L+tt2*28672+vf,e1=L+L_S+tt2*6144+vf; PINW(e0); PINW(e1);
    const u32x2 id4=mkid(); f32x4 s[4]={}; acc8(id4,hp,s[0],s[1]); acc8(id4,q2,s[2],s[3]);
#pragma unroll
    for(int q=0;q<4;q++){ acc8(id4,LDS(u32x4,e0+q*6144),s[0],s[1]); acc8(id4,LDS(u32x4,e1+q*12288),s[2],s[3]); }
    __builtin_amdgcn_raw_buffer_store_b128(pk44(s[0]+s[2],s[1]+s[3]),mk(out+(size_t)i*128*448,128*896),vo,(tl*32+kq*8)*896+cs*128,16); }
  }
  asm volatile(".Lfk_c1:");
  STAMP(25);
}
}
#if defined(__HIP_DEVICE_COMPILE__)&&!PK
#pragma clang attribute pop
#endif
extern "C" void kernel_launch(void* const* d_in,const int* in_sizes,int n_in,void* d_out,int out_size,void* d_ws,size_t ws_size,hipStream_t stream){
  typedef const unsigned short* P;
  (void)hipFuncSetAttribute((const void*)fk::fused,hipFuncAttributeMaxDynamicSharedMemorySize,163840);
  fk::fused<<<256,512,163840,stream>>>((P)d_in[0],(P)d_in[1],(P)d_in[2],(P)d_in[3],(P)d_in[5],(P)d_in[6],(P)d_in[7],(P)d_in[8],(unsigned short*)d_out,(char*)d_ws);
}
```
